```python
import torch

def _softmax(x, axis=-1):
    return torch.softmax(x, dim=axis)
def _gelu(x):
    return torch.nn.functional.gelu(x, approximate="tanh")
def _silu(x):
    return torch.nn.functional.silu(x)
def _sigmoid(x):
    return torch.sigmoid(x)
def _layernorm(x, axis=-1, eps=1e-5):
    m = x.mean(dim=axis, keepdim=True)
    v = x.var(dim=axis, keepdim=True, unbiased=False)
    return (x - m) / torch.sqrt(v + eps)
def _scatter_add(tgt, idx, src, axis=0):
    return tgt.clone().index_add_(axis, idx, src)
def _segment_sum(src, seg, n):
    out = torch.zeros((n,) + src.shape[1:], dtype=src.dtype)
    return out.index_add_(0, seg, src)
def _pad(x, pw):
    flat = tuple(v for pair in reversed(pw) for v in pair)
    return torch.nn.functional.pad(x, flat)

kernel_name = 'interleave_layers__gqa_block_cached__glu_gated_glu__decode'

def setup_inputs():

    _w = (lambda a: (lambda t: t.to(torch.bfloat16) if t.is_floating_point() else t)(torch.as_tensor(a)))
    def _A_setup():
        B, Sq, D = 1, 1, 512
        Hq, Hkv, Sk, Dh = 32, 32, 16384, 128
        assert Hq % Hkv == 0, (Hq, Hkv)
        return {
            "x": _w((lambda *s: torch.randn(*s, dtype=torch.bfloat16) * 0.05)(B, Sq, D)),
            "k_cache": _w((lambda *s: torch.randn(*s, dtype=torch.bfloat16) * 1.6)(B, Hkv, Sk, Dh)),
            "v_cache": _w((lambda *s: torch.randn(*s, dtype=torch.bfloat16) * 1.6)(B, Hkv, Sk, Dh)),
            "wq": _w((lambda *s: torch.randn(*s, dtype=torch.bfloat16) * 1.6)(D, Hq * Dh) * (D ** -0.5)),
            "wo": _w((lambda *s: torch.randn(*s, dtype=torch.bfloat16) * 1.6)(Hq * Dh, D) * ((Hq * Dh) ** -0.5)),
        }
    _A_in = _A_setup()

    def _B_setup():
        B, S, D, F = 1, 1, 512, 1376
        return {
            "x": _w((lambda *s: torch.randn(*s, dtype=torch.bfloat16) * 0.05)(B, S, D)),
            "w_gate": _w((lambda *s: torch.randn(*s, dtype=torch.bfloat16) * 0.05)(D, F)),
            "w_up": _w((lambda *s: torch.randn(*s, dtype=torch.bfloat16) * 0.05)(D, F)),
            "w_down": _w((lambda *s: torch.randn(*s, dtype=torch.bfloat16) * 0.05)(F, D)),
        }
    _B_in = _B_setup()

    return {
        "A_x": _A_in["x"],
        "A_k_cache": _A_in["k_cache"],
        "A_v_cache": _A_in["v_cache"],
        "A_wq": _A_in["wq"],
        "A_wo": _A_in["wo"],
        "B_x": _B_in["x"],
        "B_w_gate": _B_in["w_gate"],
        "B_w_up": _B_in["w_up"],
        "B_w_down": _B_in["w_down"],
    }

def reference(A_x, A_k_cache, A_v_cache, A_wq, A_wo, B_x, B_w_gate, B_w_up, B_w_down):
    def _A_ref(x, k_cache, v_cache, wq, wo):
        B, Sq, D = x.shape
        Hq, Dh = 32, 128
        Hkv = k_cache.shape[1]
        rep = Hq // Hkv
        q = (lambda x, *d: x.permute(*d))((x @ wq).reshape(B, Sq, Hq, Dh), 0, 2, 1, 3)
        k = (lambda x, n, axis: torch.repeat_interleave(x, n, dim=axis))(k_cache, rep, axis=1)
        v = (lambda x, n, axis: torch.repeat_interleave(x, n, dim=axis))(v_cache, rep, axis=1)
        scores = torch.einsum("bhqd,bhkd->bhqk", q, k) * (Dh ** -0.5)
        p = _softmax(scores, axis=-1)
        o = torch.einsum("bhqk,bhkd->bhqd", p, v)
        return (lambda x, *d: x.permute(*d))(o, 0, 2, 1, 3).reshape(B, Sq, Hq * Dh) @ wo

    def _B_ref(x, w_gate, w_up, w_down):
        g = torch.einsum("bsd,df->bsf", x, w_gate)
        u = torch.einsum("bsd,df->bsf", x, w_up)
        h = _sigmoid(g) * u
        return torch.einsum("bsf,fd->bsd", h, w_down)

    x = A_x
    x = x + _A_ref(_layernorm(x, axis=-1), A_k_cache, A_v_cache, A_wq, A_wo)
    x = x + _B_ref(_layernorm(x, axis=-1), B_w_gate, B_w_up, B_w_down)
    x = x + _A_ref(_layernorm(x, axis=-1), A_k_cache, A_v_cache, A_wq, A_wo)
    x = x + _B_ref(_layernorm(x, axis=-1), B_w_gate, B_w_up, B_w_down)
    return x
```

```cpp
#include <hip/hip_runtime.h>
#include <hip/hip_bf16.h>
#include <stdint.h>

typedef unsigned u32x4 __attribute__((ext_vector_type(4)));
typedef float f32x4 __attribute__((ext_vector_type(4)));
typedef unsigned short u16;

constexpr int D = 512, H = 32, SK = 16384, DH = 128, F = 1376;
constexpr int NSPLIT = 32;
constexpr int KPS = SK / NSPLIT;

__device__ __forceinline__ float bf2f(unsigned h) { return __builtin_bit_cast(float, h << 16); }
__device__ __forceinline__ float lo(unsigned w) { return __builtin_bit_cast(float, w << 16); }
__device__ __forceinline__ float hi(unsigned w) { return __builtin_bit_cast(float, w & 0xffff0000u); }

__device__ __forceinline__ float wave_sum(float v) {
  for (int o = 32; o > 0; o >>= 1) v += __shfl_xor(v, o);
  return v;
}
__device__ __forceinline__ float wave_max(float v) {
  for (int o = 32; o > 0; o >>= 1) v = fmaxf(v, __shfl_xor(v, o));
  return v;
}

__device__ void block_ln(const float* x, float* xn, float* red) {
  int tid = threadIdx.x;
  float a = x[tid], b = x[tid + 256];
  float s = wave_sum(a + b);
  if ((tid & 63) == 0) red[tid >> 6] = s;
  __syncthreads();
  float mean = (red[0] + red[1] + red[2] + red[3]) * (1.f / D);
  __syncthreads();
  float da = a - mean, db = b - mean;
  float v = wave_sum(da * da + db * db);
  if ((tid & 63) == 0) red[tid >> 6] = v;
  __syncthreads();
  float var = (red[0] + red[1] + red[2] + red[3]) * (1.f / D);
  float rs = 1.f / sqrtf(var + 1e-5f);
  xn[tid] = da * rs; xn[tid + 256] = db * rs;
  __syncthreads();
}

__global__ void k_init(const u16* x, float* xf) { int t = threadIdx.x; xf[t] = bf2f(x[t]); }

__global__ void __launch_bounds__(256) k_q(const float* x, const u16* W, int ldw, float* out) {
  __shared__ float xn[D]; __shared__ float red[4]; __shared__ float part[32][65];
  block_ln(x, xn, red);
  int tid = threadIdx.x, c = tid & 7, r = tid >> 3;
  int col0 = blockIdx.x * 64 + c * 8;
  float acc[8] = {0, 0, 0, 0, 0, 0, 0, 0};
  for (int k = r; k < D; k += 32) {
    u32x4 w = *(const u32x4*)(W + (size_t)k * ldw + col0);
    float xv = xn[k];
    for (int j = 0; j < 4; ++j) { acc[2 * j] += xv * lo(w[j]); acc[2 * j + 1] += xv * hi(w[j]); }
  }
  for (int j = 0; j < 8; ++j) part[r][c * 8 + j] = acc[j];
  __syncthreads();
  if (tid < 64) { float s = 0; for (int i = 0; i < 32; ++i) s += part[i][tid]; out[blockIdx.x * 64 + tid] = s; }
}

__global__ void __launch_bounds__(256) k_attn(const float* q, const u16* K, const u16* V, float* pm, float* pl, float* po) {
  __shared__ float sc[KPS]; __shared__ float red[4]; __shared__ float oacc[16][DH + 4];
  int s = blockIdx.x, h = blockIdx.y, tid = threadIdx.x, lane = tid & 63, w = tid >> 6;
  int c = tid & 15, r = tid >> 4;
  float qf[8];
  for (int j = 0; j < 8; ++j) qf[j] = q[h * DH + c * 8 + j];
  const u16* Kb = K + ((size_t)h * SK + (size_t)s * KPS) * DH;
  const u16* Vb = V + ((size_t)h * SK + (size_t)s * KPS) * DH;
#pragma unroll 8
  for (int k = r; k < KPS; k += 16) {
    u32x4 kv = *(const u32x4*)(Kb + (size_t)k * DH + c * 8);
    float d = 0;
    for (int j = 0; j < 4; ++j) d += qf[2 * j] * lo(kv[j]) + qf[2 * j + 1] * hi(kv[j]);
    d += __shfl_xor(d, 1); d += __shfl_xor(d, 2); d += __shfl_xor(d, 4); d += __shfl_xor(d, 8);
    if (c == 0) sc[k] = d * 0.08838834764831845f;
  }
  __syncthreads();
  float m = fmaxf(sc[tid], sc[tid + 256]);
  m = wave_max(m);
  if (lane == 0) red[w] = m;
  __syncthreads();
  m = fmaxf(fmaxf(red[0], red[1]), fmaxf(red[2], red[3]));
  __syncthreads();
  float p0 = __expf(sc[tid] - m), p1 = __expf(sc[tid + 256] - m);
  sc[tid] = p0; sc[tid + 256] = p1;
  float l = wave_sum(p0 + p1);
  if (lane == 0) red[w] = l;
  __syncthreads();
  l = red[0] + red[1] + red[2] + red[3];
  float acc[8] = {0, 0, 0, 0, 0, 0, 0, 0};
#pragma unroll 8
  for (int k = r; k < KPS; k += 16) {
    u32x4 vv = *(const u32x4*)(Vb + (size_t)k * DH + c * 8);
    float p = sc[k];
    for (int j = 0; j < 4; ++j) { acc[2 * j] += p * lo(vv[j]); acc[2 * j + 1] += p * hi(vv[j]); }
  }
  for (int j = 0; j < 8; ++j) oacc[r][c * 8 + j] = acc[j];
  __syncthreads();
  if (tid < DH) {
    float o = 0; for (int i = 0; i < 16; ++i) o += oacc[i][tid];
    po[(h * NSPLIT + s) * DH + tid] = o;
  }
  if (tid == 0) { pm[h * NSPLIT + s] = m; pl[h * NSPLIT + s] = l; }
}

__global__ void k_comb(const float* pm, const float* pl, const float* po, float* o) {
  int h = blockIdx.x, d = threadIdx.x;
  float M = -INFINITY;
  for (int s = 0; s < NSPLIT; ++s) M = fmaxf(M, pm[h * NSPLIT + s]);
  float L = 0, a = 0;
  for (int s = 0; s < NSPLIT; ++s) { float w = __expf(pm[h * NSPLIT + s] - M); L += w * pl[h * NSPLIT + s]; a += w * po[(h * NSPLIT + s) * DH + d]; }
  o[h * DH + d] = a / L;
}

__global__ void __launch_bounds__(256) k_res(const float* v, int Kd, const u16* W, const float* xin, float* xout, u16* out_bf) {
  __shared__ float part[4][8];
  int tid = threadIdx.x, col0 = blockIdx.x * 8;
  float acc[8] = {0, 0, 0, 0, 0, 0, 0, 0};
  for (int k = tid; k < Kd; k += 256) {
    u32x4 w = *(const u32x4*)(W + (size_t)k * D + col0);
    float xv = v[k];
    for (int j = 0; j < 4; ++j) { acc[2 * j] += xv * lo(w[j]); acc[2 * j + 1] += xv * hi(w[j]); }
  }
  for (int j = 0; j < 8; ++j) { float s = wave_sum(acc[j]); if ((tid & 63) == 0) part[tid >> 6][j] = s; }
  __syncthreads();
  if (tid < 8) {
    float r = xin[col0 + tid] + part[0][tid] + part[1][tid] + part[2][tid] + part[3][tid];
    xout[col0 + tid] = r;
    if (out_bf) { __hip_bfloat16 b = __float2bfloat16(r); out_bf[col0 + tid] = *(u16*)&b; }
  }
}

__global__ void __launch_bounds__(256) k_gu(const float* x, const u16* Wg, const u16* Wu, float* hbuf) {
  __shared__ float xn[D]; __shared__ float red[4]; __shared__ float part[4][16];
  block_ln(x, xn, red);
  int tid = threadIdx.x, col0 = blockIdx.x * 8;
  float g[8] = {0, 0, 0, 0, 0, 0, 0, 0}, u[8] = {0, 0, 0, 0, 0, 0, 0, 0};
  for (int k = tid; k < D; k += 256) {
    u32x4 wg = *(const u32x4*)(Wg + (size_t)k * F + col0);
    u32x4 wu = *(const u32x4*)(Wu + (size_t)k * F + col0);
    float xv = xn[k];
    for (int j = 0; j < 4; ++j) { g[2 * j] += xv * lo(wg[j]); g[2 * j + 1] += xv * hi(wg[j]); u[2 * j] += xv * lo(wu[j]); u[2 * j + 1] += xv * hi(wu[j]); }
  }
  for (int j = 0; j < 8; ++j) {
    float s = wave_sum(g[j]), t = wave_sum(u[j]);
    if ((tid & 63) == 0) { part[tid >> 6][j] = s; part[tid >> 6][8 + j] = t; }
  }
  __syncthreads();
  if (tid < 8) {
    float gg = part[0][tid] + part[1][tid] + part[2][tid] + part[3][tid];
    float uu = part[0][8 + tid] + part[1][8 + tid] + part[2][8 + tid] + part[3][8 + tid];
    hbuf[col0 + tid] = uu / (1.f + __expf(-gg));
  }
}

extern "C" void kernel_launch(void* const* d_in, const int* in_sizes, int n_in, void* d_out, int out_size, void* d_ws, size_t ws_size, hipStream_t stream) {
  const u16* x = (const u16*)d_in[0];
  const u16* K = (const u16*)d_in[1];
  const u16* V = (const u16*)d_in[2];
  const u16* wq = (const u16*)d_in[3];
  const u16* wo = (const u16*)d_in[4];
  const u16* wg = (const u16*)d_in[6];
  const u16* wu = (const u16*)d_in[7];
  const u16* wd = (const u16*)d_in[8];
  float* ws = (float*)d_ws;
  float* x0 = ws; float* x1 = ws + 512; float* q = ws + 1024; float* o = q + 4096; float* hb = o + 4096;
  float* pm = hb + 2048; float* pl = pm + H * NSPLIT; float* po = pl + H * NSPLIT;
  k_init<<<1, 512, 0, stream>>>(x, x0);
  for (int layer = 0; layer < 2; ++layer) {
    k_q<<<64, 256, 0, stream>>>(x0, wq, H * DH, q);
    k_attn<<<dim3(NSPLIT, H), 256, 0, stream>>>(q, K, V, pm, pl, po);
    k_comb<<<H, DH, 0, stream>>>(pm, pl, po, o);
    k_res<<<64, 256, 0, stream>>>(o, H * DH, wo, x0, x1, nullptr);
    k_gu<<<F / 8, 256, 0, stream>>>(x1, wg, wu, hb);
    k_res<<<64, 256, 0, stream>>>(hb, F, wd, x1, x0, layer == 1 ? (u16*)d_out : nullptr);
  }
}
```

```cpp
#include <hip/hip_runtime.h>
#include <stdint.h>

typedef unsigned short u16;
typedef unsigned int u32;
typedef u32 u32x2 __attribute__((ext_vector_type(2)));
typedef u32 u32x4 __attribute__((ext_vector_type(4)));
typedef u32 u32x8 __attribute__((ext_vector_type(8)));
typedef u32 u32x16 __attribute__((ext_vector_type(16)));
typedef float f32x2 __attribute__((ext_vector_type(2)));
typedef float f32x4 __attribute__((ext_vector_type(4)));
typedef __bf16 bf16x2 __attribute__((ext_vector_type(2)));
typedef __amdgpu_buffer_rsrc_t rsrc_t;
#define DI __device__ __forceinline__
#define LDSP __attribute__((address_space(3)))
#define CFG_STAMP 0

constexpr int DM = 512, NH = 32, SK = 16384, DH = 128, FF = 1376;
constexpr int NL = 39;
constexpr int RCAP = 48;
constexpr u32 EM[8] = {0x0, 0xf000, 0xffff, 0xffff, 0xf, 0x0, 0xf000, 0x3f};
constexpr int LDS_BYTES = 163840, LDS_MISC = 4 * NL * 1024;
static_assert(LDS_MISC + 4096 <= LDS_BYTES, "lds");

constexpr int HSTR = 64, HREP = 2, QSTR = 64;
constexpr int REP = 0x8000;
constexpr int WS_ACK = 1024;
constexpr int WS_TS = 2048;
constexpr int WS_TS2 = 2560;
constexpr int WS_SPARE = 0x8000;
constexpr int WS_Q0 = 0x1000;
constexpr int WS_LAYER0 = 0x10000, WS_LAYER_STR = 0x100000;
constexpr int O_ML = 0;
constexpr int O_Q = 0x4000;
constexpr int O_P1 = 0xC000;
constexpr int O_P2 = 0x2C000;
constexpr int O_P3 = 0x6C000;
constexpr int O_HB = 0xC8000;
constexpr int WS_STAMP = 0x300000;
constexpr int WS_BYTES = 0x400000;
#define OOB c.oob
constexpr u32 SENT = 0xFFFFFFFFu;
constexpr int SPIN_CAP = 1 << 16;
constexpr int SC1 = 16;

struct Params {
  const u16 *x, *K, *V, *wq, *wo, *wg, *wu, *wd;
  u16* out;
  unsigned char* ws;
};

DI rsrc_t mkrs(const void* p, int bytes) { return __builtin_amdgcn_make_buffer_rsrc((void*)p, 0, bytes, 0x00020000); }
template <int AUX> DI u32x4 ld128(rsrc_t r, int voff, int soff) { return __builtin_amdgcn_raw_buffer_load_b128(r, voff, soff, AUX); }
template <int AUX> DI u32 ld32(rsrc_t r, int voff, int soff) { return __builtin_amdgcn_raw_buffer_load_b32(r, voff, soff, AUX); }
DI void st128(rsrc_t r, int voff, int soff, u32x4 v) { __builtin_amdgcn_raw_buffer_store_b128(v, r, voff, soff, SC1); }
DI void st64(rsrc_t r, int voff, int soff, u32x2 v) { __builtin_amdgcn_raw_buffer_store_b64(v, r, voff, soff, SC1); }
DI void st32(rsrc_t r, int voff, int soff, u32 v) { __builtin_amdgcn_raw_buffer_store_b32(v, r, voff, soff, SC1); }
DI float asf(u32 u) { return __builtin_bit_cast(float, u); }
DI u32 asu(float f) { return __builtin_bit_cast(u32, f); }
DI float bflo(u32 w) { return asf(w << 16); }
DI float bfhi(u32 w) { return asf(w & 0xffff0000u); }
DI u32 pk2(float lo, float hi) { f32x2 v = {lo, hi}; return __builtin_bit_cast(u32, __builtin_convertvector(v, bf16x2)); }
DI float rbf(float a) { return bflo(pk2(a, a)); }
DI float dot2(u32 a, u32 b, float c) { return __builtin_amdgcn_fdot2_f32_bf16(__builtin_bit_cast(bf16x2, a), __builtin_bit_cast(bf16x2, b), c, false); }
template <int CTRL> DI u32 dppu(u32 x) { return (u32)__builtin_amdgcn_update_dpp(0, (int)x, CTRL, 0xf, 0xf, true); }
template <int CTRL> DI float dppf(float x) { return asf(dppu<CTRL>(asu(x))); }
constexpr int XOR1 = 0xB1, XOR2 = 0x4E, HMIR = 0x141, ROR8 = 0x128;
DI float x16_sum(float x) { auto s = __builtin_amdgcn_permlane16_swap(asu(x), asu(x), false, false); return asf(s[0]) + asf(s[1]); }
DI float x32_sum(float x) { auto s = __builtin_amdgcn_permlane32_swap(asu(x), asu(x), false, false); return asf(s[0]) + asf(s[1]); }
DI float x16_max(float x) { auto s = __builtin_amdgcn_permlane16_swap(asu(x), asu(x), false, false); return fmaxf(asf(s[0]), asf(s[1])); }
DI float x32_max(float x) { auto s = __builtin_amdgcn_permlane32_swap(asu(x), asu(x), false, false); return fmaxf(asf(s[0]), asf(s[1])); }
DI float row_sum(float x) { x += dppf<XOR1>(x); x += dppf<XOR2>(x); x += dppf<HMIR>(x); x += dppf<ROR8>(x); return x; }
DI float row_max(float x) { x = fmaxf(x, dppf<XOR1>(x)); x = fmaxf(x, dppf<XOR2>(x)); x = fmaxf(x, dppf<HMIR>(x)); x = fmaxf(x, dppf<ROR8>(x)); return x; }
DI float wave_sum(float x) { return x32_sum(x16_sum(row_sum(x))); }
DI float wave_max(float x) { return x32_max(x16_max(row_max(x))); }
DI u32 uni(bool f) { return __builtin_amdgcn_readfirstlane(f ? 1u : 0u); }
DI bool wave_all(bool ok) { return __builtin_amdgcn_ballot_w64(!ok) == 0ull; }

#define STAMP(i) do {} while (0)

struct Ctx {
  int b, h, s, w, lane, g, n, xg, j, cp, cq, fb0, nb, row, oob;
  rsrc_t rsW, rsK, rsV, rsC;
  u32 epoch, stv, lds;
};

DI void derive(Ctx& c) {
  asm volatile("v_mbcnt_lo_u32_b32 %0, -1, 0\n v_mbcnt_hi_u32_b32 %0, -1, %0" : "=v"(c.lane));
  asm volatile("v_mov_b32 %0, 0x7ff00000" : "=v"(c.oob));
  asm volatile("" : "+s"(c.b));
  asm volatile("" : "+s"(c.w));
  c.g = c.lane >> 4; c.n = c.lane & 15;
  c.xg = c.b & 7; c.j = c.b >> 3;
  c.h = c.xg * 4 + (c.j & 3); c.s = ((c.b >> 5) + 0) & 7;
  c.cp = c.xg * 32 + ((c.j + 16) & 31);
  c.cq = -1 < 0 ? c.h * 8 + c.s : c.xg * 32 + (c.j ^ -1);
  c.fb0 = 43 * (c.xg >> 1) + 22 * (c.xg & 1); c.nb = 22 - (c.xg & 1);
  c.row = 128 * c.w + 32 * c.g + 4 * (c.n & 4) + ((c.n & 8) >> 2);
}

struct Attn {
  float sc[8];
  u32 pp[8];
  float m, l;
};

DI void softmax_local(Attn& a) {
  float mx = -INFINITY;
#pragma unroll
  for (int k = 0; k < 8; ++k) { a.sc[k] = rbf(rbf(a.sc[k]) * 0.08838834764831845f); mx = fmaxf(mx, a.sc[k]); }
  mx = wave_max(mx);
  float l = 0.f;
#pragma unroll
  for (int k = 0; k < 8; ++k) {
    const float p = __builtin_amdgcn_exp2f((a.sc[k] - mx) * 1.4426950408889634f);
    l += p;
    a.sc[k] = p;
  }
  a.m = mx; a.l = wave_sum(l);
}
DI float head_scale(const u32x4& ml, float m) {
  const float M = x16_max(row_max(asf(ml[0])));
  const float Ls = x16_sum(row_sum(__expf(asf(ml[0]) - M) * asf(ml[2])));
  return __expf(m - M) * __builtin_amdgcn_rcpf(Ls);
}
DI int wo_off(const Ctx& c) { return 0 & 4 ? OOB : (512 * c.xg + (1 & 1 ? 128 * c.w + 32 * c.g + (c.n & 4) + 2 * (c.n & 8) : c.row)) * (DM * 2) + (c.cp & 31) * 32 + (c.n & 3) * 8; }
DI int wgu_off(const Ctx& c) { return c.j < c.nb && !(0 & 1) ? c.row * (FF * 2) + (c.fb0 + c.j) * 16 + (c.n & 3) * 4 : OOB; }
DI int wq_off(const Ctx& c) { return 0 & 8 ? OOB : c.row * (NH * DH * 2) + c.cq * 32 + (c.n & 3) * 8; }

DI int wd_row_off(const Ctx& c) { return c.j < c.nb ? (c.fb0 + c.j) * 8192 + c.w * 256 + c.lane * 4 : OOB; }
DI int k_off(const Ctx& c) { return (c.n >> 2) * 256 + (c.n & 3) * 16 + c.g * 64; }
DI u32 l_off(const Ctx& c) { return c.lds + c.w * NL * 1024 + c.lane * 16; }
DI u32 r_base(const Ctx& c) { return c.lds + c.w * NL * 1024 + (NL - 1) * 1024; }
DI float unshift(u32 t4) { return asf((113 + 2 * (t4 & 0xff)) << 23); }
DI bool outside(const u32x4& r, u32 t4) {
  const u32 lo = (2 * (t4 & 0xff) - 13) << 7;
  u32 mx = 0;
#pragma unroll
  for (int i = 0; i < 8; ++i) { const u32 a = (r[i >> 1] >> (16 * (i & 1))) & 0x7fff; mx = max(mx, (a - lo) * min(a, 1u)); }
  return mx >= 15 * 128;
}
DI u32x4 ld_wait(rsrc_t rs, int voff, int soff) {
  u32x4 r;
  asm volatile("s_nop 4\n buffer_load_dwordx4 %0, %1, %2, %3 offen\n s_waitcnt vmcnt(0)" : "=&v"(r) : "v"(voff), "s"(rs), "s"(soff) : "memory");
  return r;
}
DI u32x4 piece(u32 addr) {
  const u32x4 d = *(LDSP u32x4*)(size_t)addr;
  return u32x4{__builtin_amdgcn_perm(d[0], d[2], 0x05010400), __builtin_amdgcn_perm(d[0], d[2], 0x07030602),
               __builtin_amdgcn_perm(d[1], d[3], 0x05010400), __builtin_amdgcn_perm(d[1], d[3], 0x07030602)};
}
DI float dot8(const u32x4& r, const u32* q) { return dot2(r[1], q[1], dot2(r[0], q[0], 0.f)) + dot2(r[3], q[3], dot2(r[2], q[2], 0.f)); }
DI void lds_add(u32 addr, float x) { __hip_atomic_fetch_add((LDSP float*)(size_t)addr, x, __ATOMIC_RELAXED, __HIP_MEMORY_SCOPE_WAVEFRONT); }
DI u32 half_mask(const Ctx& c) { return (c.n & 2) ? 0u : (c.n & 1) ? 0xffff0000u : 0xffffu; }
DI int touch_q(const Ctx& c) { return c.lane < 32 ? (16 * c.j + 4 * c.w + (c.lane >> 3)) * (NH * DH * 2) + c.xg * 1024 + (c.lane & 7) * 128 : OOB; }
DI int touch_o(const Ctx& c) { return c.lane < 32 ? (512 * c.xg + 16 * c.j + 4 * c.w) * (DM * 2) + c.lane * 128 : OOB; }
DI int touch_gu(const Ctx& c) { return c.lane < 16 ? (16 * c.j + 4 * c.w + (c.lane >> 2)) * (FF * 2) + c.fb0 * 16 + min((c.lane & 3) * 128, c.nb * 16 - 4) : OOB; }
DI int touch_d(const Ctx& c) { return c.lane < 11 ? c.fb0 * 8192 + ((4 * c.j + c.w) * 11 + c.lane) * 128 : OOB; }

DI u32 time_base(const u32x2& tk, u32 tag, u32 t0) {
  int dt = tk[1] - t0 + 800 <= 2 * 800 ? (int)(tk[1] - t0) : 0;
  dt += (int)dppu<XOR1>(dt); dt += (int)dppu<XOR2>(dt); dt += (int)dppu<HMIR>(dt);
  return t0 + __builtin_amdgcn_readfirstlane(dt >> 3);
}

DI void chain_common(const Ctx& c, u32x16& yv, u32x4& ad) {
  const u32 m = c.lds + LDS_MISC;
  yv[1] = half_mask(c);
  yv[2] = m + c.w * 1024 + (8 * c.g + 2 * (c.n >> 2) + (c.n & 1)) * 16;
  yv[3] = m + (8 * c.w + c.lane) * 16;
  yv[4] = c.lane * 16;
  yv[12] = c.lane * 16;
  ad[0] = (c.n >> 3) * 8192 + c.w * 512 + (8 * c.g + (c.n & 7)) * 16;
  ad[1] = m + c.w * 1024 + (c.n & 3) * 64 + c.g * 4;
  ad[2] = m + 768 + c.n * 16;
  ad[3] = (c.g & 1) * REP + c.n * 4;
}

__global__ void __launch_bounds__(256, 1) __attribute__((amdgpu_num_vgpr(36))) fused_decode(Params p) {
  extern __shared__ __attribute__((aligned(16))) unsigned char smem[];
  Ctx c;
  c.lds = (u32)(size_t)(LDSP unsigned char*)smem;
  c.b = blockIdx.x; c.w = __builtin_amdgcn_readfirstlane(threadIdx.x >> 6);
  c.stv = 0;
  derive(c);
  c.rsW = mkrs(p.ws, WS_BYTES);
  {
    const size_t wb = ((size_t)c.h * SK + (size_t)c.s * 2048 + (size_t)c.w * 512) * DH * 2;
    c.rsK = mkrs((const unsigned char*)p.K + wb, 131072);
    c.rsV = mkrs((const unsigned char*)p.V + wb, 131072);
  }
  STAMP(0);
  u32 t0 = (u32)__builtin_amdgcn_s_memrealtime();
  const u32 tstart = -1 < 0 || c.xg == -1 ? t0 : t0 - 100000;
  t0 -= (c.j * 0) >> 5;
  u32 so, tb, xres, rcn = 0, t4k, thk, t4v, thv;
  u32 q4[4];
  u32x8 sv; u32x2 sw;
  {
    unsigned long long pc; int cl;
    asm volatile(
R"(
s_nop 4
buffer_load_dwordx4 v[116:119], %[ko], %[rk], 0 offen offset:0 nt
buffer_load_dwordx4 v[120:123], %[ko], %[rk], 0 offen offset:1024 nt
buffer_load_dwordx4 v[124:127], %[ko], %[rk], 0 offen offset:2048 nt
buffer_load_dwordx4 v[128:131], %[ko], %[rk], 0 offen offset:3072 nt
s_getpc_b64 %[pc]
Lcode0:
s_mov_b32 %[cl], Lcode1-Lcode0
)"
      : [pc] "=&s"(pc), [cl] "=&s"(cl)
      : [rk] "{s[64:67]}"(c.rsK), [ko] "v"(k_off(c))
      : "memory", "vcc", "scc", "v255", "a255");

    {
      const int co = (4 * c.j + c.w) * (7 * 128) + 128 - (int)(pc & 127);
      c.rsC = mkrs((const void*)(pc + co), __builtin_amdgcn_readfirstlane(min(max(cl - co, 0), 7 * 128)));
    }
    u32x4 xv; u32 ep;
    asm volatile(
R"(
s_nop 4
buffer_load_dword v132, %[tq], %[rq], 0 offen
buffer_load_dword v132, %[vo], %[rc], 0 offen
s_mov_b32 %[so], 4096
buffer_load_dwordx4 v[132:135], %[ko], %[rk], %[so] offen offset:0 nt
buffer_load_dwordx4 v[136:139], %[ko], %[rk], %[so] offen offset:1024 nt
buffer_load_dwordx4 v[140:143], %[ko], %[rk], %[so] offen offset:2048 nt
buffer_load_dwordx4 v[144:147], %[ko], %[rk], %[so] offen offset:3072 nt
)"
      : [so] "=&s"(so)
      : [rk] "{s[64:67]}"(c.rsK), [rc] "{s[52:55]}"(c.rsC), [ko] "v"(k_off(c)), [vo] "v"(c.lane * 16), [rq] "s"(mkrs(p.wq, DM * NH * DH * 2)), [tq] "v"(touch_q(c))
      : "memory", "vcc", "scc");

    if (c.b >= 248 && c.w == 0) st64(c.rsW, c.lane == 0 ? 0 : OOB, WS_TS + (c.b & 7) * 8, u32x2{t0, t0});
    asm volatile("" : "+s"(p.wo), "+s"(p.wg), "+s"(p.wu), "+s"(p.wd), "+s"(p.out), "+s"(c.rsV), "+s"(c.rsK), "+s"(c.rsW), "+s"(c.rsC));
    {
    sv[0] = WS_LAYER0 + O_P1 + c.xg * 16384 + c.s * 2048 + (c.j & 3) * 512 + c.w * 128;
    sv[1] = WS_LAYER0 + O_P1 + c.xg * 16384;
    sv[2] = WS_LAYER0 + O_P2 + c.cp * 64 + 2 * c.w * REP;
    sv[3] = WS_LAYER0 + O_P2 + c.xg * REP;
    sv[4] = WS_LAYER0 + O_HB + (c.xg * HREP * 32 + c.j) * HSTR;
    sv[5] = WS_LAYER0 + O_HB + (c.xg * HREP + (HREP == 2 ? c.w >> 1 : HREP == 4 ? (c.w >> 1) + 2 * (c.j & 1) : 0)) * 32 * HSTR;
    sv[6] = WS_LAYER0 + WS_LAYER_STR + O_Q + c.cq * QSTR;
    sv[7] = WS_LAYER0 + WS_LAYER_STR + O_Q + c.h * 8 * QSTR;
    sw[0] = c.w == 0 && c.j < c.nb;
    sw[1] = c.w == 0;
#pragma unroll
    for (int k = 0; k < 8; ++k) sv[k] = __builtin_amdgcn_readfirstlane(sv[k]);
    sw[0] = __builtin_amdgcn_readfirstlane(sw[0]); sw[1] = __builtin_amdgcn_readfirstlane(sw[1]);
    asm volatile("" : "+{s[68:75]}"(sv), "+{s[76:77]}"(sw));
    }
    const u32x4 cv = {SENT, SENT, SENT, SENT};
    int c0, c1;
    {
      const int l = c.lane & 31, hi = c.lane >> 5;
      const int p1 = O_P1 + c.xg * 16384 + c.s * 2048 + (c.j & 3) * 512 + l * 16, p2 = (l >> 2) * REP + c.cp * 64 + (l & 3) * 16;
      const int L1 = WS_LAYER_STR;
      int a01 = (hi ? O_P2 + p2 : p1) + (c.w & 1) * L1, q2 = L1 + O_Q + c.cq * QSTR + l * 16, h2 = O_HB + ((c.xg * HREP + l - 2) * 32 + c.j) * HSTR, wv = c.w;
      asm volatile("" : "+v"(a01), "+v"(q2), "+v"(h2), "+v"(wv));
      h2 = l < 2 + HREP ? h2 : OOB;
      q2 = l < 2 ? q2 : h2;
      q2 = hi ? q2 : O_P3 + p2;
      q2 = wv == 2 ? q2 : OOB;
      c0 = wv < 2 ? a01 : q2;
      c0 += WS_LAYER0;
      c1 = !hi && c.j < c.nb ? WS_LAYER0 + L1 + O_P3 + (c.fb0 + c.j) * 2048 + c.w * 512 + l * 16 : OOB;
    }
    const int xo = (128 * c.w + 4 * (8 * c.g + (c.n & 7)) + 2 * (c.n >> 3)) * 2;
    asm volatile(
R"(
s_nop 4
buffer_load_dwordx4 %[xv], %[vo], %[rx], 0 offen
buffer_load_dword %[ep], off, %[rw], 0 sc1
buffer_store_dwordx4 %[cv], %[c0], %[rw], 0 offen sc1
buffer_store_dwordx4 %[cv], %[c1], %[rw], 0 offen sc1
buffer_load_dword %[xr], %[xo], %[rx], 0 offen
s_branch LP1_%=b
LP1_%=:
s_sleep 1
LP1_%=b:
s_memrealtime vcc
s_waitcnt lgkmcnt(0)
s_sub_u32 %[so], vcc_lo, %[t0]
s_cmp_lt_u32 %[so], 27
s_cbranch_scc1 LP1_%=
s_mov_b32 %[so], 8192
buffer_load_dwordx4 v[148:151], %[ko], %[rk], %[so] offen offset:0 nt
buffer_load_dwordx4 v[152:155], %[ko], %[rk], %[so] offen offset:1024 nt
buffer_load_dwordx4 v[156:159], %[ko], %[rk], %[so] offen offset:2048 nt
buffer_load_dwordx4 v[160:163], %[ko], %[rk], %[so] offen offset:3072 nt
s_branch LP2_%=b
LP2_%=:
s_sleep 1
LP2_%=b:
s_memrealtime vcc
s_waitcnt lgkmcnt(0)
s_sub_u32 %[so], vcc_lo, %[t0]
s_cmp_lt_u32 %[so], 89
s_cbranch_scc1 LP2_%=
s_mov_b32 %[so], 12288
buffer_load_dwordx4 v[164:167], %[ko], %[rk], %[so] offen offset:0 nt
buffer_load_dword v168, %[to], %[ro], 0 offen
buffer_load_dwordx4 v[168:171], %[ko], %[rk], %[so] offen offset:1024 nt
buffer_load_dwordx4 v[172:175], %[ko], %[rk], %[so] offen offset:2048 nt
buffer_load_dwordx4 v[176:179], %[ko], %[rk], %[so] offen offset:3072 nt
s_waitcnt vmcnt(23)
s_branch LP3_%=b
LP3_%=:
s_sleep 1
LP3_%=b:
s_memrealtime vcc
s_waitcnt lgkmcnt(0)
s_sub_u32 %[so], vcc_lo, %[t0]
s_cmp_lt_u32 %[so], 214
s_cbranch_scc1 LP3_%=
s_mov_b32 %[so], 16384
buffer_load_dwordx4 v[180:183], %[ko], %[rk], %[so] offen offset:0 nt
s_waitcnt vmcnt(23)
buffer_load_dwordx4 v[184:187], %[ko], %[rk], %[so] offen offset:1024 nt
s_waitcnt vmcnt(23)
buffer_load_dwordx4 v[188:191], %[ko], %[rk], %[so] offen offset:2048 nt
s_waitcnt vmcnt(23)
buffer_load_dwordx4 v[192:195], %[ko], %[rk], %[so] offen offset:3072 nt
s_waitcnt vmcnt(23)
s_branch LP4_%=b
LP4_%=:
s_sleep 1
LP4_%=b:
s_memrealtime vcc
s_waitcnt lgkmcnt(0)
s_sub_u32 %[so], vcc_lo, %[t0]
s_cmp_lt_u32 %[so], 277
s_cbranch_scc1 LP4_%=
s_mov_b32 %[so], 20480
buffer_load_dwordx4 v[196:199], %[ko], %[rk], %[so] offen offset:0 nt
s_waitcnt vmcnt(23)
buffer_load_dwordx2 v[36:37], %[qo], %[rq], 0 offen
s_waitcnt vmcnt(23)
buffer_load_dwordx4 v[200:203], %[ko], %[rk], %[so] offen offset:1024 nt
s_waitcnt vmcnt(23)
buffer_load_dwordx4 v[204:207], %[ko], %[rk], %[so] offen offset:2048 nt
s_waitcnt vmcnt(23)
s_mov_b32 %[so], 8192
buffer_load_dwordx2 v[38:39], %[qo], %[rq], %[so] offen
s_waitcnt vmcnt(23)
s_mov_b32 %[so], 20480
buffer_load_dwordx4 v[208:211], %[ko], %[rk], %[so] offen offset:3072 nt
s_waitcnt vmcnt(23)
s_branch LP5_%=b
LP5_%=:
s_sleep 1
LP5_%=b:
s_memrealtime vcc
s_waitcnt lgkmcnt(0)
s_sub_u32 %[so], vcc_lo, %[t0]
s_cmp_lt_u32 %[so], 339
s_cbranch_scc1 LP5_%=
s_mov_b32 %[so], 24576
buffer_load_dwordx4 v[212:215], %[ko], %[rk], %[so] offen offset:0 nt
s_waitcnt vmcnt(23)
s_mov_b32 %[so], 32768
buffer_load_dwordx2 v[40:41], %[qo], %[rq], %[so] offen
s_waitcnt vmcnt(23)
s_mov_b32 %[so], 24576
buffer_load_dwordx4 v[216:219], %[ko], %[rk], %[so] offen offset:1024 nt
s_waitcnt vmcnt(23)
buffer_load_dwordx4 v[220:223], %[ko], %[rk], %[so] offen offset:2048 nt
s_waitcnt vmcnt(23)
s_mov_b32 %[so], 40960
buffer_load_dwordx2 v[42:43], %[qo], %[rq], %[so] offen
s_waitcnt vmcnt(23)
s_mov_b32 %[so], 24576
buffer_load_dwordx4 v[224:227], %[ko], %[rk], %[so] offen offset:3072 nt
s_waitcnt vmcnt(23)
s_branch LP6_%=b
LP6_%=:
s_sleep 1
LP6_%=b:
s_memrealtime vcc
s_waitcnt lgkmcnt(0)
s_sub_u32 %[so], vcc_lo, %[t0]
s_cmp_lt_u32 %[so], 401
s_cbranch_scc1 LP6_%=
s_mov_b32 %[so], 28672
buffer_load_dwordx4 v[228:231], %[ko], %[rk], %[so] offen offset:0 nt
s_waitcnt vmcnt(23)
s_mov_b32 %[so], 65536
buffer_load_dwordx2 v[44:45], %[qo], %[rq], %[so] offen
s_waitcnt vmcnt(23)
s_mov_b32 %[so], 28672
buffer_load_dwordx4 v[232:235], %[ko], %[rk], %[so] offen offset:1024 nt
s_waitcnt vmcnt(23)
buffer_load_dwordx4 v[236:239], %[ko], %[rk], %[so] offen offset:2048 nt
s_waitcnt vmcnt(23)
s_mov_b32 %[so], 73728
buffer_load_dwordx2 v[46:47], %[qo], %[rq], %[so] offen
s_waitcnt vmcnt(23)
s_mov_b32 %[so], 28672
buffer_load_dwordx4 v[240:243], %[ko], %[rk], %[so] offen offset:3072 nt
s_waitcnt vmcnt(23)
s_branch LP7_%=b
LP7_%=:
s_sleep 1
LP7_%=b:
s_memrealtime vcc
s_waitcnt lgkmcnt(0)
s_sub_u32 %[so], vcc_lo, %[t0]
s_cmp_lt_u32 %[so], 464
s_cbranch_scc1 LP7_%=
s_mov_b32 %[so], 32768
buffer_load_dwordx4 v[244:247], %[ko], %[rk], %[so] offen offset:0 nt
s_waitcnt vmcnt(23)
s_mov_b32 %[so], 98304
buffer_load_dwordx2 v[48:49], %[qo], %[rq], %[so] offen
s_waitcnt vmcnt(23)
s_mov_b32 %[so], 32768
buffer_load_dwordx4 a[0:3], %[ko], %[rk], %[so] offen offset:1024 nt
s_waitcnt vmcnt(23)
buffer_load_dwordx4 a[4:7], %[ko], %[rk], %[so] offen offset:2048 nt
s_waitcnt vmcnt(23)
s_mov_b32 %[so], 106496
buffer_load_dwordx2 v[50:51], %[qo], %[rq], %[so] offen
s_waitcnt vmcnt(23)
s_mov_b32 %[so], 32768
buffer_load_dwordx4 a[8:11], %[ko], %[rk], %[so] offen offset:3072 nt
s_waitcnt vmcnt(23)
s_branch LP8_%=b
LP8_%=:
s_sleep 1
LP8_%=b:
s_memrealtime vcc
s_waitcnt lgkmcnt(0)
s_sub_u32 %[so], vcc_lo, %[t0]
s_cmp_lt_u32 %[so], 526
s_cbranch_scc1 LP8_%=
s_mov_b32 %[so], 36864
buffer_load_dwordx4 a[12:15], %[ko], %[rk], %[so] offen offset:0 nt
s_waitcnt vmcnt(23)
buffer_load_dwordx4 a[16:19], %[ko], %[rk], %[so] offen offset:1024 nt
s_waitcnt vmcnt(23)
buffer_load_dwordx4 a[20:23], %[ko], %[rk], %[so] offen offset:2048 nt
s_waitcnt vmcnt(23)
buffer_load_dwordx4 a[24:27], %[ko], %[rk], %[so] offen offset:3072 nt
s_waitcnt vmcnt(23)
s_branch LP9_%=b
LP9_%=:
s_sleep 1
LP9_%=b:
s_memrealtime vcc
s_waitcnt lgkmcnt(0)
s_sub_u32 %[so], vcc_lo, %[t0]
s_cmp_lt_u32 %[so], 589
s_cbranch_scc1 LP9_%=
s_mov_b32 %[so], 40960
buffer_load_dwordx4 a[28:31], %[ko], %[rk], %[so] offen offset:0 nt
s_waitcnt vmcnt(23)
buffer_load_dwordx4 a[32:35], %[ko], %[rk], %[so] offen offset:1024 nt
s_waitcnt vmcnt(23)
buffer_load_dwordx4 a[36:39], %[ko], %[rk], %[so] offen offset:2048 nt
s_waitcnt vmcnt(23)
buffer_load_dwordx4 a[40:43], %[ko], %[rk], %[so] offen offset:3072 nt
s_waitcnt vmcnt(23)
s_branch LP10_%=b
LP10_%=:
s_sleep 1
LP10_%=b:
s_memrealtime vcc
s_waitcnt lgkmcnt(0)
s_sub_u32 %[so], vcc_lo, %[t0]
s_cmp_lt_u32 %[so], 651
s_cbranch_scc1 LP10_%=
s_mov_b32 %[so], 45056
buffer_load_dwordx4 a[44:47], %[ko], %[rk], %[so] offen offset:0 nt
s_waitcnt vmcnt(23)
buffer_load_dwordx4 a[48:51], %[ko], %[rk], %[so] offen offset:1024 nt
s_waitcnt vmcnt(11)
)"
      : [xv] "=&v"(xv), [ep] "=&v"(ep), [xr] "=&v"(xres), [so] "=&s"(so)
      : [rk] "{s[64:67]}"(c.rsK), [rw] "{s[56:59]}"(c.rsW), [t0] "s"(t0), [ko] "v"(k_off(c)), [vo] "v"(c.lane * 16), [rx] "s"(mkrs(p.x, DM * 2)), [rq] "s"(mkrs(p.wq, DM * NH * DH * 2)), [ro] "s"(mkrs(p.wo, NH * DH * DM * 2)), [qo] "v"(wq_off(c)), [to] "v"(touch_o(c)), [cv] "v"(cv), [c0] "v"(c0), [c1] "v"(c1), [xo] "v"(xo)
      : "memory", "vcc", "scc");

    c.epoch = __builtin_amdgcn_readfirstlane(ep);
    u32x4 xa;
    {
      const u32 one = 0x3f803f80u;
      float s = dot2(xv[1], one, dot2(xv[0], one, 0.f)) + dot2(xv[3], one, dot2(xv[2], one, 0.f));
      const float v = dot2(xv[1], xv[1], dot2(xv[0], xv[0], 0.f)) + dot2(xv[3], xv[3], dot2(xv[2], xv[2], 0.f));
      { auto r = __builtin_amdgcn_permlane32_swap(asu(s), asu(v), false, false); s = asf(r[0]) + asf(r[1]); }
      s = row_sum(x16_sum(s));
      const float S = asf(__builtin_amdgcn_readlane(asu(s), 0)) * (1.f / DM), Q = asf(__builtin_amdgcn_readlane(asu(s), 32)) * (1.f / DM);
      const float var = rbf(fmaxf(__builtin_fmaf(-S, S, Q), 0.f));
      const float mb = rbf(S);
      const float inv = __builtin_amdgcn_rcpf(rbf(__builtin_amdgcn_sqrtf(rbf(var + 1e-5f))));
      const u32 d = pk2(bflo(xres) - mb, bfhi(xres) - mb);
      const u32 xn = pk2(bflo(d) * inv, bfhi(d) * inv);
      xa = u32x4{dppu<0x00>(xn), dppu<0x55>(xn), dppu<0xAA>(xn), dppu<0xFF>(xn)};
    }
    const u32 lw = c.lds + LDS_MISC + c.w * 1024 + (c.n & 3) * 64 + c.g * 4;
    asm volatile(
R"(
.p2align 3
s_nop 1
s_nop 0
s_mov_b32 s85, 0x05040100
s_mov_b32 s86, 0x07060302
v_perm_b32 v56, v38, v36, s85
v_perm_b32 v60, v38, v36, s86
v_perm_b32 v64, v39, v37, s85
v_perm_b32 v68, v39, v37, s86
v_perm_b32 v57, v42, v40, s85
v_perm_b32 v61, v42, v40, s86
v_perm_b32 v65, v43, v41, s85
v_perm_b32 v69, v43, v41, s86
v_perm_b32 v58, v46, v44, s85
v_perm_b32 v62, v46, v44, s86
v_perm_b32 v66, v47, v45, s85
v_perm_b32 v70, v47, v45, s86
v_perm_b32 v59, v50, v48, s85
v_perm_b32 v63, v50, v48, s86
v_perm_b32 v67, v51, v49, s85
v_perm_b32 v71, v51, v49, s86
v_mfma_f32_16x16x32_bf16 v[8:11], v[24:27], v[56:59], 0
v_mfma_f32_16x16x32_bf16 v[12:15], v[24:27], v[60:63], 0
v_mfma_f32_16x16x32_bf16 v[16:19], v[24:27], v[64:67], 0
v_mfma_f32_16x16x32_bf16 v[20:23], v[24:27], v[68:71], 0
s_mov_b32 exec_lo, 0x00f0000f
s_mov_b32 exec_hi, 0xf0000f00
s_nop 3
ds_write2_b32 %[lw], v8, v12 offset0:192 offset1:196
s_nop 0
ds_write2_b32 %[lw], v16, v20 offset0:200 offset1:204
s_mov_b64 exec, -1
s_waitcnt lgkmcnt(0)
s_barrier
)"
      :
      : "{v[24:27]}"(xa), [lw] "v"(lw)
      : "memory", "vcc", "scc", "s84", "s85", "s86", "s87", "s88", "s89", "s90", "s91", "s92", "s93", "s94", "s95", "s96", "s97", "s98", "s99", "v8", "v9", "v10", "v11", "v12", "v13", "v14", "v15", "v16", "v17", "v18", "v19", "v20", "v21", "v22", "v23");

    if (c.w == 0) {
      LDSP f32x4* r = (LDSP f32x4*)(size_t)(c.lds + LDS_MISC + 768 + c.n * 16);
      const f32x4 t = (r[0] + r[64]) + (r[128] + r[192]);
      const float y = (t[0] + t[1]) + (t[2] + t[3]);
      const u32 d = pk2(y, dppf<XOR1>(y));
      st64(c.rsW, c.lane < 16 && !(c.lane & 1) ? c.lane * 4 : OOB, WS_Q0 + c.cq * 64, u32x2{d, c.epoch + 1});
      st32(c.rsW, c.lane == 0 ? 0 : OOB, WS_ACK + c.b * 4, c.epoch + 1);
    }
    STAMP(25);
    derive(c);
    const int qp = ((c.n & 3) + 4 * c.g) * 32, qs = WS_Q0 + c.h * 512;
    u32x4 qa, qb; u32x2 tk;
    asm volatile(
R"(
.p2align 3
s_nop 4
s_mov_b32 %[so], 45056
buffer_load_dwordx4 a[52:55], %[ko], %[rk], %[so] offen offset:2048 nt
v_mov_b32 v108, 0
v_pk_add_u16 v109, v116, v116
v_pk_max_u16 v108, v108, v109
v_pk_add_u16 v109, v117, v117
v_pk_max_u16 v108, v108, v109
v_pk_add_u16 v109, v118, v118
v_pk_max_u16 v108, v108, v109
v_pk_add_u16 v109, v119, v119
v_pk_max_u16 v108, v108, v109
v_pk_add_u16 v109, v120, v120
v_pk_max_u16 v108, v108, v109
v_pk_add_u16 v109, v121, v121
v_pk_max_u16 v108, v108, v109
v_pk_add_u16 v109, v122, v122
v_pk_max_u16 v108, v108, v109
v_pk_add_u16 v109, v123, v123
v_pk_max_u16 v108, v108, v109
v_pk_add_u16 v109, v124, v124
v_pk_max_u16 v108, v108, v109
v_pk_add_u16 v109, v125, v125
v_pk_max_u16 v108, v108, v109
v_pk_add_u16 v109, v126, v126
v_pk_max_u16 v108, v108, v109
v_pk_add_u16 v109, v127, v127
v_pk_max_u16 v108, v108, v109
v_pk_add_u16 v109, v128, v128
v_pk_max_u16 v108, v108, v109
v_pk_add_u16 v109, v129, v129
v_pk_max_u16 v108, v108, v109
v_pk_add_u16 v109, v130, v130
v_pk_max_u16 v108, v108, v109
v_pk_add_u16 v109, v131, v131
v_pk_max_u16 v108, v108, v109
v_pk_add_u16 v109, v132, v132
v_pk_max_u16 v108, v108, v109
v_pk_add_u16 v109, v133, v133
v_pk_max_u16 v108, v108, v109
v_pk_add_u16 v109, v134, v134
v_pk_max_u16 v108, v108, v109
v_pk_add_u16 v109, v135, v135
v_pk_max_u16 v108, v108, v109
v_pk_add_u16 v109, v136, v136
v_pk_max_u16 v108, v108, v109
v_pk_add_u16 v109, v137, v137
v_pk_max_u16 v108, v108, v109
v_pk_add_u16 v109, v138, v138
v_pk_max_u16 v108, v108, v109
v_pk_add_u16 v109, v139, v139
v_pk_max_u16 v108, v108, v109
v_pk_add_u16 v109, v140, v140
v_pk_max_u16 v108, v108, v109
v_pk_add_u16 v109, v141, v141
v_pk_max_u16 v108, v108, v109
v_pk_add_u16 v109, v142, v142
v_pk_max_u16 v108, v108, v109
v_pk_add_u16 v109, v143, v143
v_pk_max_u16 v108, v108, v109
v_pk_add_u16 v109, v144, v144
v_pk_max_u16 v108, v108, v109
v_pk_add_u16 v109, v145, v145
v_pk_max_u16 v108, v108, v109
v_pk_add_u16 v109, v146, v146
v_pk_max_u16 v108, v108, v109
v_pk_add_u16 v109, v147, v147
v_pk_max_u16 v108, v108, v109
v_pk_max_u16 v108, v108, v108 op_sel:[0,1] op_sel_hi:[1,0]
s_nop 1
v_max_u32_dpp v108, v108, v108 quad_perm:[1,0,3,2] row_mask:0xf bank_mask:0xf
s_nop 1
v_max_u32_dpp v108, v108, v108 quad_perm:[2,3,0,1] row_mask:0xf bank_mask:0xf
s_nop 1
v_max_u32_dpp v108, v108, v108 row_half_mirror row_mask:0xf bank_mask:0xf
s_nop 1
v_max_u32_dpp v108, v108, v108 row_ror:8 row_mask:0xf bank_mask:0xf
s_nop 0
v_readlane_b32 s80, v108, 0
v_readlane_b32 s81, v108, 16
v_readlane_b32 s94, v108, 32
v_readlane_b32 s95, v108, 48
s_nop 1
s_max_u32 s80, s80, s81
s_max_u32 s94, s94, s95
s_max_u32 s80, s80, s94
s_lshr_b32 s80, s80, 25
s_max_u32 s80, s80, 13
s_nop 0
s_min_u32 s80, s80, 70
s_mul_i32 s81, s80, 0x02000200
s_sub_u32 s81, s81, 0x0d020d02
s_mul_i32 s80, s80, 0x01010101
v_mov_b32 v115, 0xfbfaf9f8
s_mov_b32 s96, 0x07050301
s_mov_b32 s97, 0x06040200
s_mov_b32 s98, 0x80808080
s_mov_b32 s99, 0xfffefdfc
buffer_load_dwordx4 a[56:59], %[ko], %[rk], %[so] offen offset:3072 nt
s_movk_i32 s94, 0
s_nop 0
v_perm_b32 v104, v117, v116, s96
v_perm_b32 v105, v119, v118, s96
v_perm_b32 v106, v117, v116, s97
v_perm_b32 v107, v119, v118, s97
v_pk_mad_u16 v108, v116, 2, -1 op_sel_hi:[1,0,0]
v_pk_mad_u16 v109, v117, 2, -1 op_sel_hi:[1,0,0]
v_pk_mad_u16 v110, v118, 2, -1 op_sel_hi:[1,0,0]
v_pk_mad_u16 v111, v119, 2, -1 op_sel_hi:[1,0,0]
v_and_b32 v112, 0x7f7f7f7f, v104
v_and_b32 v113, 0x7f7f7f7f, v105
v_sub_u32 v112, s80, v112
v_sub_u32 v113, s80, v113
v_pk_min_u16 v108, v108, v109
v_pk_min_u16 v110, v110, v111
v_pk_min_u16 v108, v108, v110
v_bitop3_b32 v109, v112, v113, s98 bitop3:0xa8
v_pk_sub_u16 v108, s81, v108 clamp
v_cmp_ne_u32_e64 vcc, v108, v109
v_perm_b32 v112, s99, v115, v112
v_perm_b32 v113, s99, v115, v113
v_bitop3_b32 v112, v112, v104, s98 bitop3:0x8d
v_bitop3_b32 v113, v113, v105, s98 bitop3:0x8d
v_alignbit_b32 v113, v113, v113, 28
v_or_b32 v114, v112, v113
s_getpc_b64 s[92:93]
s_cbranch_vccnz Lexc
v_mov_b32 v252, v114
v_mov_b32 v253, v106
v_mov_b32 v254, v107
s_branch LP11_%=b
LP11_%=:
s_sleep 1
LP11_%=b:
s_memrealtime vcc
s_waitcnt lgkmcnt(0)
s_sub_u32 %[so], vcc_lo, %[t0]
s_cmp_lt_u32 %[so], 714
s_cbranch_scc1 LP11_%=
s_mov_b32 %[so], 49152
buffer_load_dwordx4 a[60:63], %[ko], %[rk], %[so] offen offset:0 nt
s_movk_i32 s94, 64
v_perm_b32 v104, v121, v120, s96
v_perm_b32 v105, v123, v122, s96
v_perm_b32 v106, v121, v120, s97
v_perm_b32 v107, v123, v122, s97
v_pk_mad_u16 v108, v120, 2, -1 op_sel_hi:[1,0,0]
v_pk_mad_u16 v109, v121, 2, -1 op_sel_hi:[1,0,0]
v_pk_mad_u16 v110, v122, 2, -1 op_sel_hi:[1,0,0]
v_pk_mad_u16 v111, v123, 2, -1 op_sel_hi:[1,0,0]
v_and_b32 v112, 0x7f7f7f7f, v104
v_and_b32 v113, 0x7f7f7f7f, v105
v_sub_u32 v112, s80, v112
v_sub_u32 v113, s80, v113
v_pk_min_u16 v108, v108, v109
v_pk_min_u16 v110, v110, v111
v_pk_min_u16 v108, v108, v110
v_bitop3_b32 v109, v112, v113, s98 bitop3:0xa8
v_pk_sub_u16 v108, s81, v108 clamp
v_cmp_ne_u32_e64 vcc, v108, v109
v_perm_b32 v112, s99, v115, v112
v_perm_b32 v113, s99, v115, v113
v_bitop3_b32 v112, v112, v104, s98 bitop3:0x8d
v_bitop3_b32 v113, v113, v105, s98 bitop3:0x8d
v_alignbit_b32 v113, v113, v113, 28
v_or_b32 v114, v112, v113
s_getpc_b64 s[92:93]
s_cbranch_vccnz Lexc
v_mov_b32 v255, v114
ds_write_b128 %[lo], v[252:255] offset:0
v_mov_b32 v248, v106
v_mov_b32 v249, v107
buffer_load_dwordx2 v[36:37], %[oo], %[ro], 0 offen offset:0
buffer_load_dwordx4 a[64:67], %[ko], %[rk], %[so] offen offset:1024 nt
s_movk_i32 s94, 128
s_nop 0
v_perm_b32 v104, v125, v124, s96
v_perm_b32 v105, v127, v126, s96
v_perm_b32 v106, v125, v124, s97
v_perm_b32 v107, v127, v126, s97
v_pk_mad_u16 v108, v124, 2, -1 op_sel_hi:[1,0,0]
v_pk_mad_u16 v109, v125, 2, -1 op_sel_hi:[1,0,0]
v_pk_mad_u16 v110, v126, 2, -1 op_sel_hi:[1,0,0]
v_pk_mad_u16 v111, v127, 2, -1 op_sel_hi:[1,0,0]
v_and_b32 v112, 0x7f7f7f7f, v104
v_and_b32 v113, 0x7f7f7f7f, v105
v_sub_u32 v112, s80, v112
v_sub_u32 v113, s80, v113
v_pk_min_u16 v108, v108, v109
v_pk_min_u16 v110, v110, v111
v_pk_min_u16 v108, v108, v110
v_bitop3_b32 v109, v112, v113, s98 bitop3:0xa8
v_pk_sub_u16 v108, s81, v108 clamp
v_cmp_ne_u32_e64 vcc, v108, v109
v_perm_b32 v112, s99, v115, v112
v_perm_b32 v113, s99, v115, v113
v_bitop3_b32 v112, v112, v104, s98 bitop3:0x8d
v_bitop3_b32 v113, v113, v105, s98 bitop3:0x8d
v_alignbit_b32 v113, v113, v113, 28
v_or_b32 v114, v112, v113
s_getpc_b64 s[92:93]
s_cbranch_vccnz Lexc
v_mov_b32 v250, v114
v_mov_b32_e64 v251, v106
ds_write_b128 %[lo], v[248:251] offset:1024
v_mov_b32_e64 v252, v107
buffer_load_dwordx4 a[68:71], %[ko], %[rk], %[so] offen offset:2048 nt
s_movk_i32 s94, 192
s_nop 0
v_perm_b32 v104, v129, v128, s96
v_perm_b32 v105, v131, v130, s96
v_perm_b32 v106, v129, v128, s97
v_perm_b32 v107, v131, v130, s97
v_pk_mad_u16 v108, v128, 2, -1 op_sel_hi:[1,0,0]
v_pk_mad_u16 v109, v129, 2, -1 op_sel_hi:[1,0,0]
v_pk_mad_u16 v110, v130, 2, -1 op_sel_hi:[1,0,0]
v_pk_mad_u16 v111, v131, 2, -1 op_sel_hi:[1,0,0]
v_and_b32 v112, 0x7f7f7f7f, v104
v_and_b32 v113, 0x7f7f7f7f, v105
v_sub_u32 v112, s80, v112
v_sub_u32 v113, s80, v113
v_pk_min_u16 v108, v108, v109
v_pk_min_u16 v110, v110, v111
v_pk_min_u16 v108, v108, v110
v_bitop3_b32 v109, v112, v113, s98 bitop3:0xa8
v_pk_sub_u16 v108, s81, v108 clamp
v_cmp_ne_u32_e64 vcc, v108, v109
v_perm_b32 v112, s99, v115, v112
v_perm_b32 v113, s99, v115, v113
v_bitop3_b32 v112, v112, v104, s98 bitop3:0x8d
v_bitop3_b32 v113, v113, v105, s98 bitop3:0x8d
v_alignbit_b32 v113, v113, v113, 28
v_or_b32 v114, v112, v113
s_getpc_b64 s[92:93]
s_cbranch_vccnz Lexc
v_mov_b32 v253, v114
v_mov_b32 v254, v106
v_mov_b32 v255, v107
ds_write_b128 %[lo], v[252:255] offset:2048
s_movk_i32 s94, 256
s_nop 0
v_perm_b32 v104, v133, v132, s96
v_perm_b32 v105, v135, v134, s96
v_perm_b32 v106, v133, v132, s97
v_perm_b32 v107, v135, v134, s97
v_pk_mad_u16 v108, v132, 2, -1 op_sel_hi:[1,0,0]
v_pk_mad_u16 v109, v133, 2, -1 op_sel_hi:[1,0,0]
v_pk_mad_u16 v110, v134, 2, -1 op_sel_hi:[1,0,0]
v_pk_mad_u16 v111, v135, 2, -1 op_sel_hi:[1,0,0]
v_and_b32 v112, 0x7f7f7f7f, v104
v_and_b32 v113, 0x7f7f7f7f, v105
v_sub_u32 v112, s80, v112
v_sub_u32 v113, s80, v113
v_pk_min_u16 v108, v108, v109
v_pk_min_u16 v110, v110, v111
v_pk_min_u16 v108, v108, v110
v_bitop3_b32 v109, v112, v113, s98 bitop3:0xa8
v_pk_sub_u16 v108, s81, v108 clamp
v_cmp_ne_u32_e64 vcc, v108, v109
v_perm_b32 v112, s99, v115, v112
v_perm_b32 v113, s99, v115, v113
v_bitop3_b32 v112, v112, v104, s98 bitop3:0x8d
v_bitop3_b32 v113, v113, v105, s98 bitop3:0x8d
v_alignbit_b32 v113, v113, v113, 28
v_or_b32 v114, v112, v113
s_getpc_b64 s[92:93]
s_cbranch_vccnz Lexc
v_mov_b32 v248, v114
v_mov_b32 v249, v106
v_mov_b32 v250, v107
buffer_load_dwordx4 a[72:75], %[ko], %[rk], %[so] offen offset:3072 nt
s_movk_i32 s94, 320
s_nop 0
v_perm_b32 v104, v137, v136, s96
v_perm_b32 v105, v139, v138, s96
v_perm_b32 v106, v137, v136, s97
v_perm_b32 v107, v139, v138, s97
v_pk_mad_u16 v108, v136, 2, -1 op_sel_hi:[1,0,0]
v_pk_mad_u16 v109, v137, 2, -1 op_sel_hi:[1,0,0]
v_pk_mad_u16 v110, v138, 2, -1 op_sel_hi:[1,0,0]
v_pk_mad_u16 v111, v139, 2, -1 op_sel_hi:[1,0,0]
v_and_b32 v112, 0x7f7f7f7f, v104
v_and_b32 v113, 0x7f7f7f7f, v105
v_sub_u32 v112, s80, v112
v_sub_u32 v113, s80, v113
v_pk_min_u16 v108, v108, v109
v_pk_min_u16 v110, v110, v111
v_pk_min_u16 v108, v108, v110
v_bitop3_b32 v109, v112, v113, s98 bitop3:0xa8
v_pk_sub_u16 v108, s81, v108 clamp
v_cmp_ne_u32_e64 vcc, v108, v109
v_perm_b32 v112, s99, v115, v112
v_perm_b32 v113, s99, v115, v113
v_bitop3_b32 v112, v112, v104, s98 bitop3:0x8d
v_bitop3_b32 v113, v113, v105, s98 bitop3:0x8d
v_alignbit_b32 v113, v113, v113, 28
v_or_b32 v114, v112, v113
s_getpc_b64 s[92:93]
s_cbranch_vccnz Lexc
v_mov_b32 v251, v114
ds_write_b128 %[lo], v[248:251] offset:3072
v_mov_b32 v252, v106
v_mov_b32 v253, v107
s_mov_b32 %[so], 8192
buffer_load_dwordx2 v[38:39], %[oo], %[ro], %[so] offen offset:0
s_branch LP12_%=b
LP12_%=:
s_sleep 1
LP12_%=b:
s_memrealtime vcc
s_waitcnt lgkmcnt(0)
s_sub_u32 %[so], vcc_lo, %[t0]
s_cmp_lt_u32 %[so], 776
s_cbranch_scc1 LP12_%=
s_mov_b32 %[so], 53248
buffer_load_dwordx4 a[76:79], %[ko], %[rk], %[so] offen offset:0 nt
s_movk_i32 s94, 384
v_perm_b32 v104, v141, v140, s96
v_perm_b32 v105, v143, v142, s96
v_perm_b32 v106, v141, v140, s97
v_perm_b32 v107, v143, v142, s97
v_pk_mad_u16 v108, v140, 2, -1 op_sel_hi:[1,0,0]
v_pk_mad_u16 v109, v141, 2, -1 op_sel_hi:[1,0,0]
v_pk_mad_u16 v110, v142, 2, -1 op_sel_hi:[1,0,0]
v_pk_mad_u16 v111, v143, 2, -1 op_sel_hi:[1,0,0]
v_and_b32 v112, 0x7f7f7f7f, v104
v_and_b32 v113, 0x7f7f7f7f, v105
v_sub_u32 v112, s80, v112
v_sub_u32 v113, s80, v113
v_pk_min_u16 v108, v108, v109
v_pk_min_u16 v110, v110, v111
v_pk_min_u16 v108, v108, v110
v_bitop3_b32 v109, v112, v113, s98 bitop3:0xa8
v_pk_sub_u16 v108, s81, v108 clamp
v_cmp_ne_u32_e64 vcc, v108, v109
v_perm_b32 v112, s99, v115, v112
v_perm_b32 v113, s99, v115, v113
v_bitop3_b32 v112, v112, v104, s98 bitop3:0x8d
v_bitop3_b32 v113, v113, v105, s98 bitop3:0x8d
v_alignbit_b32 v113, v113, v113, 28
v_or_b32 v114, v112, v113
s_getpc_b64 s[92:93]
s_cbranch_vccnz Lexc
v_mov_b32 v254, v114
v_mov_b32_e64 v255, v106
ds_write_b128 %[lo], v[252:255] offset:4096
v_mov_b32_e64 v248, v107
buffer_load_dwordx4 a[80:83], %[ko], %[rk], %[so] offen offset:1024 nt
s_movk_i32 s94, 448
s_nop 0
v_perm_b32 v104, v145, v144, s96
v_perm_b32 v105, v147, v146, s96
v_perm_b32 v106, v145, v144, s97
v_perm_b32 v107, v147, v146, s97
v_pk_mad_u16 v108, v144, 2, -1 op_sel_hi:[1,0,0]
v_pk_mad_u16 v109, v145, 2, -1 op_sel_hi:[1,0,0]
v_pk_mad_u16 v110, v146, 2, -1 op_sel_hi:[1,0,0]
v_pk_mad_u16 v111, v147, 2, -1 op_sel_hi:[1,0,0]
v_and_b32 v112, 0x7f7f7f7f, v104
v_and_b32 v113, 0x7f7f7f7f, v105
v_sub_u32 v112, s80, v112
v_sub_u32 v113, s80, v113
v_pk_min_u16 v108, v108, v109
v_pk_min_u16 v110, v110, v111
v_pk_min_u16 v108, v108, v110
v_bitop3_b32 v109, v112, v113, s98 bitop3:0xa8
v_pk_sub_u16 v108, s81, v108 clamp
v_cmp_ne_u32_e64 vcc, v108, v109
v_perm_b32 v112, s99, v115, v112
v_perm_b32 v113, s99, v115, v113
v_bitop3_b32 v112, v112, v104, s98 bitop3:0x8d
)" R"(
v_bitop3_b32 v113, v113, v105, s98 bitop3:0x8d
v_alignbit_b32 v113, v113, v113, 28
v_or_b32 v114, v112, v113
s_getpc_b64 s[92:93]
s_cbranch_vccnz Lexc
v_mov_b32 v249, v114
v_mov_b32 v250, v106
v_mov_b32 v251, v107
ds_write_b128 %[lo], v[248:251] offset:5120
s_movk_i32 s94, 512
s_nop 0
v_perm_b32 v104, v149, v148, s96
v_perm_b32 v105, v151, v150, s96
v_perm_b32 v106, v149, v148, s97
v_perm_b32 v107, v151, v150, s97
v_pk_mad_u16 v108, v148, 2, -1 op_sel_hi:[1,0,0]
v_pk_mad_u16 v109, v149, 2, -1 op_sel_hi:[1,0,0]
v_pk_mad_u16 v110, v150, 2, -1 op_sel_hi:[1,0,0]
v_pk_mad_u16 v111, v151, 2, -1 op_sel_hi:[1,0,0]
v_and_b32 v112, 0x7f7f7f7f, v104
v_and_b32 v113, 0x7f7f7f7f, v105
v_sub_u32 v112, s80, v112
v_sub_u32 v113, s80, v113
v_pk_min_u16 v108, v108, v109
v_pk_min_u16 v110, v110, v111
v_pk_min_u16 v108, v108, v110
v_bitop3_b32 v109, v112, v113, s98 bitop3:0xa8
v_pk_sub_u16 v108, s81, v108 clamp
v_cmp_ne_u32_e64 vcc, v108, v109
v_perm_b32 v112, s99, v115, v112
v_perm_b32 v113, s99, v115, v113
v_bitop3_b32 v112, v112, v104, s98 bitop3:0x8d
v_bitop3_b32 v113, v113, v105, s98 bitop3:0x8d
v_alignbit_b32 v113, v113, v113, 28
v_or_b32 v114, v112, v113
s_getpc_b64 s[92:93]
s_cbranch_vccnz Lexc
v_mov_b32 v252, v114
v_mov_b32 v253, v106
v_mov_b32 v254, v107
buffer_load_dwordx4 a[84:87], %[ko], %[rk], %[so] offen offset:2048 nt
s_movk_i32 s94, 576
s_nop 0
v_perm_b32 v104, v153, v152, s96
v_perm_b32 v105, v155, v154, s96
v_perm_b32 v106, v153, v152, s97
v_perm_b32 v107, v155, v154, s97
v_pk_mad_u16 v108, v152, 2, -1 op_sel_hi:[1,0,0]
v_pk_mad_u16 v109, v153, 2, -1 op_sel_hi:[1,0,0]
v_pk_mad_u16 v110, v154, 2, -1 op_sel_hi:[1,0,0]
v_pk_mad_u16 v111, v155, 2, -1 op_sel_hi:[1,0,0]
v_and_b32 v112, 0x7f7f7f7f, v104
v_and_b32 v113, 0x7f7f7f7f, v105
v_sub_u32 v112, s80, v112
v_sub_u32 v113, s80, v113
v_pk_min_u16 v108, v108, v109
v_pk_min_u16 v110, v110, v111
v_pk_min_u16 v108, v108, v110
v_bitop3_b32 v109, v112, v113, s98 bitop3:0xa8
v_pk_sub_u16 v108, s81, v108 clamp
v_cmp_ne_u32_e64 vcc, v108, v109
v_perm_b32 v112, s99, v115, v112
v_perm_b32 v113, s99, v115, v113
v_bitop3_b32 v112, v112, v104, s98 bitop3:0x8d
v_bitop3_b32 v113, v113, v105, s98 bitop3:0x8d
v_alignbit_b32 v113, v113, v113, 28
v_or_b32 v114, v112, v113
s_getpc_b64 s[92:93]
s_cbranch_vccnz Lexc
v_mov_b32 v255, v114
ds_write_b128 %[lo], v[252:255] offset:6144
v_mov_b32 v248, v106
v_mov_b32 v249, v107
buffer_load_dwordx2 v[40:41], %[oo], %[ro], 0 offen offset:1024
buffer_load_dwordx4 a[88:91], %[ko], %[rk], %[so] offen offset:3072 nt
s_movk_i32 s94, 640
s_nop 0
v_perm_b32 v104, v157, v156, s96
v_perm_b32 v105, v159, v158, s96
v_perm_b32 v106, v157, v156, s97
v_perm_b32 v107, v159, v158, s97
v_pk_mad_u16 v108, v156, 2, -1 op_sel_hi:[1,0,0]
v_pk_mad_u16 v109, v157, 2, -1 op_sel_hi:[1,0,0]
v_pk_mad_u16 v110, v158, 2, -1 op_sel_hi:[1,0,0]
v_pk_mad_u16 v111, v159, 2, -1 op_sel_hi:[1,0,0]
v_and_b32 v112, 0x7f7f7f7f, v104
v_and_b32 v113, 0x7f7f7f7f, v105
v_sub_u32 v112, s80, v112
v_sub_u32 v113, s80, v113
v_pk_min_u16 v108, v108, v109
v_pk_min_u16 v110, v110, v111
v_pk_min_u16 v108, v108, v110
v_bitop3_b32 v109, v112, v113, s98 bitop3:0xa8
v_pk_sub_u16 v108, s81, v108 clamp
v_cmp_ne_u32_e64 vcc, v108, v109
v_perm_b32 v112, s99, v115, v112
v_perm_b32 v113, s99, v115, v113
v_bitop3_b32 v112, v112, v104, s98 bitop3:0x8d
v_bitop3_b32 v113, v113, v105, s98 bitop3:0x8d
v_alignbit_b32 v113, v113, v113, 28
v_or_b32 v114, v112, v113
s_getpc_b64 s[92:93]
s_cbranch_vccnz Lexc
v_mov_b32 v250, v114
v_mov_b32_e64 v251, v106
ds_write_b128 %[lo], v[248:251] offset:7168
v_mov_b32 v252, v107
s_waitcnt vmcnt(23)
s_branch LP13_%=b
LP13_%=:
s_sleep 1
LP13_%=b:
s_memrealtime vcc
s_waitcnt lgkmcnt(0)
s_sub_u32 %[so], vcc_lo, %[t0]
s_cmp_lt_u32 %[so], 839
s_cbranch_scc1 LP13_%=
s_mov_b32 %[so], 57344
buffer_load_dwordx4 a[92:95], %[ko], %[rk], %[so] offen offset:0 nt
s_movk_i32 s94, 704
v_perm_b32 v104, v161, v160, s96
v_perm_b32 v105, v163, v162, s96
v_perm_b32 v106, v161, v160, s97
v_perm_b32 v107, v163, v162, s97
v_pk_mad_u16 v108, v160, 2, -1 op_sel_hi:[1,0,0]
v_pk_mad_u16 v109, v161, 2, -1 op_sel_hi:[1,0,0]
v_pk_mad_u16 v110, v162, 2, -1 op_sel_hi:[1,0,0]
v_pk_mad_u16 v111, v163, 2, -1 op_sel_hi:[1,0,0]
v_and_b32 v112, 0x7f7f7f7f, v104
v_and_b32 v113, 0x7f7f7f7f, v105
v_sub_u32 v112, s80, v112
v_sub_u32 v113, s80, v113
v_pk_min_u16 v108, v108, v109
v_pk_min_u16 v110, v110, v111
v_pk_min_u16 v108, v108, v110
v_bitop3_b32 v109, v112, v113, s98 bitop3:0xa8
v_pk_sub_u16 v108, s81, v108 clamp
v_cmp_ne_u32_e64 vcc, v108, v109
v_perm_b32 v112, s99, v115, v112
v_perm_b32 v113, s99, v115, v113
v_bitop3_b32 v112, v112, v104, s98 bitop3:0x8d
v_bitop3_b32 v113, v113, v105, s98 bitop3:0x8d
v_alignbit_b32 v113, v113, v113, 28
v_or_b32 v114, v112, v113
s_getpc_b64 s[92:93]
s_cbranch_vccnz Lexc
v_mov_b32 v253, v114
v_mov_b32 v254, v106
v_mov_b32 v255, v107
ds_write_b128 %[lo], v[252:255] offset:8192
s_movk_i32 s94, 768
s_nop 0
v_perm_b32 v104, v165, v164, s96
v_perm_b32 v105, v167, v166, s96
v_perm_b32 v106, v165, v164, s97
v_perm_b32 v107, v167, v166, s97
v_pk_mad_u16 v108, v164, 2, -1 op_sel_hi:[1,0,0]
v_pk_mad_u16 v109, v165, 2, -1 op_sel_hi:[1,0,0]
v_pk_mad_u16 v110, v166, 2, -1 op_sel_hi:[1,0,0]
v_pk_mad_u16 v111, v167, 2, -1 op_sel_hi:[1,0,0]
v_and_b32 v112, 0x7f7f7f7f, v104
v_and_b32 v113, 0x7f7f7f7f, v105
v_sub_u32 v112, s80, v112
v_sub_u32 v113, s80, v113
v_pk_min_u16 v108, v108, v109
v_pk_min_u16 v110, v110, v111
v_pk_min_u16 v108, v108, v110
v_bitop3_b32 v109, v112, v113, s98 bitop3:0xa8
v_pk_sub_u16 v108, s81, v108 clamp
v_cmp_ne_u32_e64 vcc, v108, v109
v_perm_b32 v112, s99, v115, v112
v_perm_b32 v113, s99, v115, v113
v_bitop3_b32 v112, v112, v104, s98 bitop3:0x8d
v_bitop3_b32 v113, v113, v105, s98 bitop3:0x8d
v_alignbit_b32 v113, v113, v113, 28
v_or_b32 v114, v112, v113
s_getpc_b64 s[92:93]
s_cbranch_vccnz Lexc
v_mov_b32 v248, v114
v_mov_b32 v249, v106
v_mov_b32 v250, v107
s_waitcnt vmcnt(23)
buffer_load_dwordx4 a[96:99], %[ko], %[rk], %[so] offen offset:1024 nt
s_movk_i32 s94, 832
v_perm_b32 v104, v169, v168, s96
v_perm_b32 v105, v171, v170, s96
v_perm_b32 v106, v169, v168, s97
v_perm_b32 v107, v171, v170, s97
v_pk_mad_u16 v108, v168, 2, -1 op_sel_hi:[1,0,0]
v_pk_mad_u16 v109, v169, 2, -1 op_sel_hi:[1,0,0]
v_pk_mad_u16 v110, v170, 2, -1 op_sel_hi:[1,0,0]
v_pk_mad_u16 v111, v171, 2, -1 op_sel_hi:[1,0,0]
v_and_b32 v112, 0x7f7f7f7f, v104
v_and_b32 v113, 0x7f7f7f7f, v105
v_sub_u32 v112, s80, v112
v_sub_u32 v113, s80, v113
v_pk_min_u16 v108, v108, v109
v_pk_min_u16 v110, v110, v111
v_pk_min_u16 v108, v108, v110
v_bitop3_b32 v109, v112, v113, s98 bitop3:0xa8
v_pk_sub_u16 v108, s81, v108 clamp
v_cmp_ne_u32_e64 vcc, v108, v109
v_perm_b32 v112, s99, v115, v112
v_perm_b32 v113, s99, v115, v113
v_bitop3_b32 v112, v112, v104, s98 bitop3:0x8d
v_bitop3_b32 v113, v113, v105, s98 bitop3:0x8d
v_alignbit_b32 v113, v113, v113, 28
v_or_b32 v114, v112, v113
s_getpc_b64 s[92:93]
s_cbranch_vccnz Lexc
v_mov_b32 v251, v114
ds_write_b128 %[lo], v[248:251] offset:9216
v_mov_b32 v252, v106
v_mov_b32 v253, v107
s_waitcnt vmcnt(23)
s_mov_b32 %[so], 8192
buffer_load_dwordx2 v[42:43], %[oo], %[ro], %[so] offen offset:1024
s_waitcnt vmcnt(23)
s_mov_b32 %[so], 57344
buffer_load_dwordx4 a[100:103], %[ko], %[rk], %[so] offen offset:2048 nt
s_movk_i32 s94, 896
s_nop 0
v_perm_b32 v104, v173, v172, s96
v_perm_b32 v105, v175, v174, s96
v_perm_b32 v106, v173, v172, s97
v_perm_b32 v107, v175, v174, s97
v_pk_mad_u16 v108, v172, 2, -1 op_sel_hi:[1,0,0]
v_pk_mad_u16 v109, v173, 2, -1 op_sel_hi:[1,0,0]
v_pk_mad_u16 v110, v174, 2, -1 op_sel_hi:[1,0,0]
v_pk_mad_u16 v111, v175, 2, -1 op_sel_hi:[1,0,0]
v_and_b32 v112, 0x7f7f7f7f, v104
v_and_b32 v113, 0x7f7f7f7f, v105
v_sub_u32 v112, s80, v112
v_sub_u32 v113, s80, v113
v_pk_min_u16 v108, v108, v109
v_pk_min_u16 v110, v110, v111
v_pk_min_u16 v108, v108, v110
v_bitop3_b32 v109, v112, v113, s98 bitop3:0xa8
v_pk_sub_u16 v108, s81, v108 clamp
v_cmp_ne_u32_e64 vcc, v108, v109
v_perm_b32 v112, s99, v115, v112
v_perm_b32 v113, s99, v115, v113
v_bitop3_b32 v112, v112, v104, s98 bitop3:0x8d
v_bitop3_b32 v113, v113, v105, s98 bitop3:0x8d
v_alignbit_b32 v113, v113, v113, 28
v_or_b32 v114, v112, v113
s_getpc_b64 s[92:93]
s_cbranch_vccnz Lexc
v_mov_b32 v254, v114
v_mov_b32_e64 v255, v106
ds_write_b128 %[lo], v[252:255] offset:10240
v_mov_b32 v248, v107
s_waitcnt vmcnt(23)
buffer_load_dwordx4 a[104:107], %[ko], %[rk], %[so] offen offset:3072 nt
s_movk_i32 s94, 960
s_nop 0
v_perm_b32 v104, v177, v176, s96
v_perm_b32 v105, v179, v178, s96
v_perm_b32 v106, v177, v176, s97
v_perm_b32 v107, v179, v178, s97
v_pk_mad_u16 v108, v176, 2, -1 op_sel_hi:[1,0,0]
v_pk_mad_u16 v109, v177, 2, -1 op_sel_hi:[1,0,0]
v_pk_mad_u16 v110, v178, 2, -1 op_sel_hi:[1,0,0]
v_pk_mad_u16 v111, v179, 2, -1 op_sel_hi:[1,0,0]
v_and_b32 v112, 0x7f7f7f7f, v104
v_and_b32 v113, 0x7f7f7f7f, v105
v_sub_u32 v112, s80, v112
v_sub_u32 v113, s80, v113
v_pk_min_u16 v108, v108, v109
v_pk_min_u16 v110, v110, v111
v_pk_min_u16 v108, v108, v110
v_bitop3_b32 v109, v112, v113, s98 bitop3:0xa8
v_pk_sub_u16 v108, s81, v108 clamp
v_cmp_ne_u32_e64 vcc, v108, v109
v_perm_b32 v112, s99, v115, v112
v_perm_b32 v113, s99, v115, v113
v_bitop3_b32 v112, v112, v104, s98 bitop3:0x8d
v_bitop3_b32 v113, v113, v105, s98 bitop3:0x8d
v_alignbit_b32 v113, v113, v113, 28
v_or_b32 v114, v112, v113
s_getpc_b64 s[92:93]
s_cbranch_vccnz Lexc
v_mov_b32 v249, v114
v_mov_b32 v250, v106
v_mov_b32 v251, v107
ds_write_b128 %[lo], v[248:251] offset:11264
s_waitcnt vmcnt(23)
s_branch LP14_%=b
LP14_%=:
s_sleep 1
LP14_%=b:
s_memrealtime vcc
s_waitcnt lgkmcnt(0)
s_sub_u32 %[so], vcc_lo, %[t0]
s_cmp_lt_u32 %[so], 901
s_cbranch_scc1 LP14_%=
s_mov_b32 %[so], 61440
buffer_load_dwordx4 a[108:111], %[ko], %[rk], %[so] offen offset:0 nt
s_movk_i32 s94, 1024
s_nop 0
v_perm_b32 v104, v181, v180, s96
v_perm_b32 v105, v183, v182, s96
v_perm_b32 v106, v181, v180, s97
v_perm_b32 v107, v183, v182, s97
v_pk_mad_u16 v108, v180, 2, -1 op_sel_hi:[1,0,0]
v_pk_mad_u16 v109, v181, 2, -1 op_sel_hi:[1,0,0]
v_pk_mad_u16 v110, v182, 2, -1 op_sel_hi:[1,0,0]
v_pk_mad_u16 v111, v183, 2, -1 op_sel_hi:[1,0,0]
v_and_b32 v112, 0x7f7f7f7f, v104
v_and_b32 v113, 0x7f7f7f7f, v105
v_sub_u32 v112, s80, v112
v_sub_u32 v113, s80, v113
v_pk_min_u16 v108, v108, v109
v_pk_min_u16 v110, v110, v111
v_pk_min_u16 v108, v108, v110
v_bitop3_b32 v109, v112, v113, s98 bitop3:0xa8
v_pk_sub_u16 v108, s81, v108 clamp
v_cmp_ne_u32_e64 vcc, v108, v109
v_perm_b32 v112, s99, v115, v112
v_perm_b32 v113, s99, v115, v113
v_bitop3_b32 v112, v112, v104, s98 bitop3:0x8d
v_bitop3_b32 v113, v113, v105, s98 bitop3:0x8d
v_alignbit_b32 v113, v113, v113, 28
v_or_b32 v114, v112, v113
s_getpc_b64 s[92:93]
s_cbranch_vccnz Lexc
v_mov_b32 v252, v114
v_mov_b32 v253, v106
v_mov_b32 v254, v107
s_movk_i32 s94, 1088
s_nop 0
v_perm_b32 v104, v185, v184, s96
v_perm_b32 v105, v187, v186, s96
v_perm_b32 v106, v185, v184, s97
v_perm_b32 v107, v187, v186, s97
v_pk_mad_u16 v108, v184, 2, -1 op_sel_hi:[1,0,0]
v_pk_mad_u16 v109, v185, 2, -1 op_sel_hi:[1,0,0]
v_pk_mad_u16 v110, v186, 2, -1 op_sel_hi:[1,0,0]
v_pk_mad_u16 v111, v187, 2, -1 op_sel_hi:[1,0,0]
v_and_b32 v112, 0x7f7f7f7f, v104
v_and_b32 v113, 0x7f7f7f7f, v105
v_sub_u32 v112, s80, v112
v_sub_u32 v113, s80, v113
v_pk_min_u16 v108, v108, v109
v_pk_min_u16 v110, v110, v111
v_pk_min_u16 v108, v108, v110
v_bitop3_b32 v109, v112, v113, s98 bitop3:0xa8
v_pk_sub_u16 v108, s81, v108 clamp
v_cmp_ne_u32_e64 vcc, v108, v109
v_perm_b32 v112, s99, v115, v112
v_perm_b32 v113, s99, v115, v113
v_bitop3_b32 v112, v112, v104, s98 bitop3:0x8d
v_bitop3_b32 v113, v113, v105, s98 bitop3:0x8d
v_alignbit_b32 v113, v113, v113, 28
v_or_b32 v114, v112, v113
s_getpc_b64 s[92:93]
s_cbranch_vccnz Lexc
v_mov_b32 v255, v114
ds_write_b128 %[lo], v[252:255] offset:12288
v_mov_b32 v248, v106
v_mov_b32 v249, v107
s_waitcnt vmcnt(23)
buffer_load_dwordx2 v[44:45], %[oo], %[ro], 0 offen offset:2048
s_waitcnt vmcnt(23)
buffer_load_dwordx4 a[112:115], %[ko], %[rk], %[so] offen offset:1024 nt
s_movk_i32 s94, 1152
s_nop 0
v_perm_b32 v104, v189, v188, s96
)" R"(
v_perm_b32 v105, v191, v190, s96
v_perm_b32 v106, v189, v188, s97
v_perm_b32 v107, v191, v190, s97
v_pk_mad_u16 v108, v188, 2, -1 op_sel_hi:[1,0,0]
v_pk_mad_u16 v109, v189, 2, -1 op_sel_hi:[1,0,0]
v_pk_mad_u16 v110, v190, 2, -1 op_sel_hi:[1,0,0]
v_pk_mad_u16 v111, v191, 2, -1 op_sel_hi:[1,0,0]
v_and_b32 v112, 0x7f7f7f7f, v104
v_and_b32 v113, 0x7f7f7f7f, v105
v_sub_u32 v112, s80, v112
v_sub_u32 v113, s80, v113
v_pk_min_u16 v108, v108, v109
v_pk_min_u16 v110, v110, v111
v_pk_min_u16 v108, v108, v110
v_bitop3_b32 v109, v112, v113, s98 bitop3:0xa8
v_pk_sub_u16 v108, s81, v108 clamp
v_cmp_ne_u32_e64 vcc, v108, v109
v_perm_b32 v112, s99, v115, v112
v_perm_b32 v113, s99, v115, v113
v_bitop3_b32 v112, v112, v104, s98 bitop3:0x8d
v_bitop3_b32 v113, v113, v105, s98 bitop3:0x8d
v_alignbit_b32 v113, v113, v113, 28
v_or_b32 v114, v112, v113
s_getpc_b64 s[92:93]
s_cbranch_vccnz Lexc
v_mov_b32 v250, v114
v_mov_b32_e64 v251, v106
ds_write_b128 %[lo], v[248:251] offset:13312
v_mov_b32 v252, v107
s_waitcnt vmcnt(23)
buffer_load_dwordx4 a[116:119], %[ko], %[rk], %[so] offen offset:2048 nt
s_movk_i32 s94, 1216
s_nop 0
v_perm_b32 v104, v193, v192, s96
v_perm_b32 v105, v195, v194, s96
v_perm_b32 v106, v193, v192, s97
v_perm_b32 v107, v195, v194, s97
v_pk_mad_u16 v108, v192, 2, -1 op_sel_hi:[1,0,0]
v_pk_mad_u16 v109, v193, 2, -1 op_sel_hi:[1,0,0]
v_pk_mad_u16 v110, v194, 2, -1 op_sel_hi:[1,0,0]
v_pk_mad_u16 v111, v195, 2, -1 op_sel_hi:[1,0,0]
v_and_b32 v112, 0x7f7f7f7f, v104
v_and_b32 v113, 0x7f7f7f7f, v105
v_sub_u32 v112, s80, v112
v_sub_u32 v113, s80, v113
v_pk_min_u16 v108, v108, v109
v_pk_min_u16 v110, v110, v111
v_pk_min_u16 v108, v108, v110
v_bitop3_b32 v109, v112, v113, s98 bitop3:0xa8
v_pk_sub_u16 v108, s81, v108 clamp
v_cmp_ne_u32_e64 vcc, v108, v109
v_perm_b32 v112, s99, v115, v112
v_perm_b32 v113, s99, v115, v113
v_bitop3_b32 v112, v112, v104, s98 bitop3:0x8d
v_bitop3_b32 v113, v113, v105, s98 bitop3:0x8d
v_alignbit_b32 v113, v113, v113, 28
v_or_b32 v114, v112, v113
s_getpc_b64 s[92:93]
s_cbranch_vccnz Lexc
v_mov_b32 v253, v114
v_mov_b32 v254, v106
v_mov_b32 v255, v107
ds_write_b128 %[lo], v[252:255] offset:14336
s_waitcnt vmcnt(23)
buffer_load_dwordx4 a[120:123], %[ko], %[rk], %[so] offen offset:3072 nt
s_movk_i32 s94, 1280
v_perm_b32 v104, v197, v196, s96
v_perm_b32 v105, v199, v198, s96
v_perm_b32 v106, v197, v196, s97
v_perm_b32 v107, v199, v198, s97
v_pk_mad_u16 v108, v196, 2, -1 op_sel_hi:[1,0,0]
v_pk_mad_u16 v109, v197, 2, -1 op_sel_hi:[1,0,0]
v_pk_mad_u16 v110, v198, 2, -1 op_sel_hi:[1,0,0]
v_pk_mad_u16 v111, v199, 2, -1 op_sel_hi:[1,0,0]
v_and_b32 v112, 0x7f7f7f7f, v104
v_and_b32 v113, 0x7f7f7f7f, v105
v_sub_u32 v112, s80, v112
v_sub_u32 v113, s80, v113
v_pk_min_u16 v108, v108, v109
v_pk_min_u16 v110, v110, v111
v_pk_min_u16 v108, v108, v110
v_bitop3_b32 v109, v112, v113, s98 bitop3:0xa8
v_pk_sub_u16 v108, s81, v108 clamp
v_cmp_ne_u32_e64 vcc, v108, v109
v_perm_b32 v112, s99, v115, v112
v_perm_b32 v113, s99, v115, v113
v_bitop3_b32 v112, v112, v104, s98 bitop3:0x8d
v_bitop3_b32 v113, v113, v105, s98 bitop3:0x8d
v_alignbit_b32 v113, v113, v113, 28
v_or_b32 v114, v112, v113
s_getpc_b64 s[92:93]
s_cbranch_vccnz Lexc
v_mov_b32 v248, v114
v_mov_b32 v249, v106
v_mov_b32 v250, v107
s_movk_i32 s94, 1344
s_nop 0
v_perm_b32 v104, v201, v200, s96
v_perm_b32 v105, v203, v202, s96
v_perm_b32 v106, v201, v200, s97
v_perm_b32 v107, v203, v202, s97
v_pk_mad_u16 v108, v200, 2, -1 op_sel_hi:[1,0,0]
v_pk_mad_u16 v109, v201, 2, -1 op_sel_hi:[1,0,0]
v_pk_mad_u16 v110, v202, 2, -1 op_sel_hi:[1,0,0]
v_pk_mad_u16 v111, v203, 2, -1 op_sel_hi:[1,0,0]
v_and_b32 v112, 0x7f7f7f7f, v104
v_and_b32 v113, 0x7f7f7f7f, v105
v_sub_u32 v112, s80, v112
v_sub_u32 v113, s80, v113
v_pk_min_u16 v108, v108, v109
v_pk_min_u16 v110, v110, v111
v_pk_min_u16 v108, v108, v110
v_bitop3_b32 v109, v112, v113, s98 bitop3:0xa8
v_pk_sub_u16 v108, s81, v108 clamp
v_cmp_ne_u32_e64 vcc, v108, v109
v_perm_b32 v112, s99, v115, v112
v_perm_b32 v113, s99, v115, v113
v_bitop3_b32 v112, v112, v104, s98 bitop3:0x8d
v_bitop3_b32 v113, v113, v105, s98 bitop3:0x8d
v_alignbit_b32 v113, v113, v113, 28
v_or_b32 v114, v112, v113
s_getpc_b64 s[92:93]
s_cbranch_vccnz Lexc
v_mov_b32 v251, v114
ds_write_b128 %[lo], v[248:251] offset:15360
v_mov_b32 v252, v106
v_mov_b32 v253, v107
s_waitcnt vmcnt(23)
s_mov_b32 %[so], 8192
buffer_load_dwordx2 v[46:47], %[oo], %[ro], %[so] offen offset:2048
s_waitcnt vmcnt(23)
s_branch LP15_%=b
LP15_%=:
s_sleep 1
LP15_%=b:
s_memrealtime vcc
s_waitcnt lgkmcnt(0)
s_sub_u32 %[so], vcc_lo, %[t0]
s_cmp_lt_u32 %[so], 963
s_cbranch_scc1 LP15_%=
s_mov_b32 %[so], 65536
buffer_load_dwordx4 a[124:127], %[ko], %[rk], %[so] offen offset:0 nt
s_movk_i32 s94, 1408
v_perm_b32 v104, v205, v204, s96
v_perm_b32 v105, v207, v206, s96
v_perm_b32 v106, v205, v204, s97
v_perm_b32 v107, v207, v206, s97
v_pk_mad_u16 v108, v204, 2, -1 op_sel_hi:[1,0,0]
v_pk_mad_u16 v109, v205, 2, -1 op_sel_hi:[1,0,0]
v_pk_mad_u16 v110, v206, 2, -1 op_sel_hi:[1,0,0]
v_pk_mad_u16 v111, v207, 2, -1 op_sel_hi:[1,0,0]
v_and_b32 v112, 0x7f7f7f7f, v104
v_and_b32 v113, 0x7f7f7f7f, v105
v_sub_u32 v112, s80, v112
v_sub_u32 v113, s80, v113
v_pk_min_u16 v108, v108, v109
v_pk_min_u16 v110, v110, v111
v_pk_min_u16 v108, v108, v110
v_bitop3_b32 v109, v112, v113, s98 bitop3:0xa8
v_pk_sub_u16 v108, s81, v108 clamp
v_cmp_ne_u32_e64 vcc, v108, v109
v_perm_b32 v112, s99, v115, v112
v_perm_b32 v113, s99, v115, v113
v_bitop3_b32 v112, v112, v104, s98 bitop3:0x8d
v_bitop3_b32 v113, v113, v105, s98 bitop3:0x8d
v_alignbit_b32 v113, v113, v113, 28
v_or_b32 v114, v112, v113
s_getpc_b64 s[92:93]
s_cbranch_vccnz Lexc
v_mov_b32 v254, v114
v_mov_b32_e64 v255, v106
ds_write_b128 %[lo], v[252:255] offset:16384
v_mov_b32 v248, v107
s_waitcnt vmcnt(23)
buffer_load_dwordx4 a[128:131], %[ko], %[rk], %[so] offen offset:1024 nt
s_movk_i32 s94, 1472
s_nop 0
v_perm_b32 v104, v209, v208, s96
v_perm_b32 v105, v211, v210, s96
v_perm_b32 v106, v209, v208, s97
v_perm_b32 v107, v211, v210, s97
v_pk_mad_u16 v108, v208, 2, -1 op_sel_hi:[1,0,0]
v_pk_mad_u16 v109, v209, 2, -1 op_sel_hi:[1,0,0]
v_pk_mad_u16 v110, v210, 2, -1 op_sel_hi:[1,0,0]
v_pk_mad_u16 v111, v211, 2, -1 op_sel_hi:[1,0,0]
v_and_b32 v112, 0x7f7f7f7f, v104
v_and_b32 v113, 0x7f7f7f7f, v105
v_sub_u32 v112, s80, v112
v_sub_u32 v113, s80, v113
v_pk_min_u16 v108, v108, v109
v_pk_min_u16 v110, v110, v111
v_pk_min_u16 v108, v108, v110
v_bitop3_b32 v109, v112, v113, s98 bitop3:0xa8
v_pk_sub_u16 v108, s81, v108 clamp
v_cmp_ne_u32_e64 vcc, v108, v109
v_perm_b32 v112, s99, v115, v112
v_perm_b32 v113, s99, v115, v113
v_bitop3_b32 v112, v112, v104, s98 bitop3:0x8d
v_bitop3_b32 v113, v113, v105, s98 bitop3:0x8d
v_alignbit_b32 v113, v113, v113, 28
v_or_b32 v114, v112, v113
s_getpc_b64 s[92:93]
s_cbranch_vccnz Lexc
v_mov_b32 v249, v114
v_mov_b32 v250, v106
v_mov_b32 v251, v107
ds_write_b128 %[lo], v[248:251] offset:17408
s_waitcnt vmcnt(23)
buffer_load_dwordx4 a[132:135], %[ko], %[rk], %[so] offen offset:2048 nt
s_movk_i32 s94, 1536
v_perm_b32 v104, v213, v212, s96
v_perm_b32 v105, v215, v214, s96
v_perm_b32 v106, v213, v212, s97
v_perm_b32 v107, v215, v214, s97
v_pk_mad_u16 v108, v212, 2, -1 op_sel_hi:[1,0,0]
v_pk_mad_u16 v109, v213, 2, -1 op_sel_hi:[1,0,0]
v_pk_mad_u16 v110, v214, 2, -1 op_sel_hi:[1,0,0]
v_pk_mad_u16 v111, v215, 2, -1 op_sel_hi:[1,0,0]
v_and_b32 v112, 0x7f7f7f7f, v104
v_and_b32 v113, 0x7f7f7f7f, v105
v_sub_u32 v112, s80, v112
v_sub_u32 v113, s80, v113
v_pk_min_u16 v108, v108, v109
v_pk_min_u16 v110, v110, v111
v_pk_min_u16 v108, v108, v110
v_bitop3_b32 v109, v112, v113, s98 bitop3:0xa8
v_pk_sub_u16 v108, s81, v108 clamp
v_cmp_ne_u32_e64 vcc, v108, v109
v_perm_b32 v112, s99, v115, v112
v_perm_b32 v113, s99, v115, v113
v_bitop3_b32 v112, v112, v104, s98 bitop3:0x8d
v_bitop3_b32 v113, v113, v105, s98 bitop3:0x8d
v_alignbit_b32 v113, v113, v113, 28
v_or_b32 v114, v112, v113
s_getpc_b64 s[92:93]
s_cbranch_vccnz Lexc
v_mov_b32 v252, v114
v_mov_b32 v253, v106
v_mov_b32 v254, v107
s_movk_i32 s94, 1600
s_nop 0
v_perm_b32 v104, v217, v216, s96
v_perm_b32 v105, v219, v218, s96
v_perm_b32 v106, v217, v216, s97
v_perm_b32 v107, v219, v218, s97
v_pk_mad_u16 v108, v216, 2, -1 op_sel_hi:[1,0,0]
v_pk_mad_u16 v109, v217, 2, -1 op_sel_hi:[1,0,0]
v_pk_mad_u16 v110, v218, 2, -1 op_sel_hi:[1,0,0]
v_pk_mad_u16 v111, v219, 2, -1 op_sel_hi:[1,0,0]
v_and_b32 v112, 0x7f7f7f7f, v104
v_and_b32 v113, 0x7f7f7f7f, v105
v_sub_u32 v112, s80, v112
v_sub_u32 v113, s80, v113
v_pk_min_u16 v108, v108, v109
v_pk_min_u16 v110, v110, v111
v_pk_min_u16 v108, v108, v110
v_bitop3_b32 v109, v112, v113, s98 bitop3:0xa8
v_pk_sub_u16 v108, s81, v108 clamp
v_cmp_ne_u32_e64 vcc, v108, v109
v_perm_b32 v112, s99, v115, v112
v_perm_b32 v113, s99, v115, v113
v_bitop3_b32 v112, v112, v104, s98 bitop3:0x8d
v_bitop3_b32 v113, v113, v105, s98 bitop3:0x8d
v_alignbit_b32 v113, v113, v113, 28
v_or_b32 v114, v112, v113
s_getpc_b64 s[92:93]
s_cbranch_vccnz Lexc
v_mov_b32 v255, v114
ds_write_b128 %[lo], v[252:255] offset:18432
v_mov_b32 v248, v106
v_mov_b32 v249, v107
s_waitcnt vmcnt(23)
buffer_load_dwordx2 v[48:49], %[oo], %[ro], 0 offen offset:3072
s_waitcnt vmcnt(23)
buffer_load_dwordx4 a[136:139], %[ko], %[rk], %[so] offen offset:3072 nt
s_movk_i32 s94, 1664
s_nop 0
v_perm_b32 v104, v221, v220, s96
v_perm_b32 v105, v223, v222, s96
v_perm_b32 v106, v221, v220, s97
v_perm_b32 v107, v223, v222, s97
v_pk_mad_u16 v108, v220, 2, -1 op_sel_hi:[1,0,0]
v_pk_mad_u16 v109, v221, 2, -1 op_sel_hi:[1,0,0]
v_pk_mad_u16 v110, v222, 2, -1 op_sel_hi:[1,0,0]
v_pk_mad_u16 v111, v223, 2, -1 op_sel_hi:[1,0,0]
v_and_b32 v112, 0x7f7f7f7f, v104
v_and_b32 v113, 0x7f7f7f7f, v105
v_sub_u32 v112, s80, v112
v_sub_u32 v113, s80, v113
v_pk_min_u16 v108, v108, v109
v_pk_min_u16 v110, v110, v111
v_pk_min_u16 v108, v108, v110
v_bitop3_b32 v109, v112, v113, s98 bitop3:0xa8
v_pk_sub_u16 v108, s81, v108 clamp
v_cmp_ne_u32_e64 vcc, v108, v109
v_perm_b32 v112, s99, v115, v112
v_perm_b32 v113, s99, v115, v113
v_bitop3_b32 v112, v112, v104, s98 bitop3:0x8d
v_bitop3_b32 v113, v113, v105, s98 bitop3:0x8d
v_alignbit_b32 v113, v113, v113, 28
v_or_b32 v114, v112, v113
s_getpc_b64 s[92:93]
s_cbranch_vccnz Lexc
v_mov_b32 v250, v114
v_mov_b32_e64 v251, v106
ds_write_b128 %[lo], v[248:251] offset:19456
v_mov_b32 v252, v107
s_waitcnt vmcnt(23)
s_branch LP16_%=b
LP16_%=:
s_sleep 1
LP16_%=b:
s_memrealtime vcc
s_waitcnt lgkmcnt(0)
s_sub_u32 %[so], vcc_lo, %[t0]
s_cmp_lt_u32 %[so], 1026
s_cbranch_scc1 LP16_%=
s_mov_b32 %[so], 69632
buffer_load_dwordx4 a[140:143], %[ko], %[rk], %[so] offen offset:0 nt
s_movk_i32 s94, 1728
v_perm_b32 v104, v225, v224, s96
v_perm_b32 v105, v227, v226, s96
v_perm_b32 v106, v225, v224, s97
v_perm_b32 v107, v227, v226, s97
v_pk_mad_u16 v108, v224, 2, -1 op_sel_hi:[1,0,0]
v_pk_mad_u16 v109, v225, 2, -1 op_sel_hi:[1,0,0]
v_pk_mad_u16 v110, v226, 2, -1 op_sel_hi:[1,0,0]
v_pk_mad_u16 v111, v227, 2, -1 op_sel_hi:[1,0,0]
v_and_b32 v112, 0x7f7f7f7f, v104
v_and_b32 v113, 0x7f7f7f7f, v105
v_sub_u32 v112, s80, v112
v_sub_u32 v113, s80, v113
v_pk_min_u16 v108, v108, v109
v_pk_min_u16 v110, v110, v111
v_pk_min_u16 v108, v108, v110
v_bitop3_b32 v109, v112, v113, s98 bitop3:0xa8
v_pk_sub_u16 v108, s81, v108 clamp
v_cmp_ne_u32_e64 vcc, v108, v109
v_perm_b32 v112, s99, v115, v112
v_perm_b32 v113, s99, v115, v113
v_bitop3_b32 v112, v112, v104, s98 bitop3:0x8d
v_bitop3_b32 v113, v113, v105, s98 bitop3:0x8d
v_alignbit_b32 v113, v113, v113, 28
v_or_b32 v114, v112, v113
s_getpc_b64 s[92:93]
s_cbranch_vccnz Lexc
v_mov_b32 v253, v114
v_mov_b32 v254, v106
v_mov_b32 v255, v107
ds_write_b128 %[lo], v[252:255] offset:20480
s_waitcnt vmcnt(23)
buffer_load_dwordx4 a[144:147], %[ko], %[rk], %[so] offen offset:1024 nt
s_movk_i32 s94, 1792
v_perm_b32 v104, v229, v228, s96
v_perm_b32 v105, v231, v230, s96
v_perm_b32 v106, v229, v228, s97
v_perm_b32 v107, v231, v230, s97
v_pk_mad_u16 v108, v228, 2, -1 op_sel_hi:[1,0,0]
v_pk_mad_u16 v109, v229, 2, -1 op_sel_hi:[1,0,0]
v_pk_mad_u16 v110, v230, 2, -1 op_sel_hi:[1,0,0]
v_pk_mad_u16 v111, v231, 2, -1 op_sel_hi:[1,0,0]
)" R"(
v_and_b32 v112, 0x7f7f7f7f, v104
v_and_b32 v113, 0x7f7f7f7f, v105
v_sub_u32 v112, s80, v112
v_sub_u32 v113, s80, v113
v_pk_min_u16 v108, v108, v109
v_pk_min_u16 v110, v110, v111
v_pk_min_u16 v108, v108, v110
v_bitop3_b32 v109, v112, v113, s98 bitop3:0xa8
v_pk_sub_u16 v108, s81, v108 clamp
v_cmp_ne_u32_e64 vcc, v108, v109
v_perm_b32 v112, s99, v115, v112
v_perm_b32 v113, s99, v115, v113
v_bitop3_b32 v112, v112, v104, s98 bitop3:0x8d
v_bitop3_b32 v113, v113, v105, s98 bitop3:0x8d
v_alignbit_b32 v113, v113, v113, 28
v_or_b32 v114, v112, v113
s_getpc_b64 s[92:93]
s_cbranch_vccnz Lexc
v_mov_b32 v248, v114
v_mov_b32 v249, v106
v_mov_b32 v250, v107
s_waitcnt vmcnt(23)
s_mov_b32 %[so], 8192
buffer_load_dwordx2 v[50:51], %[oo], %[ro], %[so] offen offset:3072
s_waitcnt vmcnt(23)
s_mov_b32 %[so], 69632
buffer_load_dwordx4 a[148:151], %[ko], %[rk], %[so] offen offset:2048 nt
s_movk_i32 s94, 1856
s_nop 0
v_perm_b32 v104, v233, v232, s96
v_perm_b32 v105, v235, v234, s96
v_perm_b32 v106, v233, v232, s97
v_perm_b32 v107, v235, v234, s97
v_pk_mad_u16 v108, v232, 2, -1 op_sel_hi:[1,0,0]
v_pk_mad_u16 v109, v233, 2, -1 op_sel_hi:[1,0,0]
v_pk_mad_u16 v110, v234, 2, -1 op_sel_hi:[1,0,0]
v_pk_mad_u16 v111, v235, 2, -1 op_sel_hi:[1,0,0]
v_and_b32 v112, 0x7f7f7f7f, v104
v_and_b32 v113, 0x7f7f7f7f, v105
v_sub_u32 v112, s80, v112
v_sub_u32 v113, s80, v113
v_pk_min_u16 v108, v108, v109
v_pk_min_u16 v110, v110, v111
v_pk_min_u16 v108, v108, v110
v_bitop3_b32 v109, v112, v113, s98 bitop3:0xa8
v_pk_sub_u16 v108, s81, v108 clamp
v_cmp_ne_u32_e64 vcc, v108, v109
v_perm_b32 v112, s99, v115, v112
v_perm_b32 v113, s99, v115, v113
v_bitop3_b32 v112, v112, v104, s98 bitop3:0x8d
v_bitop3_b32 v113, v113, v105, s98 bitop3:0x8d
v_alignbit_b32 v113, v113, v113, 28
v_or_b32 v114, v112, v113
s_getpc_b64 s[92:93]
s_cbranch_vccnz Lexc
v_mov_b32 v251, v114
ds_write_b128 %[lo], v[248:251] offset:21504
v_mov_b32 v252, v106
v_mov_b32 v253, v107
s_movk_i32 s94, 1920
s_nop 0
v_perm_b32 v104, v237, v236, s96
v_perm_b32 v105, v239, v238, s96
v_perm_b32 v106, v237, v236, s97
v_perm_b32 v107, v239, v238, s97
v_pk_mad_u16 v108, v236, 2, -1 op_sel_hi:[1,0,0]
v_pk_mad_u16 v109, v237, 2, -1 op_sel_hi:[1,0,0]
v_pk_mad_u16 v110, v238, 2, -1 op_sel_hi:[1,0,0]
v_pk_mad_u16 v111, v239, 2, -1 op_sel_hi:[1,0,0]
v_and_b32 v112, 0x7f7f7f7f, v104
v_and_b32 v113, 0x7f7f7f7f, v105
v_sub_u32 v112, s80, v112
v_sub_u32 v113, s80, v113
v_pk_min_u16 v108, v108, v109
v_pk_min_u16 v110, v110, v111
v_pk_min_u16 v108, v108, v110
v_bitop3_b32 v109, v112, v113, s98 bitop3:0xa8
v_pk_sub_u16 v108, s81, v108 clamp
v_cmp_ne_u32_e64 vcc, v108, v109
v_perm_b32 v112, s99, v115, v112
v_perm_b32 v113, s99, v115, v113
v_bitop3_b32 v112, v112, v104, s98 bitop3:0x8d
v_bitop3_b32 v113, v113, v105, s98 bitop3:0x8d
v_alignbit_b32 v113, v113, v113, 28
v_or_b32 v114, v112, v113
s_getpc_b64 s[92:93]
s_cbranch_vccnz Lexc
v_mov_b32 v254, v114
v_mov_b32_e64 v255, v106
ds_write_b128 %[lo], v[252:255] offset:22528
v_mov_b32 v248, v107
s_waitcnt vmcnt(23)
buffer_load_dword a152, %[tu], %[rg], 0 offen
s_waitcnt vmcnt(23)
buffer_load_dwordx4 a[152:155], %[ko], %[rk], %[so] offen offset:3072 nt
s_movk_i32 s94, 1984
v_perm_b32 v104, v241, v240, s96
v_perm_b32 v105, v243, v242, s96
v_perm_b32 v106, v241, v240, s97
v_perm_b32 v107, v243, v242, s97
v_pk_mad_u16 v108, v240, 2, -1 op_sel_hi:[1,0,0]
v_pk_mad_u16 v109, v241, 2, -1 op_sel_hi:[1,0,0]
v_pk_mad_u16 v110, v242, 2, -1 op_sel_hi:[1,0,0]
v_pk_mad_u16 v111, v243, 2, -1 op_sel_hi:[1,0,0]
v_and_b32 v112, 0x7f7f7f7f, v104
v_and_b32 v113, 0x7f7f7f7f, v105
v_sub_u32 v112, s80, v112
v_sub_u32 v113, s80, v113
v_pk_min_u16 v108, v108, v109
v_pk_min_u16 v110, v110, v111
v_pk_min_u16 v108, v108, v110
v_bitop3_b32 v109, v112, v113, s98 bitop3:0xa8
v_pk_sub_u16 v108, s81, v108 clamp
v_cmp_ne_u32_e64 vcc, v108, v109
v_perm_b32 v112, s99, v115, v112
v_perm_b32 v113, s99, v115, v113
v_bitop3_b32 v112, v112, v104, s98 bitop3:0x8d
v_bitop3_b32 v113, v113, v105, s98 bitop3:0x8d
v_alignbit_b32 v113, v113, v113, 28
v_or_b32 v114, v112, v113
s_getpc_b64 s[92:93]
s_cbranch_vccnz Lexc
v_mov_b32 v249, v114
v_mov_b32 v250, v106
v_mov_b32 v251, v107
ds_write_b128 %[lo], v[248:251] offset:23552
s_waitcnt vmcnt(23)
s_branch LP17_%=b
LP17_%=:
s_sleep 1
LP17_%=b:
s_memrealtime vcc
s_waitcnt lgkmcnt(0)
s_sub_u32 %[so], vcc_lo, %[t0]
s_cmp_lt_u32 %[so], 1112
s_cbranch_scc1 LP17_%=
s_mov_b32 %[so], 73728
buffer_load_dwordx4 a[156:159], %[ko], %[rk], %[so] offen offset:0 nt
s_movk_i32 s94, 2048
s_nop 0
v_perm_b32 v104, v245, v244, s96
v_perm_b32 v105, v247, v246, s96
v_perm_b32 v106, v245, v244, s97
v_perm_b32 v107, v247, v246, s97
v_pk_mad_u16 v108, v244, 2, -1 op_sel_hi:[1,0,0]
v_pk_mad_u16 v109, v245, 2, -1 op_sel_hi:[1,0,0]
v_pk_mad_u16 v110, v246, 2, -1 op_sel_hi:[1,0,0]
v_pk_mad_u16 v111, v247, 2, -1 op_sel_hi:[1,0,0]
v_and_b32 v112, 0x7f7f7f7f, v104
v_and_b32 v113, 0x7f7f7f7f, v105
v_sub_u32 v112, s80, v112
v_sub_u32 v113, s80, v113
v_pk_min_u16 v108, v108, v109
v_pk_min_u16 v110, v110, v111
v_pk_min_u16 v108, v108, v110
v_bitop3_b32 v109, v112, v113, s98 bitop3:0xa8
v_pk_sub_u16 v108, s81, v108 clamp
v_cmp_ne_u32_e64 vcc, v108, v109
v_perm_b32 v112, s99, v115, v112
v_perm_b32 v113, s99, v115, v113
v_bitop3_b32 v112, v112, v104, s98 bitop3:0x8d
v_bitop3_b32 v113, v113, v105, s98 bitop3:0x8d
v_alignbit_b32 v113, v113, v113, 28
v_or_b32 v114, v112, v113
s_getpc_b64 s[92:93]
s_cbranch_vccnz Lexc
v_mov_b32 v252, v114
v_mov_b32 v253, v106
v_mov_b32 v254, v107
s_waitcnt vmcnt(23)
buffer_load_dwordx4 a[160:163], %[ko], %[rk], %[so] offen offset:1024 nt
s_movk_i32 s94, 2112
v_accvgpr_read_b32 v108, a0
v_accvgpr_read_b32 v109, a1
v_accvgpr_read_b32 v110, a2
v_accvgpr_read_b32 v111, a3
v_perm_b32 v104, v109, v108, s96
v_perm_b32 v105, v111, v110, s96
v_perm_b32 v106, v109, v108, s97
v_perm_b32 v107, v111, v110, s97
v_pk_mad_u16 v108, v108, 2, -1 op_sel_hi:[1,0,0]
v_pk_mad_u16 v109, v109, 2, -1 op_sel_hi:[1,0,0]
v_pk_mad_u16 v110, v110, 2, -1 op_sel_hi:[1,0,0]
v_pk_mad_u16 v111, v111, 2, -1 op_sel_hi:[1,0,0]
v_and_b32 v112, 0x7f7f7f7f, v104
v_and_b32 v113, 0x7f7f7f7f, v105
v_sub_u32 v112, s80, v112
v_sub_u32 v113, s80, v113
v_pk_min_u16 v108, v108, v109
v_pk_min_u16 v110, v110, v111
v_pk_min_u16 v108, v108, v110
v_bitop3_b32 v109, v112, v113, s98 bitop3:0xa8
v_pk_sub_u16 v108, s81, v108 clamp
v_cmp_ne_u32_e64 vcc, v108, v109
v_perm_b32 v112, s99, v115, v112
v_perm_b32 v113, s99, v115, v113
v_bitop3_b32 v112, v112, v104, s98 bitop3:0x8d
v_bitop3_b32 v113, v113, v105, s98 bitop3:0x8d
v_alignbit_b32 v113, v113, v113, 28
v_or_b32 v114, v112, v113
s_getpc_b64 s[92:93]
s_cbranch_vccnz Lexc
v_mov_b32 v255, v114
ds_write_b128 %[lo], v[252:255] offset:24576
v_mov_b32 v248, v106
v_mov_b32 v249, v107
s_movk_i32 s94, 2176
s_nop 0
v_accvgpr_read_b32 v108, a4
v_accvgpr_read_b32 v109, a5
v_accvgpr_read_b32 v110, a6
v_accvgpr_read_b32 v111, a7
v_perm_b32 v104, v109, v108, s96
v_perm_b32 v105, v111, v110, s96
v_perm_b32 v106, v109, v108, s97
v_perm_b32 v107, v111, v110, s97
v_pk_mad_u16 v108, v108, 2, -1 op_sel_hi:[1,0,0]
v_pk_mad_u16 v109, v109, 2, -1 op_sel_hi:[1,0,0]
v_pk_mad_u16 v110, v110, 2, -1 op_sel_hi:[1,0,0]
v_pk_mad_u16 v111, v111, 2, -1 op_sel_hi:[1,0,0]
v_and_b32 v112, 0x7f7f7f7f, v104
v_and_b32 v113, 0x7f7f7f7f, v105
v_sub_u32 v112, s80, v112
v_sub_u32 v113, s80, v113
v_pk_min_u16 v108, v108, v109
v_pk_min_u16 v110, v110, v111
v_pk_min_u16 v108, v108, v110
v_bitop3_b32 v109, v112, v113, s98 bitop3:0xa8
v_pk_sub_u16 v108, s81, v108 clamp
v_cmp_ne_u32_e64 vcc, v108, v109
v_perm_b32 v112, s99, v115, v112
v_perm_b32 v113, s99, v115, v113
v_bitop3_b32 v112, v112, v104, s98 bitop3:0x8d
v_bitop3_b32 v113, v113, v105, s98 bitop3:0x8d
v_alignbit_b32 v113, v113, v113, 28
v_or_b32 v114, v112, v113
s_getpc_b64 s[92:93]
s_cbranch_vccnz Lexc
v_mov_b32 v250, v114
v_mov_b32_e64 v251, v106
ds_write_b128 %[lo], v[248:251] offset:25600
v_mov_b32 v252, v107
s_waitcnt vmcnt(23)
buffer_load_dwordx4 a[164:167], %[ko], %[rk], %[so] offen offset:2048 nt
s_movk_i32 s94, 2240
s_nop 0
v_accvgpr_read_b32 v108, a8
v_accvgpr_read_b32 v109, a9
v_accvgpr_read_b32 v110, a10
v_accvgpr_read_b32 v111, a11
v_perm_b32 v104, v109, v108, s96
v_perm_b32 v105, v111, v110, s96
v_perm_b32 v106, v109, v108, s97
v_perm_b32 v107, v111, v110, s97
v_pk_mad_u16 v108, v108, 2, -1 op_sel_hi:[1,0,0]
v_pk_mad_u16 v109, v109, 2, -1 op_sel_hi:[1,0,0]
v_pk_mad_u16 v110, v110, 2, -1 op_sel_hi:[1,0,0]
v_pk_mad_u16 v111, v111, 2, -1 op_sel_hi:[1,0,0]
v_and_b32 v112, 0x7f7f7f7f, v104
v_and_b32 v113, 0x7f7f7f7f, v105
v_sub_u32 v112, s80, v112
v_sub_u32 v113, s80, v113
v_pk_min_u16 v108, v108, v109
v_pk_min_u16 v110, v110, v111
v_pk_min_u16 v108, v108, v110
v_bitop3_b32 v109, v112, v113, s98 bitop3:0xa8
v_pk_sub_u16 v108, s81, v108 clamp
v_cmp_ne_u32_e64 vcc, v108, v109
v_perm_b32 v112, s99, v115, v112
v_perm_b32 v113, s99, v115, v113
v_bitop3_b32 v112, v112, v104, s98 bitop3:0x8d
v_bitop3_b32 v113, v113, v105, s98 bitop3:0x8d
v_alignbit_b32 v113, v113, v113, 28
v_or_b32 v114, v112, v113
s_getpc_b64 s[92:93]
s_cbranch_vccnz Lexc
v_mov_b32 v253, v114
v_mov_b32 v254, v106
v_mov_b32 v255, v107
ds_write_b128 %[lo], v[252:255] offset:26624
s_waitcnt vmcnt(23)
buffer_load_dwordx4 a[168:171], %[ko], %[rk], %[so] offen offset:3072 nt
s_movk_i32 s94, 2304
v_accvgpr_read_b32 v108, a12
v_accvgpr_read_b32 v109, a13
v_accvgpr_read_b32 v110, a14
v_accvgpr_read_b32 v111, a15
v_perm_b32 v104, v109, v108, s96
v_perm_b32 v105, v111, v110, s96
v_perm_b32 v106, v109, v108, s97
v_perm_b32 v107, v111, v110, s97
v_pk_mad_u16 v108, v108, 2, -1 op_sel_hi:[1,0,0]
v_pk_mad_u16 v109, v109, 2, -1 op_sel_hi:[1,0,0]
v_pk_mad_u16 v110, v110, 2, -1 op_sel_hi:[1,0,0]
v_pk_mad_u16 v111, v111, 2, -1 op_sel_hi:[1,0,0]
v_and_b32 v112, 0x7f7f7f7f, v104
v_and_b32 v113, 0x7f7f7f7f, v105
v_sub_u32 v112, s80, v112
v_sub_u32 v113, s80, v113
v_pk_min_u16 v108, v108, v109
v_pk_min_u16 v110, v110, v111
v_pk_min_u16 v108, v108, v110
v_bitop3_b32 v109, v112, v113, s98 bitop3:0xa8
v_pk_sub_u16 v108, s81, v108 clamp
v_cmp_ne_u32_e64 vcc, v108, v109
v_perm_b32 v112, s99, v115, v112
v_perm_b32 v113, s99, v115, v113
v_bitop3_b32 v112, v112, v104, s98 bitop3:0x8d
v_bitop3_b32 v113, v113, v105, s98 bitop3:0x8d
v_alignbit_b32 v113, v113, v113, 28
v_or_b32 v114, v112, v113
s_getpc_b64 s[92:93]
s_cbranch_vccnz Lexc
v_mov_b32 v248, v114
v_mov_b32 v249, v106
v_mov_b32 v250, v107
buffer_load_dwordx2 %[tk], %[tko], %[rw], 0 offen sc1
buffer_load_dwordx4 %[qa], %[qp], %[rw], %[qs] offen sc1
buffer_load_dwordx4 %[qb], %[qp], %[rw], %[qs] offen offset:16 sc1
s_waitcnt vmcnt(23)
s_branch LP18_%=b
LP18_%=:
s_sleep 1
LP18_%=b:
s_memrealtime vcc
s_waitcnt lgkmcnt(0)
s_sub_u32 %[so], vcc_lo, %[t0]
s_cmp_lt_u32 %[so], 1174
s_cbranch_scc1 LP18_%=
s_mov_b32 %[so], 77824
buffer_load_dwordx4 a[172:175], %[ko], %[rk], %[so] offen offset:0 nt
s_movk_i32 s94, 2368
s_nop 0
v_accvgpr_read_b32 v108, a16
v_accvgpr_read_b32 v109, a17
v_accvgpr_read_b32 v110, a18
v_accvgpr_read_b32 v111, a19
v_perm_b32 v104, v109, v108, s96
v_perm_b32 v105, v111, v110, s96
v_perm_b32 v106, v109, v108, s97
v_perm_b32 v107, v111, v110, s97
v_pk_mad_u16 v108, v108, 2, -1 op_sel_hi:[1,0,0]
v_pk_mad_u16 v109, v109, 2, -1 op_sel_hi:[1,0,0]
v_pk_mad_u16 v110, v110, 2, -1 op_sel_hi:[1,0,0]
v_pk_mad_u16 v111, v111, 2, -1 op_sel_hi:[1,0,0]
v_and_b32 v112, 0x7f7f7f7f, v104
v_and_b32 v113, 0x7f7f7f7f, v105
v_sub_u32 v112, s80, v112
v_sub_u32 v113, s80, v113
v_pk_min_u16 v108, v108, v109
v_pk_min_u16 v110, v110, v111
v_pk_min_u16 v108, v108, v110
v_bitop3_b32 v109, v112, v113, s98 bitop3:0xa8
v_pk_sub_u16 v108, s81, v108 clamp
v_cmp_ne_u32_e64 vcc, v108, v109
v_perm_b32 v112, s99, v115, v112
v_perm_b32 v113, s99, v115, v113
v_bitop3_b32 v112, v112, v104, s98 bitop3:0x8d
v_bitop3_b32 v113, v113, v105, s98 bitop3:0x8d
v_alignbit_b32 v113, v113, v113, 28
v_or_b32 v114, v112, v113
s_getpc_b64 s[92:93]
s_cbranch_vccnz Lexc
v_mov_b32 v251, v114
ds_write_b128 %[lo], v[248:251] offset:27648
v_mov_b32 v252, v106
v_mov_b32 v253, v107
s_movk_i32 s94, 2432
s_nop 0
)" R"(
v_accvgpr_read_b32 v108, a20
v_accvgpr_read_b32 v109, a21
v_accvgpr_read_b32 v110, a22
v_accvgpr_read_b32 v111, a23
v_perm_b32 v104, v109, v108, s96
v_perm_b32 v105, v111, v110, s96
v_perm_b32 v106, v109, v108, s97
v_perm_b32 v107, v111, v110, s97
v_pk_mad_u16 v108, v108, 2, -1 op_sel_hi:[1,0,0]
v_pk_mad_u16 v109, v109, 2, -1 op_sel_hi:[1,0,0]
v_pk_mad_u16 v110, v110, 2, -1 op_sel_hi:[1,0,0]
v_pk_mad_u16 v111, v111, 2, -1 op_sel_hi:[1,0,0]
v_and_b32 v112, 0x7f7f7f7f, v104
v_and_b32 v113, 0x7f7f7f7f, v105
v_sub_u32 v112, s80, v112
v_sub_u32 v113, s80, v113
v_pk_min_u16 v108, v108, v109
v_pk_min_u16 v110, v110, v111
v_pk_min_u16 v108, v108, v110
v_bitop3_b32 v109, v112, v113, s98 bitop3:0xa8
v_pk_sub_u16 v108, s81, v108 clamp
v_cmp_ne_u32_e64 vcc, v108, v109
v_perm_b32 v112, s99, v115, v112
v_perm_b32 v113, s99, v115, v113
v_bitop3_b32 v112, v112, v104, s98 bitop3:0x8d
v_bitop3_b32 v113, v113, v105, s98 bitop3:0x8d
v_alignbit_b32 v113, v113, v113, 28
v_or_b32 v114, v112, v113
s_getpc_b64 s[92:93]
s_cbranch_vccnz Lexc
v_mov_b32 v254, v114
v_mov_b32_e64 v255, v106
ds_write_b128 %[lo], v[252:255] offset:28672
v_mov_b32 v248, v107
s_waitcnt vmcnt(23)
buffer_load_dwordx4 a[176:179], %[ko], %[rk], %[so] offen offset:1024 nt
s_movk_i32 s94, 2496
s_nop 0
v_accvgpr_read_b32 v108, a24
v_accvgpr_read_b32 v109, a25
v_accvgpr_read_b32 v110, a26
v_accvgpr_read_b32 v111, a27
v_perm_b32 v104, v109, v108, s96
v_perm_b32 v105, v111, v110, s96
v_perm_b32 v106, v109, v108, s97
v_perm_b32 v107, v111, v110, s97
v_pk_mad_u16 v108, v108, 2, -1 op_sel_hi:[1,0,0]
v_pk_mad_u16 v109, v109, 2, -1 op_sel_hi:[1,0,0]
v_pk_mad_u16 v110, v110, 2, -1 op_sel_hi:[1,0,0]
v_pk_mad_u16 v111, v111, 2, -1 op_sel_hi:[1,0,0]
v_and_b32 v112, 0x7f7f7f7f, v104
v_and_b32 v113, 0x7f7f7f7f, v105
v_sub_u32 v112, s80, v112
v_sub_u32 v113, s80, v113
v_pk_min_u16 v108, v108, v109
v_pk_min_u16 v110, v110, v111
v_pk_min_u16 v108, v108, v110
v_bitop3_b32 v109, v112, v113, s98 bitop3:0xa8
v_pk_sub_u16 v108, s81, v108 clamp
v_cmp_ne_u32_e64 vcc, v108, v109
v_perm_b32 v112, s99, v115, v112
v_perm_b32 v113, s99, v115, v113
v_bitop3_b32 v112, v112, v104, s98 bitop3:0x8d
v_bitop3_b32 v113, v113, v105, s98 bitop3:0x8d
v_alignbit_b32 v113, v113, v113, 28
v_or_b32 v114, v112, v113
s_getpc_b64 s[92:93]
s_cbranch_vccnz Lexc
v_mov_b32 v249, v114
v_mov_b32 v250, v106
v_mov_b32 v251, v107
ds_write_b128 %[lo], v[248:251] offset:29696
s_waitcnt vmcnt(23)
buffer_load_dwordx4 a[180:183], %[ko], %[rk], %[so] offen offset:2048 nt
s_movk_i32 s94, 2560
v_accvgpr_read_b32 v108, a28
v_accvgpr_read_b32 v109, a29
v_accvgpr_read_b32 v110, a30
v_accvgpr_read_b32 v111, a31
v_perm_b32 v104, v109, v108, s96
v_perm_b32 v105, v111, v110, s96
v_perm_b32 v106, v109, v108, s97
v_perm_b32 v107, v111, v110, s97
v_pk_mad_u16 v108, v108, 2, -1 op_sel_hi:[1,0,0]
v_pk_mad_u16 v109, v109, 2, -1 op_sel_hi:[1,0,0]
v_pk_mad_u16 v110, v110, 2, -1 op_sel_hi:[1,0,0]
v_pk_mad_u16 v111, v111, 2, -1 op_sel_hi:[1,0,0]
v_and_b32 v112, 0x7f7f7f7f, v104
v_and_b32 v113, 0x7f7f7f7f, v105
v_sub_u32 v112, s80, v112
v_sub_u32 v113, s80, v113
v_pk_min_u16 v108, v108, v109
v_pk_min_u16 v110, v110, v111
v_pk_min_u16 v108, v108, v110
v_bitop3_b32 v109, v112, v113, s98 bitop3:0xa8
v_pk_sub_u16 v108, s81, v108 clamp
v_cmp_ne_u32_e64 vcc, v108, v109
v_perm_b32 v112, s99, v115, v112
v_perm_b32 v113, s99, v115, v113
v_bitop3_b32 v112, v112, v104, s98 bitop3:0x8d
v_bitop3_b32 v113, v113, v105, s98 bitop3:0x8d
v_alignbit_b32 v113, v113, v113, 28
v_or_b32 v114, v112, v113
s_getpc_b64 s[92:93]
s_cbranch_vccnz Lexc
v_mov_b32 v252, v114
v_mov_b32 v253, v106
v_mov_b32 v254, v107
s_waitcnt vmcnt(23)
buffer_load_dwordx4 a[184:187], %[ko], %[rk], %[so] offen offset:3072 nt
s_movk_i32 s94, 2624
v_accvgpr_read_b32 v108, a32
v_accvgpr_read_b32 v109, a33
v_accvgpr_read_b32 v110, a34
v_accvgpr_read_b32 v111, a35
v_perm_b32 v104, v109, v108, s96
v_perm_b32 v105, v111, v110, s96
v_perm_b32 v106, v109, v108, s97
v_perm_b32 v107, v111, v110, s97
v_pk_mad_u16 v108, v108, 2, -1 op_sel_hi:[1,0,0]
v_pk_mad_u16 v109, v109, 2, -1 op_sel_hi:[1,0,0]
v_pk_mad_u16 v110, v110, 2, -1 op_sel_hi:[1,0,0]
v_pk_mad_u16 v111, v111, 2, -1 op_sel_hi:[1,0,0]
v_and_b32 v112, 0x7f7f7f7f, v104
v_and_b32 v113, 0x7f7f7f7f, v105
v_sub_u32 v112, s80, v112
v_sub_u32 v113, s80, v113
v_pk_min_u16 v108, v108, v109
v_pk_min_u16 v110, v110, v111
v_pk_min_u16 v108, v108, v110
v_bitop3_b32 v109, v112, v113, s98 bitop3:0xa8
v_pk_sub_u16 v108, s81, v108 clamp
v_cmp_ne_u32_e64 vcc, v108, v109
v_perm_b32 v112, s99, v115, v112
v_perm_b32 v113, s99, v115, v113
v_bitop3_b32 v112, v112, v104, s98 bitop3:0x8d
v_bitop3_b32 v113, v113, v105, s98 bitop3:0x8d
v_alignbit_b32 v113, v113, v113, 28
v_or_b32 v114, v112, v113
s_getpc_b64 s[92:93]
s_cbranch_vccnz Lexc
v_mov_b32 v255, v114
ds_write_b128 %[lo], v[252:255] offset:30720
v_mov_b32 v248, v106
v_mov_b32 v249, v107
s_waitcnt vmcnt(23)
s_branch LP19_%=b
LP19_%=:
s_sleep 1
LP19_%=b:
s_memrealtime vcc
s_waitcnt lgkmcnt(0)
s_sub_u32 %[so], vcc_lo, %[t0]
s_cmp_lt_u32 %[so], 1237
s_cbranch_scc1 LP19_%=
s_mov_b32 %[so], 81920
buffer_load_dwordx4 a[188:191], %[ko], %[rk], %[so] offen offset:0 nt
s_movk_i32 s94, 2688
s_nop 0
v_accvgpr_read_b32 v108, a36
v_accvgpr_read_b32 v109, a37
v_accvgpr_read_b32 v110, a38
v_accvgpr_read_b32 v111, a39
v_perm_b32 v104, v109, v108, s96
v_perm_b32 v105, v111, v110, s96
v_perm_b32 v106, v109, v108, s97
v_perm_b32 v107, v111, v110, s97
v_pk_mad_u16 v108, v108, 2, -1 op_sel_hi:[1,0,0]
v_pk_mad_u16 v109, v109, 2, -1 op_sel_hi:[1,0,0]
v_pk_mad_u16 v110, v110, 2, -1 op_sel_hi:[1,0,0]
v_pk_mad_u16 v111, v111, 2, -1 op_sel_hi:[1,0,0]
v_and_b32 v112, 0x7f7f7f7f, v104
v_and_b32 v113, 0x7f7f7f7f, v105
v_sub_u32 v112, s80, v112
v_sub_u32 v113, s80, v113
v_pk_min_u16 v108, v108, v109
v_pk_min_u16 v110, v110, v111
v_pk_min_u16 v108, v108, v110
v_bitop3_b32 v109, v112, v113, s98 bitop3:0xa8
v_pk_sub_u16 v108, s81, v108 clamp
v_cmp_ne_u32_e64 vcc, v108, v109
v_perm_b32 v112, s99, v115, v112
v_perm_b32 v113, s99, v115, v113
v_bitop3_b32 v112, v112, v104, s98 bitop3:0x8d
v_bitop3_b32 v113, v113, v105, s98 bitop3:0x8d
v_alignbit_b32 v113, v113, v113, 28
v_or_b32 v114, v112, v113
s_getpc_b64 s[92:93]
s_cbranch_vccnz Lexc
v_mov_b32 v250, v114
v_mov_b32_e64 v251, v106
ds_write_b128 %[lo], v[248:251] offset:31744
v_mov_b32 v252, v107
s_movk_i32 s94, 2752
v_accvgpr_read_b32 v108, a40
v_accvgpr_read_b32 v109, a41
v_accvgpr_read_b32 v110, a42
v_accvgpr_read_b32 v111, a43
v_perm_b32 v104, v109, v108, s96
v_perm_b32 v105, v111, v110, s96
v_perm_b32 v106, v109, v108, s97
v_perm_b32 v107, v111, v110, s97
v_pk_mad_u16 v108, v108, 2, -1 op_sel_hi:[1,0,0]
v_pk_mad_u16 v109, v109, 2, -1 op_sel_hi:[1,0,0]
v_pk_mad_u16 v110, v110, 2, -1 op_sel_hi:[1,0,0]
v_pk_mad_u16 v111, v111, 2, -1 op_sel_hi:[1,0,0]
v_and_b32 v112, 0x7f7f7f7f, v104
v_and_b32 v113, 0x7f7f7f7f, v105
v_sub_u32 v112, s80, v112
v_sub_u32 v113, s80, v113
v_pk_min_u16 v108, v108, v109
v_pk_min_u16 v110, v110, v111
v_pk_min_u16 v108, v108, v110
v_bitop3_b32 v109, v112, v113, s98 bitop3:0xa8
v_pk_sub_u16 v108, s81, v108 clamp
v_cmp_ne_u32_e64 vcc, v108, v109
v_perm_b32 v112, s99, v115, v112
v_perm_b32 v113, s99, v115, v113
v_bitop3_b32 v112, v112, v104, s98 bitop3:0x8d
v_bitop3_b32 v113, v113, v105, s98 bitop3:0x8d
v_alignbit_b32 v113, v113, v113, 28
v_or_b32 v114, v112, v113
s_getpc_b64 s[92:93]
s_cbranch_vccnz Lexc
v_mov_b32 v253, v114
v_mov_b32 v254, v106
v_mov_b32 v255, v107
ds_write_b128 %[lo], v[252:255] offset:32768
s_waitcnt vmcnt(23)
buffer_load_dword a192, %[tu], %[ru], 0 offen
s_waitcnt vmcnt(23)
buffer_load_dwordx4 a[192:195], %[ko], %[rk], %[so] offen offset:1024 nt
s_movk_i32 s94, 2816
s_nop 0
v_accvgpr_read_b32 v108, a44
v_accvgpr_read_b32 v109, a45
v_accvgpr_read_b32 v110, a46
v_accvgpr_read_b32 v111, a47
v_perm_b32 v104, v109, v108, s96
v_perm_b32 v105, v111, v110, s96
v_perm_b32 v106, v109, v108, s97
v_perm_b32 v107, v111, v110, s97
v_pk_mad_u16 v108, v108, 2, -1 op_sel_hi:[1,0,0]
v_pk_mad_u16 v109, v109, 2, -1 op_sel_hi:[1,0,0]
v_pk_mad_u16 v110, v110, 2, -1 op_sel_hi:[1,0,0]
v_pk_mad_u16 v111, v111, 2, -1 op_sel_hi:[1,0,0]
v_and_b32 v112, 0x7f7f7f7f, v104
v_and_b32 v113, 0x7f7f7f7f, v105
v_sub_u32 v112, s80, v112
v_sub_u32 v113, s80, v113
v_pk_min_u16 v108, v108, v109
v_pk_min_u16 v110, v110, v111
v_pk_min_u16 v108, v108, v110
v_bitop3_b32 v109, v112, v113, s98 bitop3:0xa8
v_pk_sub_u16 v108, s81, v108 clamp
v_cmp_ne_u32_e64 vcc, v108, v109
v_perm_b32 v112, s99, v115, v112
v_perm_b32 v113, s99, v115, v113
v_bitop3_b32 v112, v112, v104, s98 bitop3:0x8d
v_bitop3_b32 v113, v113, v105, s98 bitop3:0x8d
v_alignbit_b32 v113, v113, v113, 28
v_or_b32 v114, v112, v113
s_getpc_b64 s[92:93]
s_cbranch_vccnz Lexc
v_mov_b32 v248, v114
v_mov_b32 v249, v106
v_mov_b32 v250, v107
s_waitcnt vmcnt(23)
buffer_load_dwordx4 a[196:199], %[ko], %[rk], %[so] offen offset:2048 nt
s_movk_i32 s94, 2880
v_accvgpr_read_b32 v108, a48
v_accvgpr_read_b32 v109, a49
v_accvgpr_read_b32 v110, a50
v_accvgpr_read_b32 v111, a51
v_perm_b32 v104, v109, v108, s96
v_perm_b32 v105, v111, v110, s96
v_perm_b32 v106, v109, v108, s97
v_perm_b32 v107, v111, v110, s97
v_pk_mad_u16 v108, v108, 2, -1 op_sel_hi:[1,0,0]
v_pk_mad_u16 v109, v109, 2, -1 op_sel_hi:[1,0,0]
v_pk_mad_u16 v110, v110, 2, -1 op_sel_hi:[1,0,0]
v_pk_mad_u16 v111, v111, 2, -1 op_sel_hi:[1,0,0]
v_and_b32 v112, 0x7f7f7f7f, v104
v_and_b32 v113, 0x7f7f7f7f, v105
v_sub_u32 v112, s80, v112
v_sub_u32 v113, s80, v113
v_pk_min_u16 v108, v108, v109
v_pk_min_u16 v110, v110, v111
v_pk_min_u16 v108, v108, v110
v_bitop3_b32 v109, v112, v113, s98 bitop3:0xa8
v_pk_sub_u16 v108, s81, v108 clamp
v_cmp_ne_u32_e64 vcc, v108, v109
v_perm_b32 v112, s99, v115, v112
v_perm_b32 v113, s99, v115, v113
v_bitop3_b32 v112, v112, v104, s98 bitop3:0x8d
v_bitop3_b32 v113, v113, v105, s98 bitop3:0x8d
v_alignbit_b32 v113, v113, v113, 28
v_or_b32 v114, v112, v113
s_getpc_b64 s[92:93]
s_cbranch_vccnz Lexc
v_mov_b32 v251, v114
ds_write_b128 %[lo], v[248:251] offset:33792
v_mov_b32 v252, v106
v_mov_b32 v253, v107
s_waitcnt vmcnt(23)
buffer_load_dwordx4 a[200:203], %[ko], %[rk], %[so] offen offset:3072 nt
s_movk_i32 s94, 2944
v_accvgpr_read_b32 v108, a52
v_accvgpr_read_b32 v109, a53
v_accvgpr_read_b32 v110, a54
v_accvgpr_read_b32 v111, a55
v_perm_b32 v104, v109, v108, s96
v_perm_b32 v105, v111, v110, s96
v_perm_b32 v106, v109, v108, s97
v_perm_b32 v107, v111, v110, s97
v_pk_mad_u16 v108, v108, 2, -1 op_sel_hi:[1,0,0]
v_pk_mad_u16 v109, v109, 2, -1 op_sel_hi:[1,0,0]
v_pk_mad_u16 v110, v110, 2, -1 op_sel_hi:[1,0,0]
v_pk_mad_u16 v111, v111, 2, -1 op_sel_hi:[1,0,0]
v_and_b32 v112, 0x7f7f7f7f, v104
v_and_b32 v113, 0x7f7f7f7f, v105
v_sub_u32 v112, s80, v112
v_sub_u32 v113, s80, v113
v_pk_min_u16 v108, v108, v109
v_pk_min_u16 v110, v110, v111
v_pk_min_u16 v108, v108, v110
v_bitop3_b32 v109, v112, v113, s98 bitop3:0xa8
v_pk_sub_u16 v108, s81, v108 clamp
v_cmp_ne_u32_e64 vcc, v108, v109
v_perm_b32 v112, s99, v115, v112
v_perm_b32 v113, s99, v115, v113
v_bitop3_b32 v112, v112, v104, s98 bitop3:0x8d
v_bitop3_b32 v113, v113, v105, s98 bitop3:0x8d
v_alignbit_b32 v113, v113, v113, 28
v_or_b32 v114, v112, v113
s_getpc_b64 s[92:93]
s_cbranch_vccnz Lexc
v_mov_b32 v254, v114
v_mov_b32_e64 v255, v106
ds_write_b128 %[lo], v[252:255] offset:34816
v_mov_b32 v248, v107
s_movk_i32 s94, 3008
v_accvgpr_read_b32 v108, a56
v_accvgpr_read_b32 v109, a57
v_accvgpr_read_b32 v110, a58
v_accvgpr_read_b32 v111, a59
v_perm_b32 v104, v109, v108, s96
v_perm_b32 v105, v111, v110, s96
v_perm_b32 v106, v109, v108, s97
v_perm_b32 v107, v111, v110, s97
v_pk_mad_u16 v108, v108, 2, -1 op_sel_hi:[1,0,0]
v_pk_mad_u16 v109, v109, 2, -1 op_sel_hi:[1,0,0]
v_pk_mad_u16 v110, v110, 2, -1 op_sel_hi:[1,0,0]
v_pk_mad_u16 v111, v111, 2, -1 op_sel_hi:[1,0,0]
v_and_b32 v112, 0x7f7f7f7f, v104
v_and_b32 v113, 0x7f7f7f7f, v105
v_sub_u32 v112, s80, v112
v_sub_u32 v113, s80, v113
v_pk_min_u16 v108, v108, v109
v_pk_min_u16 v110, v110, v111
v_pk_min_u16 v108, v108, v110
v_bitop3_b32 v109, v112, v113, s98 bitop3:0xa8
v_pk_sub_u16 v108, s81, v108 clamp
)" R"(
v_cmp_ne_u32_e64 vcc, v108, v109
v_perm_b32 v112, s99, v115, v112
v_perm_b32 v113, s99, v115, v113
v_bitop3_b32 v112, v112, v104, s98 bitop3:0x8d
v_bitop3_b32 v113, v113, v105, s98 bitop3:0x8d
v_alignbit_b32 v113, v113, v113, 28
v_or_b32 v114, v112, v113
s_getpc_b64 s[92:93]
s_cbranch_vccnz Lexc
v_mov_b32 v249, v114
v_mov_b32 v250, v106
v_mov_b32 v251, v107
ds_write_b128 %[lo], v[248:251] offset:35840
s_waitcnt vmcnt(23)
s_branch LP20_%=b
LP20_%=:
s_sleep 1
LP20_%=b:
s_memrealtime vcc
s_waitcnt lgkmcnt(0)
s_sub_u32 %[so], vcc_lo, %[t0]
s_cmp_lt_u32 %[so], 1322
s_cbranch_scc1 LP20_%=
s_mov_b32 %[so], 86016
buffer_load_dwordx4 a[204:207], %[ko], %[rk], %[so] offen offset:0 nt
s_movk_i32 s94, 3072
s_nop 0
v_accvgpr_read_b32 v108, a60
v_accvgpr_read_b32 v109, a61
v_accvgpr_read_b32 v110, a62
v_accvgpr_read_b32 v111, a63
v_perm_b32 v104, v109, v108, s96
v_perm_b32 v105, v111, v110, s96
v_perm_b32 v106, v109, v108, s97
v_perm_b32 v107, v111, v110, s97
v_pk_mad_u16 v108, v108, 2, -1 op_sel_hi:[1,0,0]
v_pk_mad_u16 v109, v109, 2, -1 op_sel_hi:[1,0,0]
v_pk_mad_u16 v110, v110, 2, -1 op_sel_hi:[1,0,0]
v_pk_mad_u16 v111, v111, 2, -1 op_sel_hi:[1,0,0]
v_and_b32 v112, 0x7f7f7f7f, v104
v_and_b32 v113, 0x7f7f7f7f, v105
v_sub_u32 v112, s80, v112
v_sub_u32 v113, s80, v113
v_pk_min_u16 v108, v108, v109
v_pk_min_u16 v110, v110, v111
v_pk_min_u16 v108, v108, v110
v_bitop3_b32 v109, v112, v113, s98 bitop3:0xa8
v_pk_sub_u16 v108, s81, v108 clamp
v_cmp_ne_u32_e64 vcc, v108, v109
v_perm_b32 v112, s99, v115, v112
v_perm_b32 v113, s99, v115, v113
v_bitop3_b32 v112, v112, v104, s98 bitop3:0x8d
v_bitop3_b32 v113, v113, v105, s98 bitop3:0x8d
v_alignbit_b32 v113, v113, v113, 28
v_or_b32 v114, v112, v113
s_getpc_b64 s[92:93]
s_cbranch_vccnz Lexc
v_mov_b32 v252, v114
v_mov_b32 v253, v106
v_mov_b32 v254, v107
s_waitcnt vmcnt(23)
buffer_load_dwordx4 a[208:211], %[ko], %[rk], %[so] offen offset:1024 nt
s_movk_i32 s94, 3136
v_accvgpr_read_b32 v108, a64
v_accvgpr_read_b32 v109, a65
v_accvgpr_read_b32 v110, a66
v_accvgpr_read_b32 v111, a67
v_perm_b32 v104, v109, v108, s96
v_perm_b32 v105, v111, v110, s96
v_perm_b32 v106, v109, v108, s97
v_perm_b32 v107, v111, v110, s97
v_pk_mad_u16 v108, v108, 2, -1 op_sel_hi:[1,0,0]
v_pk_mad_u16 v109, v109, 2, -1 op_sel_hi:[1,0,0]
v_pk_mad_u16 v110, v110, 2, -1 op_sel_hi:[1,0,0]
v_pk_mad_u16 v111, v111, 2, -1 op_sel_hi:[1,0,0]
v_and_b32 v112, 0x7f7f7f7f, v104
v_and_b32 v113, 0x7f7f7f7f, v105
v_sub_u32 v112, s80, v112
v_sub_u32 v113, s80, v113
v_pk_min_u16 v108, v108, v109
v_pk_min_u16 v110, v110, v111
v_pk_min_u16 v108, v108, v110
v_bitop3_b32 v109, v112, v113, s98 bitop3:0xa8
v_pk_sub_u16 v108, s81, v108 clamp
v_cmp_ne_u32_e64 vcc, v108, v109
v_perm_b32 v112, s99, v115, v112
v_perm_b32 v113, s99, v115, v113
v_bitop3_b32 v112, v112, v104, s98 bitop3:0x8d
v_bitop3_b32 v113, v113, v105, s98 bitop3:0x8d
v_alignbit_b32 v113, v113, v113, 28
v_or_b32 v114, v112, v113
s_getpc_b64 s[92:93]
s_cbranch_vccnz Lexc
v_mov_b32 v255, v114
ds_write_b128 %[lo], v[252:255] offset:36864
v_mov_b32 v248, v106
v_mov_b32 v249, v107
ds_write_b128 %[lo], v[248:251] offset:37888
s_waitcnt vmcnt(11)
s_nop 0
s_mov_b32 s85, 0x05040100
s_mov_b32 s86, 0x07060302
v_perm_b32 v72, v38, v36, s85
v_perm_b32 v76, v38, v36, s86
v_perm_b32 v80, v39, v37, s85
v_perm_b32 v84, v39, v37, s86
v_perm_b32 v73, v42, v40, s85
v_perm_b32 v77, v42, v40, s86
v_perm_b32 v81, v43, v41, s85
v_perm_b32 v85, v43, v41, s86
v_perm_b32 v74, v46, v44, s85
v_perm_b32 v78, v46, v44, s86
v_perm_b32 v82, v47, v45, s85
v_perm_b32 v86, v47, v45, s86
v_perm_b32 v75, v50, v48, s85
v_perm_b32 v79, v50, v48, s86
v_perm_b32 v83, v51, v49, s85
v_perm_b32 v87, v51, v49, s86
)"
      : [qa] "=&v"(qa), [qb] "=&v"(qb), [tk] "=&v"(tk), [so] "=&s"(so), "+{s78}"(rcn), "=&{s80}"(t4k), "=&{s81}"(thk)
      : [rk] "{s[64:67]}"(c.rsK), [rw] "{s[56:59]}"(c.rsW), [t0] "s"(t0), [ko] "v"(k_off(c)), [lo] "v"(l_off(c)), [ro] "s"(mkrs(p.wo, NH * DH * DM * 2)), [rg] "s"(mkrs(p.wg, DM * FF * 2)), [ru] "s"(mkrs(p.wu, DM * FF * 2)), [oo] "v"(wo_off(c)), [tu] "v"(touch_gu(c)), [tko] "v"(WS_TS + (c.lane & 7) * 8), [qp] "v"(qp), [qs] "s"(qs), "{s79}"(r_base(c))
      : "memory", "vcc", "scc", "s84", "s85", "s86", "s87", "s88", "s89", "s90", "s91", "s92", "s93", "s94", "s95", "s96", "s97", "s98", "s99");

    const u32 tag = c.epoch + 1;
    if (!wave_all(qa[1] == tag && qa[3] == tag && qb[1] == tag && qb[3] == tag)) {
#pragma clang loop unroll(disable)
      for (int spin = 0; spin < SPIN_CAP; ++spin) {
        asm volatile("" ::: "memory");
        qa = ld128<SC1>(c.rsW, qp, qs); qb = ld128<SC1>(c.rsW, qp + 16, qs);
        if (qa[1] == tag && qa[3] == tag && qb[1] == tag && qb[3] == tag) break;
        __builtin_amdgcn_s_sleep(4);
      }
    }
    tb = time_base(tk, tag, t0);
    if (5 == 5) {
      int hi = tk[1] - t0 + 800 <= 2 * 800 ? (int)(tk[1] - t0) : 0, lo = hi;
      hi = max(hi, (int)dppu<XOR1>(hi)); hi = max(hi, (int)dppu<XOR2>(hi)); hi = max(hi, (int)dppu<HMIR>(hi));
      lo = min(lo, (int)dppu<XOR1>(lo)); lo = min(lo, (int)dppu<XOR2>(lo)); lo = min(lo, (int)dppu<HMIR>(lo));
      const int late = (int)(t0 - tb);
      const bool big = hi - lo > 155;
      int up = max(late - (big ? (20) : (60)), 0);
      if (2 && big) up = up * (2 == 1 ? 85 : 128) >> 8;
      const u32 tn = big ? (tb + 30) | 1u : (tb + min(max(hi + late - (15), 0), 40)) & ~1u;
      t0 -= __builtin_amdgcn_readfirstlane(up); tb = __builtin_amdgcn_readfirstlane(tn);
      asm volatile("" : "+s"(t0), "+s"(tb));
    }
    q4[0] = qa[0]; q4[1] = qa[2]; q4[2] = qb[0]; q4[3] = qb[2];
  }
  STAMP(1);
  {
    derive(c);
    Attn a;
    asm volatile(
R"(
.p2align 3
s_nop 4
s_nop 0
s_mov_b32 s96, 0x07050301
s_mov_b32 s97, 0x06040200
s_mov_b32 s98, 0x80808080
s_mov_b32 s99, 0xfffefdfc
s_mov_b32 vcc_lo, 0x000f000f
s_mov_b32 vcc_hi, 0x000f000f
v_cndmask_b32 v52, 0, %[q0], vcc
v_cndmask_b32 v53, 0, %[q1], vcc
v_cndmask_b32 v54, 0, %[q2], vcc
v_cndmask_b32 v55, 0, %[q3], vcc
s_nop 1
s_nop 0
v_mfma_f32_16x16x32_bf16 v[36:39], v[116:119], v[52:55], 0
v_mfma_f32_16x16x32_bf16 v[40:43], v[120:123], v[52:55], 0
v_mfma_f32_16x16x32_bf16 v[44:47], v[124:127], v[52:55], 0
v_mfma_f32_16x16x32_bf16 v[48:51], v[128:131], v[52:55], 0
s_mov_b32 vcc_lo, 0x00f000f0
s_mov_b32 vcc_hi, 0x00f000f0
v_cndmask_b32 v52, 0, %[q0], vcc
v_cndmask_b32 v53, 0, %[q1], vcc
v_cndmask_b32 v54, 0, %[q2], vcc
v_cndmask_b32 v55, 0, %[q3], vcc
s_nop 1
s_nop 0
v_mfma_f32_16x16x32_bf16 v[36:39], v[132:135], v[52:55], v[36:39]
v_mfma_f32_16x16x32_bf16 v[40:43], v[136:139], v[52:55], v[40:43]
v_mfma_f32_16x16x32_bf16 v[44:47], v[140:143], v[52:55], v[44:47]
v_mfma_f32_16x16x32_bf16 v[48:51], v[144:147], v[52:55], v[48:51]
s_mov_b32 %[so], 86016
buffer_load_dwordx4 v[252:255], %[ko], %[rk], %[so] offen offset:2048 nt
s_mov_b32 vcc_lo, 0x0f000f00
s_mov_b32 vcc_hi, 0x0f000f00
v_cndmask_b32 v52, 0, %[q0], vcc
v_cndmask_b32 v53, 0, %[q1], vcc
v_cndmask_b32 v54, 0, %[q2], vcc
v_cndmask_b32 v55, 0, %[q3], vcc
s_nop 1
s_nop 0
v_mfma_f32_16x16x32_bf16 v[36:39], v[148:151], v[52:55], v[36:39]
v_mfma_f32_16x16x32_bf16 v[40:43], v[152:155], v[52:55], v[40:43]
v_mfma_f32_16x16x32_bf16 v[44:47], v[156:159], v[52:55], v[44:47]
v_mfma_f32_16x16x32_bf16 v[48:51], v[160:163], v[52:55], v[48:51]
s_mov_b32 vcc_lo, 0xf000f000
s_mov_b32 vcc_hi, 0xf000f000
v_cndmask_b32 v52, 0, %[q0], vcc
v_cndmask_b32 v53, 0, %[q1], vcc
v_cndmask_b32 v54, 0, %[q2], vcc
v_cndmask_b32 v55, 0, %[q3], vcc
s_nop 1
v_mfma_f32_16x16x32_bf16 v[36:39], v[164:167], v[52:55], v[36:39]
v_mfma_f32_16x16x32_bf16 v[40:43], v[168:171], v[52:55], v[40:43]
v_mfma_f32_16x16x32_bf16 v[44:47], v[172:175], v[52:55], v[44:47]
v_mfma_f32_16x16x32_bf16 v[48:51], v[176:179], v[52:55], v[48:51]
s_nop 4
v_cndmask_b32 v36, v36, v38, %[m2]
v_cndmask_b32 v37, v37, v39, %[m2]
v_cndmask_b32 v40, v40, v42, %[m2]
v_cndmask_b32 v41, v41, v43, %[m2]
v_cndmask_b32 v44, v44, v46, %[m2]
v_cndmask_b32 v45, v45, v47, %[m2]
v_cndmask_b32 v48, v48, v50, %[m2]
v_cndmask_b32 v49, v49, v51, %[m2]
v_cndmask_b32 v38, v40, v37, %[m1]
v_cndmask_b32 v39, v36, v41, %[m1]
v_cndmask_b32 v46, v48, v45, %[m1]
v_cndmask_b32 v47, v44, v49, %[m1]
v_add_f32_dpp v39, v38, v39 quad_perm:[1,0,3,2] row_mask:0xf bank_mask:0xf
v_add_f32_dpp v47, v46, v47 quad_perm:[1,0,3,2] row_mask:0xf bank_mask:0xf
v_cndmask_b32 v40, v47, v39, %[m2]
v_cndmask_b32 v41, v39, v47, %[m2]
s_nop 0
v_add_f32_dpp %[sc0], v40, v41 quad_perm:[2,3,0,1] row_mask:0xf bank_mask:0xf
buffer_load_dwordx4 v[248:251], %[ko], %[rk], %[so] offen offset:3072 nt
s_mov_b32 vcc_lo, 0x000f000f
s_mov_b32 vcc_hi, 0x000f000f
v_cndmask_b32 v52, 0, %[q0], vcc
v_cndmask_b32 v53, 0, %[q1], vcc
v_cndmask_b32 v54, 0, %[q2], vcc
v_cndmask_b32 v55, 0, %[q3], vcc
s_nop 1
v_mfma_f32_16x16x32_bf16 v[36:39], v[180:183], v[52:55], 0
v_mfma_f32_16x16x32_bf16 v[40:43], v[184:187], v[52:55], 0
v_mfma_f32_16x16x32_bf16 v[44:47], v[188:191], v[52:55], 0
v_mfma_f32_16x16x32_bf16 v[48:51], v[192:195], v[52:55], 0
s_mov_b32 vcc_lo, 0x00f000f0
s_mov_b32 vcc_hi, 0x00f000f0
v_cndmask_b32 v52, 0, %[q0], vcc
v_cndmask_b32 v53, 0, %[q1], vcc
v_cndmask_b32 v54, 0, %[q2], vcc
v_cndmask_b32 v55, 0, %[q3], vcc
s_nop 1
v_mfma_f32_16x16x32_bf16 v[36:39], v[196:199], v[52:55], v[36:39]
v_mfma_f32_16x16x32_bf16 v[40:43], v[200:203], v[52:55], v[40:43]
v_mfma_f32_16x16x32_bf16 v[44:47], v[204:207], v[52:55], v[44:47]
v_mfma_f32_16x16x32_bf16 v[48:51], v[208:211], v[52:55], v[48:51]
s_branch LP21_%=b
LP21_%=:
s_sleep 1
LP21_%=b:
s_memrealtime vcc
s_waitcnt lgkmcnt(0)
s_sub_u32 %[so], vcc_lo, %[t0]
s_cmp_lt_u32 %[so], 1385
s_cbranch_scc1 LP21_%=
s_cmp_lt_u32 %[so], 1465
s_cbranch_scc0 LP21_%=f
s_waitcnt vmcnt(10)
s_cmp_lt_u32 %[so], 1415
s_cbranch_scc0 LP21_%=f
s_waitcnt vmcnt(6)
LP21_%=f:
s_mov_b32 %[so], 90112
buffer_load_dwordx4 v[116:119], %[ko], %[rk], %[so] offen offset:0 nt
s_mov_b32 vcc_lo, 0x0f000f00
s_mov_b32 vcc_hi, 0x0f000f00
v_cndmask_b32 v52, 0, %[q0], vcc
v_cndmask_b32 v53, 0, %[q1], vcc
v_cndmask_b32 v54, 0, %[q2], vcc
v_cndmask_b32 v55, 0, %[q3], vcc
s_nop 1
s_nop 0
v_mfma_f32_16x16x32_bf16 v[36:39], v[212:215], v[52:55], v[36:39]
v_mfma_f32_16x16x32_bf16 v[40:43], v[216:219], v[52:55], v[40:43]
v_mfma_f32_16x16x32_bf16 v[44:47], v[220:223], v[52:55], v[44:47]
v_mfma_f32_16x16x32_bf16 v[48:51], v[224:227], v[52:55], v[48:51]
s_mov_b32 vcc_lo, 0xf000f000
s_mov_b32 vcc_hi, 0xf000f000
v_cndmask_b32 v52, 0, %[q0], vcc
v_cndmask_b32 v53, 0, %[q1], vcc
v_cndmask_b32 v54, 0, %[q2], vcc
v_cndmask_b32 v55, 0, %[q3], vcc
s_nop 1
v_mfma_f32_16x16x32_bf16 v[36:39], v[228:231], v[52:55], v[36:39]
v_mfma_f32_16x16x32_bf16 v[40:43], v[232:235], v[52:55], v[40:43]
v_mfma_f32_16x16x32_bf16 v[44:47], v[236:239], v[52:55], v[44:47]
v_mfma_f32_16x16x32_bf16 v[48:51], v[240:243], v[52:55], v[48:51]
s_nop 4
v_cndmask_b32 v36, v36, v38, %[m2]
v_cndmask_b32 v37, v37, v39, %[m2]
v_cndmask_b32 v40, v40, v42, %[m2]
v_cndmask_b32 v41, v41, v43, %[m2]
v_cndmask_b32 v44, v44, v46, %[m2]
v_cndmask_b32 v45, v45, v47, %[m2]
v_cndmask_b32 v48, v48, v50, %[m2]
v_cndmask_b32 v49, v49, v51, %[m2]
v_cndmask_b32 v38, v40, v37, %[m1]
v_cndmask_b32 v39, v36, v41, %[m1]
v_cndmask_b32 v46, v48, v45, %[m1]
v_cndmask_b32 v47, v44, v49, %[m1]
v_add_f32_dpp v39, v38, v39 quad_perm:[1,0,3,2] row_mask:0xf bank_mask:0xf
v_add_f32_dpp v47, v46, v47 quad_perm:[1,0,3,2] row_mask:0xf bank_mask:0xf
v_cndmask_b32 v40, v47, v39, %[m2]
v_cndmask_b32 v41, v39, v47, %[m2]
s_nop 0
s_nop 0
v_add_f32_dpp %[sc1], v40, v41 quad_perm:[2,3,0,1] row_mask:0xf bank_mask:0xf
buffer_load_dword v88, %[go], %[rg], 0 offen offset:0
buffer_load_dwordx4 v[120:123], %[ko], %[rk], %[so] offen offset:1024 nt
s_mov_b32 vcc_lo, 0x000f000f
s_mov_b32 vcc_hi, 0x000f000f
v_cndmask_b32 v52, 0, %[q0], vcc
v_cndmask_b32 v53, 0, %[q1], vcc
v_cndmask_b32 v54, 0, %[q2], vcc
v_cndmask_b32 v55, 0, %[q3], vcc
s_nop 1
s_nop 0
v_mfma_f32_16x16x32_bf16 v[36:39], v[244:247], v[52:55], 0
v_mfma_f32_16x16x32_bf16 v[40:43], a[0:3], v[52:55], 0
v_mfma_f32_16x16x32_bf16 v[44:47], a[4:7], v[52:55], 0
v_mfma_f32_16x16x32_bf16 v[48:51], a[8:11], v[52:55], 0
s_mov_b32 vcc_lo, 0x00f000f0
s_mov_b32 vcc_hi, 0x00f000f0
v_cndmask_b32 v52, 0, %[q0], vcc
v_cndmask_b32 v53, 0, %[q1], vcc
v_cndmask_b32 v54, 0, %[q2], vcc
v_cndmask_b32 v55, 0, %[q3], vcc
s_nop 1
v_mfma_f32_16x16x32_bf16 v[36:39], a[12:15], v[52:55], v[36:39]
v_mfma_f32_16x16x32_bf16 v[40:43], a[16:19], v[52:55], v[40:43]
v_mfma_f32_16x16x32_bf16 v[44:47], a[20:23], v[52:55], v[44:47]
v_mfma_f32_16x16x32_bf16 v[48:51], a[24:27], v[52:55], v[48:51]
s_waitcnt vmcnt(15)
buffer_load_dwordx4 v[124:127], %[ko], %[rk], %[so] offen offset:2048 nt
s_mov_b32 vcc_lo, 0x0f000f00
s_mov_b32 vcc_hi, 0x0f000f00
v_cndmask_b32 v52, 0, %[q0], vcc
v_cndmask_b32 v53, 0, %[q1], vcc
v_cndmask_b32 v54, 0, %[q2], vcc
v_cndmask_b32 v55, 0, %[q3], vcc
s_nop 1
s_nop 0
v_mfma_f32_16x16x32_bf16 v[36:39], a[28:31], v[52:55], v[36:39]
v_mfma_f32_16x16x32_bf16 v[40:43], a[32:35], v[52:55], v[40:43]
v_mfma_f32_16x16x32_bf16 v[44:47], a[36:39], v[52:55], v[44:47]
v_mfma_f32_16x16x32_bf16 v[48:51], a[40:43], v[52:55], v[48:51]
s_mov_b32 vcc_lo, 0xf000f000
s_mov_b32 vcc_hi, 0xf000f000
v_cndmask_b32 v52, 0, %[q0], vcc
v_cndmask_b32 v53, 0, %[q1], vcc
v_cndmask_b32 v54, 0, %[q2], vcc
v_cndmask_b32 v55, 0, %[q3], vcc
s_nop 1
v_mfma_f32_16x16x32_bf16 v[36:39], a[44:47], v[52:55], v[36:39]
v_mfma_f32_16x16x32_bf16 v[40:43], a[48:51], v[52:55], v[40:43]
v_mfma_f32_16x16x32_bf16 v[44:47], a[52:55], v[52:55], v[44:47]
v_mfma_f32_16x16x32_bf16 v[48:51], a[56:59], v[52:55], v[48:51]
s_nop 4
v_cndmask_b32 v36, v36, v38, %[m2]
v_cndmask_b32 v37, v37, v39, %[m2]
v_cndmask_b32 v40, v40, v42, %[m2]
v_cndmask_b32 v41, v41, v43, %[m2]
v_cndmask_b32 v44, v44, v46, %[m2]
v_cndmask_b32 v45, v45, v47, %[m2]
v_cndmask_b32 v48, v48, v50, %[m2]
v_cndmask_b32 v49, v49, v51, %[m2]
v_cndmask_b32 v38, v40, v37, %[m1]
v_cndmask_b32 v39, v36, v41, %[m1]
v_cndmask_b32 v46, v48, v45, %[m1]
v_cndmask_b32 v47, v44, v49, %[m1]
v_add_f32_dpp v39, v38, v39 quad_perm:[1,0,3,2] row_mask:0xf bank_mask:0xf
v_add_f32_dpp v47, v46, v47 quad_perm:[1,0,3,2] row_mask:0xf bank_mask:0xf
v_cndmask_b32 v40, v47, v39, %[m2]
v_cndmask_b32 v41, v39, v47, %[m2]
s_nop 0
v_add_f32_dpp %[sc2], v40, v41 quad_perm:[2,3,0,1] row_mask:0xf bank_mask:0xf
s_waitcnt vmcnt(15)
buffer_load_dwordx4 v[128:131], %[ko], %[rk], %[so] offen offset:3072 nt
s_mov_b32 vcc_lo, 0x000f000f
s_mov_b32 vcc_hi, 0x000f000f
v_cndmask_b32 v52, 0, %[q0], vcc
v_cndmask_b32 v53, 0, %[q1], vcc
v_cndmask_b32 v54, 0, %[q2], vcc
v_cndmask_b32 v55, 0, %[q3], vcc
s_nop 1
s_nop 0
v_mfma_f32_16x16x32_bf16 v[36:39], a[60:63], v[52:55], 0
v_mfma_f32_16x16x32_bf16 v[40:43], a[64:67], v[52:55], 0
v_mfma_f32_16x16x32_bf16 v[44:47], a[68:71], v[52:55], 0
v_mfma_f32_16x16x32_bf16 v[48:51], a[72:75], v[52:55], 0
s_mov_b32 vcc_lo, 0x00f000f0
s_mov_b32 vcc_hi, 0x00f000f0
v_cndmask_b32 v52, 0, %[q0], vcc
v_cndmask_b32 v53, 0, %[q1], vcc
v_cndmask_b32 v54, 0, %[q2], vcc
v_cndmask_b32 v55, 0, %[q3], vcc
s_nop 1
v_mfma_f32_16x16x32_bf16 v[36:39], a[76:79], v[52:55], v[36:39]
v_mfma_f32_16x16x32_bf16 v[40:43], a[80:83], v[52:55], v[40:43]
v_mfma_f32_16x16x32_bf16 v[44:47], a[84:87], v[52:55], v[44:47]
v_mfma_f32_16x16x32_bf16 v[48:51], a[88:91], v[52:55], v[48:51]
s_movk_i32 s94, 3200
v_accvgpr_read_b32 v108, a68
v_accvgpr_read_b32 v109, a69
v_accvgpr_read_b32 v110, a70
v_accvgpr_read_b32 v111, a71
v_perm_b32 v104, v109, v108, s96
v_perm_b32 v105, v111, v110, s96
v_perm_b32 v106, v109, v108, s97
v_perm_b32 v107, v111, v110, s97
v_pk_mad_u16 v108, v108, 2, -1 op_sel_hi:[1,0,0]
v_pk_mad_u16 v109, v109, 2, -1 op_sel_hi:[1,0,0]
v_pk_mad_u16 v110, v110, 2, -1 op_sel_hi:[1,0,0]
v_pk_mad_u16 v111, v111, 2, -1 op_sel_hi:[1,0,0]
v_and_b32 v112, 0x7f7f7f7f, v104
v_and_b32 v113, 0x7f7f7f7f, v105
v_sub_u32 v112, s80, v112
v_sub_u32 v113, s80, v113
v_pk_min_u16 v108, v108, v109
v_pk_min_u16 v110, v110, v111
v_pk_min_u16 v108, v108, v110
v_bitop3_b32 v109, v112, v113, s98 bitop3:0xa8
v_pk_sub_u16 v108, s81, v108 clamp
v_cmp_ne_u32_e64 vcc, v108, v109
v_perm_b32 v112, s99, v115, v112
v_perm_b32 v113, s99, v115, v113
v_bitop3_b32 v112, v112, v104, s98 bitop3:0x8d
v_bitop3_b32 v113, v113, v105, s98 bitop3:0x8d
v_alignbit_b32 v113, v113, v113, 28
v_or_b32 v114, v112, v113
s_getpc_b64 s[92:93]
s_cbranch_vccnz Lexc
v_accvgpr_write_b32 a212, v114
v_accvgpr_write_b32 a213, v106
v_accvgpr_write_b32 a214, v107
s_movk_i32 s94, 3264
v_accvgpr_read_b32 v108, a72
v_accvgpr_read_b32 v109, a73
v_accvgpr_read_b32 v110, a74
v_accvgpr_read_b32 v111, a75
v_perm_b32 v104, v109, v108, s96
v_perm_b32 v105, v111, v110, s96
v_perm_b32 v106, v109, v108, s97
v_perm_b32 v107, v111, v110, s97
v_pk_mad_u16 v108, v108, 2, -1 op_sel_hi:[1,0,0]
v_pk_mad_u16 v109, v109, 2, -1 op_sel_hi:[1,0,0]
v_pk_mad_u16 v110, v110, 2, -1 op_sel_hi:[1,0,0]
v_pk_mad_u16 v111, v111, 2, -1 op_sel_hi:[1,0,0]
v_and_b32 v112, 0x7f7f7f7f, v104
v_and_b32 v113, 0x7f7f7f7f, v105
v_sub_u32 v112, s80, v112
v_sub_u32 v113, s80, v113
v_pk_min_u16 v108, v108, v109
v_pk_min_u16 v110, v110, v111
v_pk_min_u16 v108, v108, v110
v_bitop3_b32 v109, v112, v113, s98 bitop3:0xa8
v_pk_sub_u16 v108, s81, v108 clamp
v_cmp_ne_u32_e64 vcc, v108, v109
v_perm_b32 v112, s99, v115, v112
v_perm_b32 v113, s99, v115, v113
v_bitop3_b32 v112, v112, v104, s98 bitop3:0x8d
v_bitop3_b32 v113, v113, v105, s98 bitop3:0x8d
v_alignbit_b32 v113, v113, v113, 28
v_or_b32 v114, v112, v113
s_getpc_b64 s[92:93]
s_cbranch_vccnz Lexc
v_accvgpr_write_b32 a215, v114
v_accvgpr_write_b32 a216, v106
v_accvgpr_write_b32 a217, v107
s_waitcnt vmcnt(15)
buffer_load_dword v96, %[go], %[ru], 0 offen offset:0
s_waitcnt vmcnt(15)
s_branch LP22_%=b
LP22_%=:
s_sleep 1
)" R"(
LP22_%=b:
s_memrealtime vcc
s_waitcnt lgkmcnt(0)
s_sub_u32 %[so], vcc_lo, %[t0]
s_cmp_lt_u32 %[so], 1447
s_cbranch_scc1 LP22_%=
s_cmp_lt_u32 %[so], 1527
s_cbranch_scc0 LP22_%=f
s_waitcnt vmcnt(10)
s_cmp_lt_u32 %[so], 1477
s_cbranch_scc0 LP22_%=f
s_waitcnt vmcnt(6)
LP22_%=f:
s_mov_b32 %[so], 94208
buffer_load_dwordx4 v[132:135], %[ko], %[rk], %[so] offen offset:0 nt
s_mov_b32 vcc_lo, 0x0f000f00
s_mov_b32 vcc_hi, 0x0f000f00
v_cndmask_b32 v52, 0, %[q0], vcc
v_cndmask_b32 v53, 0, %[q1], vcc
v_cndmask_b32 v54, 0, %[q2], vcc
v_cndmask_b32 v55, 0, %[q3], vcc
s_nop 1
v_mfma_f32_16x16x32_bf16 v[36:39], a[92:95], v[52:55], v[36:39]
v_mfma_f32_16x16x32_bf16 v[40:43], a[96:99], v[52:55], v[40:43]
v_mfma_f32_16x16x32_bf16 v[44:47], a[100:103], v[52:55], v[44:47]
s_movk_i32 s94, 3328
v_accvgpr_read_b32 v108, a76
v_accvgpr_read_b32 v109, a77
v_accvgpr_read_b32 v110, a78
v_accvgpr_read_b32 v111, a79
v_perm_b32 v104, v109, v108, s96
v_perm_b32 v105, v111, v110, s96
v_perm_b32 v106, v109, v108, s97
v_perm_b32 v107, v111, v110, s97
v_pk_mad_u16 v108, v108, 2, -1 op_sel_hi:[1,0,0]
v_pk_mad_u16 v109, v109, 2, -1 op_sel_hi:[1,0,0]
v_pk_mad_u16 v110, v110, 2, -1 op_sel_hi:[1,0,0]
v_pk_mad_u16 v111, v111, 2, -1 op_sel_hi:[1,0,0]
v_and_b32 v112, 0x7f7f7f7f, v104
v_and_b32 v113, 0x7f7f7f7f, v105
v_sub_u32 v112, s80, v112
v_sub_u32 v113, s80, v113
v_pk_min_u16 v108, v108, v109
v_pk_min_u16 v110, v110, v111
v_pk_min_u16 v108, v108, v110
v_bitop3_b32 v109, v112, v113, s98 bitop3:0xa8
v_pk_sub_u16 v108, s81, v108 clamp
v_cmp_ne_u32_e64 vcc, v108, v109
v_perm_b32 v112, s99, v115, v112
v_perm_b32 v113, s99, v115, v113
v_bitop3_b32 v112, v112, v104, s98 bitop3:0x8d
v_bitop3_b32 v113, v113, v105, s98 bitop3:0x8d
v_alignbit_b32 v113, v113, v113, 28
v_or_b32 v114, v112, v113
s_getpc_b64 s[92:93]
s_cbranch_vccnz Lexc
v_accvgpr_write_b32 a218, v114
v_accvgpr_write_b32 a219, v106
v_accvgpr_write_b32 a220, v107
s_waitcnt vmcnt(15)
buffer_load_dwordx4 v[136:139], %[ko], %[rk], %[so] offen offset:1024 nt
v_mfma_f32_16x16x32_bf16 v[48:51], a[104:107], v[52:55], v[48:51]
s_mov_b32 vcc_lo, 0xf000f000
s_mov_b32 vcc_hi, 0xf000f000
v_cndmask_b32 v52, 0, %[q0], vcc
v_cndmask_b32 v53, 0, %[q1], vcc
v_cndmask_b32 v54, 0, %[q2], vcc
v_cndmask_b32 v55, 0, %[q3], vcc
s_nop 1
v_mfma_f32_16x16x32_bf16 v[36:39], a[108:111], v[52:55], v[36:39]
v_mfma_f32_16x16x32_bf16 v[40:43], a[112:115], v[52:55], v[40:43]
s_movk_i32 s94, 3392
v_accvgpr_read_b32 v108, a80
v_accvgpr_read_b32 v109, a81
v_accvgpr_read_b32 v110, a82
v_accvgpr_read_b32 v111, a83
v_perm_b32 v104, v109, v108, s96
v_perm_b32 v105, v111, v110, s96
v_perm_b32 v106, v109, v108, s97
v_perm_b32 v107, v111, v110, s97
v_pk_mad_u16 v108, v108, 2, -1 op_sel_hi:[1,0,0]
v_pk_mad_u16 v109, v109, 2, -1 op_sel_hi:[1,0,0]
v_pk_mad_u16 v110, v110, 2, -1 op_sel_hi:[1,0,0]
v_pk_mad_u16 v111, v111, 2, -1 op_sel_hi:[1,0,0]
v_and_b32 v112, 0x7f7f7f7f, v104
v_and_b32 v113, 0x7f7f7f7f, v105
v_sub_u32 v112, s80, v112
v_sub_u32 v113, s80, v113
v_pk_min_u16 v108, v108, v109
v_pk_min_u16 v110, v110, v111
v_pk_min_u16 v108, v108, v110
v_bitop3_b32 v109, v112, v113, s98 bitop3:0xa8
v_pk_sub_u16 v108, s81, v108 clamp
v_cmp_ne_u32_e64 vcc, v108, v109
v_perm_b32 v112, s99, v115, v112
v_perm_b32 v113, s99, v115, v113
v_bitop3_b32 v112, v112, v104, s98 bitop3:0x8d
v_bitop3_b32 v113, v113, v105, s98 bitop3:0x8d
v_alignbit_b32 v113, v113, v113, 28
v_or_b32 v114, v112, v113
s_getpc_b64 s[92:93]
s_cbranch_vccnz Lexc
v_accvgpr_write_b32 a221, v114
v_accvgpr_write_b32 a222, v106
v_accvgpr_write_b32 a223, v107
s_waitcnt vmcnt(15)
buffer_load_dwordx4 v[140:143], %[ko], %[rk], %[so] offen offset:2048 nt
v_mfma_f32_16x16x32_bf16 v[44:47], a[116:119], v[52:55], v[44:47]
v_mfma_f32_16x16x32_bf16 v[48:51], a[120:123], v[52:55], v[48:51]
s_movk_i32 s94, 3456
s_nop 0
v_accvgpr_read_b32 v108, a84
v_accvgpr_read_b32 v109, a85
v_accvgpr_read_b32 v110, a86
v_accvgpr_read_b32 v111, a87
v_perm_b32 v104, v109, v108, s96
v_perm_b32 v105, v111, v110, s96
v_perm_b32 v106, v109, v108, s97
v_perm_b32 v107, v111, v110, s97
v_pk_mad_u16 v108, v108, 2, -1 op_sel_hi:[1,0,0]
v_pk_mad_u16 v109, v109, 2, -1 op_sel_hi:[1,0,0]
v_pk_mad_u16 v110, v110, 2, -1 op_sel_hi:[1,0,0]
v_pk_mad_u16 v111, v111, 2, -1 op_sel_hi:[1,0,0]
v_and_b32 v112, 0x7f7f7f7f, v104
v_and_b32 v113, 0x7f7f7f7f, v105
v_sub_u32 v112, s80, v112
v_sub_u32 v113, s80, v113
v_pk_min_u16 v108, v108, v109
v_pk_min_u16 v110, v110, v111
v_pk_min_u16 v108, v108, v110
v_bitop3_b32 v109, v112, v113, s98 bitop3:0xa8
v_pk_sub_u16 v108, s81, v108 clamp
v_cmp_ne_u32_e64 vcc, v108, v109
v_perm_b32 v112, s99, v115, v112
v_perm_b32 v113, s99, v115, v113
v_bitop3_b32 v112, v112, v104, s98 bitop3:0x8d
v_bitop3_b32 v113, v113, v105, s98 bitop3:0x8d
v_alignbit_b32 v113, v113, v113, 28
v_or_b32 v114, v112, v113
s_getpc_b64 s[92:93]
s_cbranch_vccnz Lexc
s_nop 0
v_accvgpr_write_b32 a224, v114
v_accvgpr_write_b32 a225, v106
v_accvgpr_write_b32 a226, v107
v_cndmask_b32 v36, v36, v38, %[m2]
v_cndmask_b32 v37, v37, v39, %[m2]
v_cndmask_b32 v40, v40, v42, %[m2]
v_cndmask_b32 v41, v41, v43, %[m2]
v_cndmask_b32 v44, v44, v46, %[m2]
v_cndmask_b32 v45, v45, v47, %[m2]
v_cndmask_b32 v48, v48, v50, %[m2]
v_cndmask_b32 v49, v49, v51, %[m2]
v_cndmask_b32 v38, v40, v37, %[m1]
v_cndmask_b32 v39, v36, v41, %[m1]
v_cndmask_b32 v46, v48, v45, %[m1]
v_cndmask_b32 v47, v44, v49, %[m1]
v_add_f32_dpp v39, v38, v39 quad_perm:[1,0,3,2] row_mask:0xf bank_mask:0xf
v_add_f32_dpp v47, v46, v47 quad_perm:[1,0,3,2] row_mask:0xf bank_mask:0xf
v_cndmask_b32 v40, v47, v39, %[m2]
v_cndmask_b32 v41, v39, v47, %[m2]
s_nop 0
v_add_f32_dpp %[sc3], v40, v41 quad_perm:[2,3,0,1] row_mask:0xf bank_mask:0xf
s_waitcnt vmcnt(15)
buffer_load_dwordx4 v[144:147], %[ko], %[rk], %[so] offen offset:3072 nt
s_mov_b32 vcc_lo, 0x000f000f
s_mov_b32 vcc_hi, 0x000f000f
v_cndmask_b32 v52, 0, %[q0], vcc
v_cndmask_b32 v53, 0, %[q1], vcc
v_cndmask_b32 v54, 0, %[q2], vcc
v_cndmask_b32 v55, 0, %[q3], vcc
s_nop 1
v_mfma_f32_16x16x32_bf16 v[36:39], a[124:127], v[52:55], 0
v_mfma_f32_16x16x32_bf16 v[40:43], a[128:131], v[52:55], 0
v_mfma_f32_16x16x32_bf16 v[44:47], a[132:135], v[52:55], 0
s_movk_i32 s94, 3520
v_accvgpr_read_b32 v108, a88
v_accvgpr_read_b32 v109, a89
v_accvgpr_read_b32 v110, a90
v_accvgpr_read_b32 v111, a91
v_perm_b32 v104, v109, v108, s96
v_perm_b32 v105, v111, v110, s96
v_perm_b32 v106, v109, v108, s97
v_perm_b32 v107, v111, v110, s97
v_pk_mad_u16 v108, v108, 2, -1 op_sel_hi:[1,0,0]
v_pk_mad_u16 v109, v109, 2, -1 op_sel_hi:[1,0,0]
v_pk_mad_u16 v110, v110, 2, -1 op_sel_hi:[1,0,0]
v_pk_mad_u16 v111, v111, 2, -1 op_sel_hi:[1,0,0]
v_and_b32 v112, 0x7f7f7f7f, v104
v_and_b32 v113, 0x7f7f7f7f, v105
v_sub_u32 v112, s80, v112
v_sub_u32 v113, s80, v113
v_pk_min_u16 v108, v108, v109
v_pk_min_u16 v110, v110, v111
v_pk_min_u16 v108, v108, v110
v_bitop3_b32 v109, v112, v113, s98 bitop3:0xa8
v_pk_sub_u16 v108, s81, v108 clamp
v_cmp_ne_u32_e64 vcc, v108, v109
v_perm_b32 v112, s99, v115, v112
v_perm_b32 v113, s99, v115, v113
v_bitop3_b32 v112, v112, v104, s98 bitop3:0x8d
v_bitop3_b32 v113, v113, v105, s98 bitop3:0x8d
v_alignbit_b32 v113, v113, v113, 28
v_or_b32 v114, v112, v113
s_getpc_b64 s[92:93]
s_cbranch_vccnz Lexc
v_accvgpr_write_b32 a227, v114
v_accvgpr_write_b32 a228, v106
v_accvgpr_write_b32 a229, v107
s_waitcnt vmcnt(15)
buffer_load_dword v92, %[go], %[rg], 0 offen offset:2752
s_waitcnt vmcnt(15)
s_branch LP23_%=b
LP23_%=:
s_sleep 1
LP23_%=b:
s_memrealtime vcc
s_waitcnt lgkmcnt(0)
s_sub_u32 %[so], vcc_lo, %[t0]
s_cmp_lt_u32 %[so], 1510
s_cbranch_scc1 LP23_%=
s_cmp_lt_u32 %[so], 1590
s_cbranch_scc0 LP23_%=f
s_waitcnt vmcnt(10)
s_cmp_lt_u32 %[so], 1540
s_cbranch_scc0 LP23_%=f
s_waitcnt vmcnt(6)
LP23_%=f:
s_mov_b32 %[so], 98304
buffer_load_dwordx4 v[148:151], %[ko], %[rk], %[so] offen offset:0 nt
v_mfma_f32_16x16x32_bf16 v[48:51], a[136:139], v[52:55], 0
s_mov_b32 vcc_lo, 0x00f000f0
s_mov_b32 vcc_hi, 0x00f000f0
v_cndmask_b32 v52, 0, %[q0], vcc
v_cndmask_b32 v53, 0, %[q1], vcc
v_cndmask_b32 v54, 0, %[q2], vcc
v_cndmask_b32 v55, 0, %[q3], vcc
s_nop 1
v_mfma_f32_16x16x32_bf16 v[36:39], a[140:143], v[52:55], v[36:39]
v_mfma_f32_16x16x32_bf16 v[40:43], a[144:147], v[52:55], v[40:43]
s_movk_i32 s94, 3584
v_accvgpr_read_b32 v108, a92
v_accvgpr_read_b32 v109, a93
v_accvgpr_read_b32 v110, a94
v_accvgpr_read_b32 v111, a95
v_perm_b32 v104, v109, v108, s96
v_perm_b32 v105, v111, v110, s96
v_perm_b32 v106, v109, v108, s97
v_perm_b32 v107, v111, v110, s97
v_pk_mad_u16 v108, v108, 2, -1 op_sel_hi:[1,0,0]
v_pk_mad_u16 v109, v109, 2, -1 op_sel_hi:[1,0,0]
v_pk_mad_u16 v110, v110, 2, -1 op_sel_hi:[1,0,0]
v_pk_mad_u16 v111, v111, 2, -1 op_sel_hi:[1,0,0]
v_and_b32 v112, 0x7f7f7f7f, v104
v_and_b32 v113, 0x7f7f7f7f, v105
v_sub_u32 v112, s80, v112
v_sub_u32 v113, s80, v113
v_pk_min_u16 v108, v108, v109
v_pk_min_u16 v110, v110, v111
v_pk_min_u16 v108, v108, v110
v_bitop3_b32 v109, v112, v113, s98 bitop3:0xa8
v_pk_sub_u16 v108, s81, v108 clamp
v_cmp_ne_u32_e64 vcc, v108, v109
v_perm_b32 v112, s99, v115, v112
v_perm_b32 v113, s99, v115, v113
v_bitop3_b32 v112, v112, v104, s98 bitop3:0x8d
v_bitop3_b32 v113, v113, v105, s98 bitop3:0x8d
v_alignbit_b32 v113, v113, v113, 28
v_or_b32 v114, v112, v113
s_getpc_b64 s[92:93]
s_cbranch_vccnz Lexc
v_accvgpr_write_b32 a230, v114
v_accvgpr_write_b32 a231, v106
v_accvgpr_write_b32 a232, v107
s_waitcnt vmcnt(15)
buffer_load_dwordx4 v[152:155], %[ko], %[rk], %[so] offen offset:1024 nt
v_mfma_f32_16x16x32_bf16 v[44:47], a[148:151], v[52:55], v[44:47]
v_mfma_f32_16x16x32_bf16 v[48:51], a[152:155], v[52:55], v[48:51]
s_mov_b32 vcc_lo, 0x0f000f00
s_mov_b32 vcc_hi, 0x0f000f00
v_cndmask_b32 v52, 0, %[q0], vcc
v_cndmask_b32 v53, 0, %[q1], vcc
v_cndmask_b32 v54, 0, %[q2], vcc
v_cndmask_b32 v55, 0, %[q3], vcc
s_nop 1
v_mfma_f32_16x16x32_bf16 v[36:39], a[156:159], v[52:55], v[36:39]
s_movk_i32 s94, 3648
v_accvgpr_read_b32 v108, a96
v_accvgpr_read_b32 v109, a97
v_accvgpr_read_b32 v110, a98
v_accvgpr_read_b32 v111, a99
v_perm_b32 v104, v109, v108, s96
v_perm_b32 v105, v111, v110, s96
v_perm_b32 v106, v109, v108, s97
v_perm_b32 v107, v111, v110, s97
v_pk_mad_u16 v108, v108, 2, -1 op_sel_hi:[1,0,0]
v_pk_mad_u16 v109, v109, 2, -1 op_sel_hi:[1,0,0]
v_pk_mad_u16 v110, v110, 2, -1 op_sel_hi:[1,0,0]
v_pk_mad_u16 v111, v111, 2, -1 op_sel_hi:[1,0,0]
v_and_b32 v112, 0x7f7f7f7f, v104
v_and_b32 v113, 0x7f7f7f7f, v105
v_sub_u32 v112, s80, v112
v_sub_u32 v113, s80, v113
v_pk_min_u16 v108, v108, v109
v_pk_min_u16 v110, v110, v111
v_pk_min_u16 v108, v108, v110
v_bitop3_b32 v109, v112, v113, s98 bitop3:0xa8
v_pk_sub_u16 v108, s81, v108 clamp
v_cmp_ne_u32_e64 vcc, v108, v109
v_perm_b32 v112, s99, v115, v112
v_perm_b32 v113, s99, v115, v113
v_bitop3_b32 v112, v112, v104, s98 bitop3:0x8d
v_bitop3_b32 v113, v113, v105, s98 bitop3:0x8d
v_alignbit_b32 v113, v113, v113, 28
v_or_b32 v114, v112, v113
s_getpc_b64 s[92:93]
s_cbranch_vccnz Lexc
v_accvgpr_write_b32 a233, v114
v_accvgpr_write_b32 a234, v106
v_accvgpr_write_b32 a235, v107
s_waitcnt vmcnt(15)
buffer_load_dwordx4 v[156:159], %[ko], %[rk], %[so] offen offset:2048 nt
v_mfma_f32_16x16x32_bf16 v[40:43], a[160:163], v[52:55], v[40:43]
v_mfma_f32_16x16x32_bf16 v[44:47], a[164:167], v[52:55], v[44:47]
v_mfma_f32_16x16x32_bf16 v[48:51], a[168:171], v[52:55], v[48:51]
s_movk_i32 s94, 3712
s_nop 0
v_accvgpr_read_b32 v108, a100
v_accvgpr_read_b32 v109, a101
v_accvgpr_read_b32 v110, a102
v_accvgpr_read_b32 v111, a103
v_perm_b32 v104, v109, v108, s96
v_perm_b32 v105, v111, v110, s96
v_perm_b32 v106, v109, v108, s97
v_perm_b32 v107, v111, v110, s97
v_pk_mad_u16 v108, v108, 2, -1 op_sel_hi:[1,0,0]
v_pk_mad_u16 v109, v109, 2, -1 op_sel_hi:[1,0,0]
v_pk_mad_u16 v110, v110, 2, -1 op_sel_hi:[1,0,0]
v_pk_mad_u16 v111, v111, 2, -1 op_sel_hi:[1,0,0]
v_and_b32 v112, 0x7f7f7f7f, v104
v_and_b32 v113, 0x7f7f7f7f, v105
v_sub_u32 v112, s80, v112
v_sub_u32 v113, s80, v113
v_pk_min_u16 v108, v108, v109
v_pk_min_u16 v110, v110, v111
v_pk_min_u16 v108, v108, v110
v_bitop3_b32 v109, v112, v113, s98 bitop3:0xa8
v_pk_sub_u16 v108, s81, v108 clamp
v_cmp_ne_u32_e64 vcc, v108, v109
)" R"(
v_perm_b32 v112, s99, v115, v112
v_perm_b32 v113, s99, v115, v113
v_bitop3_b32 v112, v112, v104, s98 bitop3:0x8d
v_bitop3_b32 v113, v113, v105, s98 bitop3:0x8d
v_alignbit_b32 v113, v113, v113, 28
v_or_b32 v114, v112, v113
s_getpc_b64 s[92:93]
s_cbranch_vccnz Lexc
v_accvgpr_write_b32 a236, v114
v_accvgpr_write_b32 a237, v106
v_accvgpr_write_b32 a238, v107
s_waitcnt vmcnt(15)
buffer_load_dword v100, %[go], %[ru], 0 offen offset:2752
s_waitcnt vmcnt(15)
buffer_load_dwordx4 v[160:163], %[ko], %[rk], %[so] offen offset:3072 nt
s_mov_b32 vcc_lo, 0xf000f000
s_mov_b32 vcc_hi, 0xf000f000
v_cndmask_b32 v52, 0, %[q0], vcc
v_cndmask_b32 v53, 0, %[q1], vcc
v_cndmask_b32 v54, 0, %[q2], vcc
v_cndmask_b32 v55, 0, %[q3], vcc
s_nop 1
v_mfma_f32_16x16x32_bf16 v[36:39], a[172:175], v[52:55], v[36:39]
v_mfma_f32_16x16x32_bf16 v[40:43], a[176:179], v[52:55], v[40:43]
v_mfma_f32_16x16x32_bf16 v[44:47], a[180:183], v[52:55], v[44:47]
s_movk_i32 s94, 3776
s_nop 0
v_accvgpr_read_b32 v108, a104
v_accvgpr_read_b32 v109, a105
v_accvgpr_read_b32 v110, a106
v_accvgpr_read_b32 v111, a107
v_perm_b32 v104, v109, v108, s96
v_perm_b32 v105, v111, v110, s96
v_perm_b32 v106, v109, v108, s97
v_perm_b32 v107, v111, v110, s97
v_pk_mad_u16 v108, v108, 2, -1 op_sel_hi:[1,0,0]
v_pk_mad_u16 v109, v109, 2, -1 op_sel_hi:[1,0,0]
v_pk_mad_u16 v110, v110, 2, -1 op_sel_hi:[1,0,0]
v_pk_mad_u16 v111, v111, 2, -1 op_sel_hi:[1,0,0]
v_and_b32 v112, 0x7f7f7f7f, v104
v_and_b32 v113, 0x7f7f7f7f, v105
v_sub_u32 v112, s80, v112
v_sub_u32 v113, s80, v113
v_pk_min_u16 v108, v108, v109
v_pk_min_u16 v110, v110, v111
v_pk_min_u16 v108, v108, v110
v_bitop3_b32 v109, v112, v113, s98 bitop3:0xa8
v_pk_sub_u16 v108, s81, v108 clamp
v_cmp_ne_u32_e64 vcc, v108, v109
v_perm_b32 v112, s99, v115, v112
v_perm_b32 v113, s99, v115, v113
v_bitop3_b32 v112, v112, v104, s98 bitop3:0x8d
v_bitop3_b32 v113, v113, v105, s98 bitop3:0x8d
v_alignbit_b32 v113, v113, v113, 28
v_or_b32 v114, v112, v113
s_getpc_b64 s[92:93]
s_cbranch_vccnz Lexc
v_accvgpr_write_b32 a239, v114
v_accvgpr_write_b32 a240, v106
v_accvgpr_write_b32 a241, v107
s_waitcnt vmcnt(15)
s_branch LP24_%=b
LP24_%=:
s_sleep 1
LP24_%=b:
s_memrealtime vcc
s_waitcnt lgkmcnt(0)
s_sub_u32 %[so], vcc_lo, %[t0]
s_cmp_lt_u32 %[so], 1572
s_cbranch_scc1 LP24_%=
s_cmp_lt_u32 %[so], 1652
s_cbranch_scc0 LP24_%=f
s_waitcnt vmcnt(10)
s_cmp_lt_u32 %[so], 1602
s_cbranch_scc0 LP24_%=f
s_waitcnt vmcnt(6)
LP24_%=f:
s_mov_b32 %[so], 102400
buffer_load_dwordx4 v[164:167], %[ko], %[rk], %[so] offen offset:0 nt
v_mfma_f32_16x16x32_bf16 v[48:51], a[184:187], v[52:55], v[48:51]
s_movk_i32 s94, 3840
v_accvgpr_read_b32 v108, a108
v_accvgpr_read_b32 v109, a109
v_accvgpr_read_b32 v110, a110
v_accvgpr_read_b32 v111, a111
v_perm_b32 v104, v109, v108, s96
v_perm_b32 v105, v111, v110, s96
v_perm_b32 v106, v109, v108, s97
v_perm_b32 v107, v111, v110, s97
v_pk_mad_u16 v108, v108, 2, -1 op_sel_hi:[1,0,0]
v_pk_mad_u16 v109, v109, 2, -1 op_sel_hi:[1,0,0]
v_pk_mad_u16 v110, v110, 2, -1 op_sel_hi:[1,0,0]
v_pk_mad_u16 v111, v111, 2, -1 op_sel_hi:[1,0,0]
v_and_b32 v112, 0x7f7f7f7f, v104
v_and_b32 v113, 0x7f7f7f7f, v105
v_sub_u32 v112, s80, v112
v_sub_u32 v113, s80, v113
v_pk_min_u16 v108, v108, v109
v_pk_min_u16 v110, v110, v111
v_pk_min_u16 v108, v108, v110
v_bitop3_b32 v109, v112, v113, s98 bitop3:0xa8
v_pk_sub_u16 v108, s81, v108 clamp
v_cmp_ne_u32_e64 vcc, v108, v109
v_perm_b32 v112, s99, v115, v112
v_perm_b32 v113, s99, v115, v113
v_bitop3_b32 v112, v112, v104, s98 bitop3:0x8d
v_bitop3_b32 v113, v113, v105, s98 bitop3:0x8d
v_alignbit_b32 v113, v113, v113, 28
v_or_b32 v114, v112, v113
s_getpc_b64 s[92:93]
s_cbranch_vccnz Lexc
s_nop 0
v_accvgpr_write_b32 a242, v114
v_accvgpr_write_b32 a243, v106
v_accvgpr_write_b32 a244, v107
v_cndmask_b32 v36, v36, v38, %[m2]
v_cndmask_b32 v37, v37, v39, %[m2]
v_cndmask_b32 v40, v40, v42, %[m2]
v_cndmask_b32 v41, v41, v43, %[m2]
v_cndmask_b32 v44, v44, v46, %[m2]
v_cndmask_b32 v45, v45, v47, %[m2]
v_cndmask_b32 v48, v48, v50, %[m2]
v_cndmask_b32 v49, v49, v51, %[m2]
v_cndmask_b32 v38, v40, v37, %[m1]
v_cndmask_b32 v39, v36, v41, %[m1]
v_cndmask_b32 v46, v48, v45, %[m1]
v_cndmask_b32 v47, v44, v49, %[m1]
v_add_f32_dpp v39, v38, v39 quad_perm:[1,0,3,2] row_mask:0xf bank_mask:0xf
v_add_f32_dpp v47, v46, v47 quad_perm:[1,0,3,2] row_mask:0xf bank_mask:0xf
v_cndmask_b32 v40, v47, v39, %[m2]
v_cndmask_b32 v41, v39, v47, %[m2]
s_nop 0
v_add_f32_dpp %[sc4], v40, v41 quad_perm:[2,3,0,1] row_mask:0xf bank_mask:0xf
s_waitcnt vmcnt(15)
buffer_load_dwordx4 v[168:171], %[ko], %[rk], %[so] offen offset:1024 nt
s_mov_b32 vcc_lo, 0x000f000f
s_mov_b32 vcc_hi, 0x000f000f
v_cndmask_b32 v52, 0, %[q0], vcc
v_cndmask_b32 v53, 0, %[q1], vcc
v_cndmask_b32 v54, 0, %[q2], vcc
v_cndmask_b32 v55, 0, %[q3], vcc
s_nop 1
v_mfma_f32_16x16x32_bf16 v[36:39], a[188:191], v[52:55], 0
v_mfma_f32_16x16x32_bf16 v[40:43], a[192:195], v[52:55], 0
v_mfma_f32_16x16x32_bf16 v[44:47], a[196:199], v[52:55], 0
s_movk_i32 s94, 3904
v_accvgpr_read_b32 v108, a112
v_accvgpr_read_b32 v109, a113
v_accvgpr_read_b32 v110, a114
v_accvgpr_read_b32 v111, a115
v_perm_b32 v104, v109, v108, s96
v_perm_b32 v105, v111, v110, s96
v_perm_b32 v106, v109, v108, s97
v_perm_b32 v107, v111, v110, s97
v_pk_mad_u16 v108, v108, 2, -1 op_sel_hi:[1,0,0]
v_pk_mad_u16 v109, v109, 2, -1 op_sel_hi:[1,0,0]
v_pk_mad_u16 v110, v110, 2, -1 op_sel_hi:[1,0,0]
v_pk_mad_u16 v111, v111, 2, -1 op_sel_hi:[1,0,0]
v_and_b32 v112, 0x7f7f7f7f, v104
v_and_b32 v113, 0x7f7f7f7f, v105
v_sub_u32 v112, s80, v112
v_sub_u32 v113, s80, v113
v_pk_min_u16 v108, v108, v109
v_pk_min_u16 v110, v110, v111
v_pk_min_u16 v108, v108, v110
v_bitop3_b32 v109, v112, v113, s98 bitop3:0xa8
v_pk_sub_u16 v108, s81, v108 clamp
v_cmp_ne_u32_e64 vcc, v108, v109
v_perm_b32 v112, s99, v115, v112
v_perm_b32 v113, s99, v115, v113
v_bitop3_b32 v112, v112, v104, s98 bitop3:0x8d
v_bitop3_b32 v113, v113, v105, s98 bitop3:0x8d
v_alignbit_b32 v113, v113, v113, 28
v_or_b32 v114, v112, v113
s_getpc_b64 s[92:93]
s_cbranch_vccnz Lexc
v_accvgpr_write_b32 a245, v114
v_accvgpr_write_b32 a246, v106
v_accvgpr_write_b32 a247, v107
s_movk_i32 s94, 3968
v_accvgpr_read_b32 v108, a116
v_accvgpr_read_b32 v109, a117
v_accvgpr_read_b32 v110, a118
v_accvgpr_read_b32 v111, a119
v_perm_b32 v104, v109, v108, s96
v_perm_b32 v105, v111, v110, s96
v_perm_b32 v106, v109, v108, s97
v_perm_b32 v107, v111, v110, s97
v_pk_mad_u16 v108, v108, 2, -1 op_sel_hi:[1,0,0]
v_pk_mad_u16 v109, v109, 2, -1 op_sel_hi:[1,0,0]
v_pk_mad_u16 v110, v110, 2, -1 op_sel_hi:[1,0,0]
v_pk_mad_u16 v111, v111, 2, -1 op_sel_hi:[1,0,0]
v_and_b32 v112, 0x7f7f7f7f, v104
v_and_b32 v113, 0x7f7f7f7f, v105
v_sub_u32 v112, s80, v112
v_sub_u32 v113, s80, v113
v_pk_min_u16 v108, v108, v109
v_pk_min_u16 v110, v110, v111
v_pk_min_u16 v108, v108, v110
v_bitop3_b32 v109, v112, v113, s98 bitop3:0xa8
v_pk_sub_u16 v108, s81, v108 clamp
v_cmp_ne_u32_e64 vcc, v108, v109
v_perm_b32 v112, s99, v115, v112
v_perm_b32 v113, s99, v115, v113
v_bitop3_b32 v112, v112, v104, s98 bitop3:0x8d
v_bitop3_b32 v113, v113, v105, s98 bitop3:0x8d
v_alignbit_b32 v113, v113, v113, 28
v_or_b32 v114, v112, v113
s_getpc_b64 s[92:93]
s_cbranch_vccnz Lexc
v_accvgpr_write_b32 a248, v114
v_accvgpr_write_b32 a249, v106
v_accvgpr_write_b32 a250, v107
s_waitcnt vmcnt(15)
buffer_load_dwordx4 v[172:175], %[ko], %[rk], %[so] offen offset:2048 nt
v_mfma_f32_16x16x32_bf16 v[48:51], a[200:203], v[52:55], 0
s_mov_b32 vcc_lo, 0x00f000f0
s_mov_b32 vcc_hi, 0x00f000f0
v_cndmask_b32 v52, 0, %[q0], vcc
v_cndmask_b32 v53, 0, %[q1], vcc
v_cndmask_b32 v54, 0, %[q2], vcc
v_cndmask_b32 v55, 0, %[q3], vcc
s_nop 1
v_mfma_f32_16x16x32_bf16 v[36:39], a[204:207], v[52:55], v[36:39]
v_mfma_f32_16x16x32_bf16 v[40:43], a[208:211], v[52:55], v[40:43]
s_movk_i32 s94, 4032
v_accvgpr_read_b32 v108, a120
v_accvgpr_read_b32 v109, a121
v_accvgpr_read_b32 v110, a122
v_accvgpr_read_b32 v111, a123
v_perm_b32 v104, v109, v108, s96
v_perm_b32 v105, v111, v110, s96
v_perm_b32 v106, v109, v108, s97
v_perm_b32 v107, v111, v110, s97
v_pk_mad_u16 v108, v108, 2, -1 op_sel_hi:[1,0,0]
v_pk_mad_u16 v109, v109, 2, -1 op_sel_hi:[1,0,0]
v_pk_mad_u16 v110, v110, 2, -1 op_sel_hi:[1,0,0]
v_pk_mad_u16 v111, v111, 2, -1 op_sel_hi:[1,0,0]
v_and_b32 v112, 0x7f7f7f7f, v104
v_and_b32 v113, 0x7f7f7f7f, v105
v_sub_u32 v112, s80, v112
v_sub_u32 v113, s80, v113
v_pk_min_u16 v108, v108, v109
v_pk_min_u16 v110, v110, v111
v_pk_min_u16 v108, v108, v110
v_bitop3_b32 v109, v112, v113, s98 bitop3:0xa8
v_pk_sub_u16 v108, s81, v108 clamp
v_cmp_ne_u32_e64 vcc, v108, v109
v_perm_b32 v112, s99, v115, v112
v_perm_b32 v113, s99, v115, v113
v_bitop3_b32 v112, v112, v104, s98 bitop3:0x8d
v_bitop3_b32 v113, v113, v105, s98 bitop3:0x8d
v_alignbit_b32 v113, v113, v113, 28
v_or_b32 v114, v112, v113
s_getpc_b64 s[92:93]
s_cbranch_vccnz Lexc
v_accvgpr_write_b32 a251, v114
v_accvgpr_write_b32 a252, v106
v_accvgpr_write_b32 a253, v107
s_waitcnt vmcnt(15)
s_mov_b32 %[so], 11008
buffer_load_dword v89, %[go], %[rg], %[so] offen offset:0
s_waitcnt vmcnt(15)
s_nop 0
s_mov_b32 %[so], 102400
buffer_load_dwordx4 v[176:179], %[ko], %[rk], %[so] offen offset:3072 nt
v_mfma_f32_16x16x32_bf16 v[44:47], v[252:255], v[52:55], v[44:47]
v_mfma_f32_16x16x32_bf16 v[48:51], v[248:251], v[52:55], v[48:51]
s_mov_b32 vcc_lo, 0x0f000f00
s_mov_b32 vcc_hi, 0x0f000f00
v_cndmask_b32 v52, 0, %[q0], vcc
v_cndmask_b32 v53, 0, %[q1], vcc
v_cndmask_b32 v54, 0, %[q2], vcc
v_cndmask_b32 v55, 0, %[q3], vcc
s_nop 1
v_mfma_f32_16x16x32_bf16 v[36:39], v[116:119], v[52:55], v[36:39]
s_movk_i32 s94, 4096
v_accvgpr_read_b32 v108, a124
v_accvgpr_read_b32 v109, a125
v_accvgpr_read_b32 v110, a126
v_accvgpr_read_b32 v111, a127
v_perm_b32 v104, v109, v108, s96
v_perm_b32 v105, v111, v110, s96
v_perm_b32 v106, v109, v108, s97
v_perm_b32 v107, v111, v110, s97
v_pk_mad_u16 v108, v108, 2, -1 op_sel_hi:[1,0,0]
v_pk_mad_u16 v109, v109, 2, -1 op_sel_hi:[1,0,0]
v_pk_mad_u16 v110, v110, 2, -1 op_sel_hi:[1,0,0]
v_pk_mad_u16 v111, v111, 2, -1 op_sel_hi:[1,0,0]
v_and_b32 v112, 0x7f7f7f7f, v104
v_and_b32 v113, 0x7f7f7f7f, v105
v_sub_u32 v112, s80, v112
v_sub_u32 v113, s80, v113
v_pk_min_u16 v108, v108, v109
v_pk_min_u16 v110, v110, v111
v_pk_min_u16 v108, v108, v110
v_bitop3_b32 v109, v112, v113, s98 bitop3:0xa8
v_pk_sub_u16 v108, s81, v108 clamp
v_cmp_ne_u32_e64 vcc, v108, v109
v_perm_b32 v112, s99, v115, v112
v_perm_b32 v113, s99, v115, v113
v_bitop3_b32 v112, v112, v104, s98 bitop3:0x8d
v_bitop3_b32 v113, v113, v105, s98 bitop3:0x8d
v_alignbit_b32 v113, v113, v113, 28
v_or_b32 v114, v112, v113
s_getpc_b64 s[92:93]
s_cbranch_vccnz Lexc
v_accvgpr_write_b32 a254, v114
v_accvgpr_write_b32 a255, v106
v_accvgpr_write_b32 a0, v107
s_waitcnt vmcnt(15)
s_branch LP25_%=b
LP25_%=:
s_sleep 1
LP25_%=b:
s_memrealtime vcc
s_waitcnt lgkmcnt(0)
s_sub_u32 %[so], vcc_lo, %[t0]
s_cmp_lt_u32 %[so], 1635
s_cbranch_scc1 LP25_%=
s_cmp_lt_u32 %[so], 1715
s_cbranch_scc0 LP25_%=f
s_waitcnt vmcnt(10)
s_cmp_lt_u32 %[so], 1665
s_cbranch_scc0 LP25_%=f
s_waitcnt vmcnt(6)
s_nop 0
LP25_%=f:
s_mov_b32 %[so], 106496
buffer_load_dwordx4 v[180:183], %[ko], %[rk], %[so] offen offset:0 nt
v_mfma_f32_16x16x32_bf16 v[40:43], v[120:123], v[52:55], v[40:43]
v_mfma_f32_16x16x32_bf16 v[44:47], v[124:127], v[52:55], v[44:47]
v_mfma_f32_16x16x32_bf16 v[48:51], v[128:131], v[52:55], v[48:51]
s_movk_i32 s94, 4160
s_nop 0
v_accvgpr_read_b32 v108, a128
v_accvgpr_read_b32 v109, a129
v_accvgpr_read_b32 v110, a130
v_accvgpr_read_b32 v111, a131
v_perm_b32 v104, v109, v108, s96
v_perm_b32 v105, v111, v110, s96
v_perm_b32 v106, v109, v108, s97
v_perm_b32 v107, v111, v110, s97
v_pk_mad_u16 v108, v108, 2, -1 op_sel_hi:[1,0,0]
v_pk_mad_u16 v109, v109, 2, -1 op_sel_hi:[1,0,0]
v_pk_mad_u16 v110, v110, 2, -1 op_sel_hi:[1,0,0]
v_pk_mad_u16 v111, v111, 2, -1 op_sel_hi:[1,0,0]
v_and_b32 v112, 0x7f7f7f7f, v104
v_and_b32 v113, 0x7f7f7f7f, v105
v_sub_u32 v112, s80, v112
v_sub_u32 v113, s80, v113
v_pk_min_u16 v108, v108, v109
v_pk_min_u16 v110, v110, v111
v_pk_min_u16 v108, v108, v110
v_bitop3_b32 v109, v112, v113, s98 bitop3:0xa8
v_pk_sub_u16 v108, s81, v108 clamp
)" R"(
v_cmp_ne_u32_e64 vcc, v108, v109
v_perm_b32 v112, s99, v115, v112
v_perm_b32 v113, s99, v115, v113
v_bitop3_b32 v112, v112, v104, s98 bitop3:0x8d
v_bitop3_b32 v113, v113, v105, s98 bitop3:0x8d
v_alignbit_b32 v113, v113, v113, 28
v_or_b32 v114, v112, v113
s_getpc_b64 s[92:93]
s_cbranch_vccnz Lexc
v_accvgpr_write_b32 a1, v114
v_accvgpr_write_b32 a2, v106
v_accvgpr_write_b32 a3, v107
s_waitcnt vmcnt(15)
buffer_load_dwordx4 v[184:187], %[ko], %[rk], %[so] offen offset:1024 nt
s_mov_b32 vcc_lo, 0xf000f000
s_mov_b32 vcc_hi, 0xf000f000
v_cndmask_b32 v52, 0, %[q0], vcc
v_cndmask_b32 v53, 0, %[q1], vcc
v_cndmask_b32 v54, 0, %[q2], vcc
v_cndmask_b32 v55, 0, %[q3], vcc
s_nop 1
v_mfma_f32_16x16x32_bf16 v[36:39], v[132:135], v[52:55], v[36:39]
s_movk_i32 s94, 4224
v_accvgpr_read_b32 v108, a132
v_accvgpr_read_b32 v109, a133
v_accvgpr_read_b32 v110, a134
v_accvgpr_read_b32 v111, a135
v_perm_b32 v104, v109, v108, s96
v_perm_b32 v105, v111, v110, s96
v_perm_b32 v106, v109, v108, s97
v_perm_b32 v107, v111, v110, s97
v_pk_mad_u16 v108, v108, 2, -1 op_sel_hi:[1,0,0]
v_pk_mad_u16 v109, v109, 2, -1 op_sel_hi:[1,0,0]
v_pk_mad_u16 v110, v110, 2, -1 op_sel_hi:[1,0,0]
v_pk_mad_u16 v111, v111, 2, -1 op_sel_hi:[1,0,0]
v_and_b32 v112, 0x7f7f7f7f, v104
v_and_b32 v113, 0x7f7f7f7f, v105
v_sub_u32 v112, s80, v112
v_sub_u32 v113, s80, v113
v_pk_min_u16 v108, v108, v109
v_pk_min_u16 v110, v110, v111
v_pk_min_u16 v108, v108, v110
v_bitop3_b32 v109, v112, v113, s98 bitop3:0xa8
v_pk_sub_u16 v108, s81, v108 clamp
v_cmp_ne_u32_e64 vcc, v108, v109
v_perm_b32 v112, s99, v115, v112
v_perm_b32 v113, s99, v115, v113
v_bitop3_b32 v112, v112, v104, s98 bitop3:0x8d
v_bitop3_b32 v113, v113, v105, s98 bitop3:0x8d
v_alignbit_b32 v113, v113, v113, 28
v_or_b32 v114, v112, v113
s_getpc_b64 s[92:93]
s_cbranch_vccnz Lexc
v_accvgpr_write_b32 a4, v114
v_accvgpr_write_b32 a5, v106
v_accvgpr_write_b32 a6, v107
s_waitcnt vmcnt(15)
s_mov_b32 %[so], 11008
buffer_load_dword v97, %[go], %[ru], %[so] offen offset:0
s_waitcnt vmcnt(15)
s_mov_b32 %[so], 106496
buffer_load_dwordx4 v[188:191], %[ko], %[rk], %[so] offen offset:2048 nt
v_mfma_f32_16x16x32_bf16 v[40:43], v[136:139], v[52:55], v[40:43]
v_mfma_f32_16x16x32_bf16 v[44:47], v[140:143], v[52:55], v[44:47]
s_movk_i32 s94, 4288
v_accvgpr_read_b32 v108, a136
v_accvgpr_read_b32 v109, a137
v_accvgpr_read_b32 v110, a138
v_accvgpr_read_b32 v111, a139
v_perm_b32 v104, v109, v108, s96
v_perm_b32 v105, v111, v110, s96
v_perm_b32 v106, v109, v108, s97
v_perm_b32 v107, v111, v110, s97
v_pk_mad_u16 v108, v108, 2, -1 op_sel_hi:[1,0,0]
v_pk_mad_u16 v109, v109, 2, -1 op_sel_hi:[1,0,0]
v_pk_mad_u16 v110, v110, 2, -1 op_sel_hi:[1,0,0]
v_pk_mad_u16 v111, v111, 2, -1 op_sel_hi:[1,0,0]
v_and_b32 v112, 0x7f7f7f7f, v104
v_and_b32 v113, 0x7f7f7f7f, v105
v_sub_u32 v112, s80, v112
v_sub_u32 v113, s80, v113
v_pk_min_u16 v108, v108, v109
v_pk_min_u16 v110, v110, v111
v_pk_min_u16 v108, v108, v110
v_bitop3_b32 v109, v112, v113, s98 bitop3:0xa8
v_pk_sub_u16 v108, s81, v108 clamp
v_cmp_ne_u32_e64 vcc, v108, v109
v_perm_b32 v112, s99, v115, v112
v_perm_b32 v113, s99, v115, v113
v_bitop3_b32 v112, v112, v104, s98 bitop3:0x8d
v_bitop3_b32 v113, v113, v105, s98 bitop3:0x8d
v_alignbit_b32 v113, v113, v113, 28
v_or_b32 v114, v112, v113
s_getpc_b64 s[92:93]
s_cbranch_vccnz Lexc
v_accvgpr_write_b32 a7, v114
v_accvgpr_write_b32 a8, v106
v_accvgpr_write_b32 a9, v107
s_waitcnt vmcnt(15)
buffer_load_dwordx4 v[192:195], %[ko], %[rk], %[so] offen offset:3072 nt
v_mfma_f32_16x16x32_bf16 v[48:51], v[144:147], v[52:55], v[48:51]
s_movk_i32 s94, 4352
s_nop 0
v_accvgpr_read_b32 v108, a140
v_accvgpr_read_b32 v109, a141
v_accvgpr_read_b32 v110, a142
v_accvgpr_read_b32 v111, a143
v_perm_b32 v104, v109, v108, s96
v_perm_b32 v105, v111, v110, s96
v_perm_b32 v106, v109, v108, s97
v_perm_b32 v107, v111, v110, s97
v_pk_mad_u16 v108, v108, 2, -1 op_sel_hi:[1,0,0]
v_pk_mad_u16 v109, v109, 2, -1 op_sel_hi:[1,0,0]
v_pk_mad_u16 v110, v110, 2, -1 op_sel_hi:[1,0,0]
v_pk_mad_u16 v111, v111, 2, -1 op_sel_hi:[1,0,0]
v_and_b32 v112, 0x7f7f7f7f, v104
v_and_b32 v113, 0x7f7f7f7f, v105
v_sub_u32 v112, s80, v112
v_sub_u32 v113, s80, v113
v_pk_min_u16 v108, v108, v109
v_pk_min_u16 v110, v110, v111
v_pk_min_u16 v108, v108, v110
v_bitop3_b32 v109, v112, v113, s98 bitop3:0xa8
v_pk_sub_u16 v108, s81, v108 clamp
v_cmp_ne_u32_e64 vcc, v108, v109
v_perm_b32 v112, s99, v115, v112
v_perm_b32 v113, s99, v115, v113
v_bitop3_b32 v112, v112, v104, s98 bitop3:0x8d
v_bitop3_b32 v113, v113, v105, s98 bitop3:0x8d
v_alignbit_b32 v113, v113, v113, 28
v_or_b32 v114, v112, v113
s_getpc_b64 s[92:93]
s_cbranch_vccnz Lexc
s_nop 0
v_accvgpr_write_b32 a10, v114
v_accvgpr_write_b32 a11, v106
v_accvgpr_write_b32 a12, v107
v_cndmask_b32 v36, v36, v38, %[m2]
v_cndmask_b32 v37, v37, v39, %[m2]
v_cndmask_b32 v40, v40, v42, %[m2]
v_cndmask_b32 v41, v41, v43, %[m2]
v_cndmask_b32 v44, v44, v46, %[m2]
v_cndmask_b32 v45, v45, v47, %[m2]
v_cndmask_b32 v48, v48, v50, %[m2]
v_cndmask_b32 v49, v49, v51, %[m2]
v_cndmask_b32 v38, v40, v37, %[m1]
v_cndmask_b32 v39, v36, v41, %[m1]
v_cndmask_b32 v46, v48, v45, %[m1]
v_cndmask_b32 v47, v44, v49, %[m1]
v_add_f32_dpp v39, v38, v39 quad_perm:[1,0,3,2] row_mask:0xf bank_mask:0xf
v_add_f32_dpp v47, v46, v47 quad_perm:[1,0,3,2] row_mask:0xf bank_mask:0xf
v_cndmask_b32 v40, v47, v39, %[m2]
v_cndmask_b32 v41, v39, v47, %[m2]
s_nop 0
v_add_f32_dpp %[sc5], v40, v41 quad_perm:[2,3,0,1] row_mask:0xf bank_mask:0xf
s_waitcnt vmcnt(15)
s_branch LP26_%=b
LP26_%=:
s_sleep 1
LP26_%=b:
s_memrealtime vcc
s_waitcnt lgkmcnt(0)
s_sub_u32 %[so], vcc_lo, %[t0]
s_cmp_lt_u32 %[so], 1697
s_cbranch_scc1 LP26_%=
s_cmp_lt_u32 %[so], 1777
s_cbranch_scc0 LP26_%=f
s_waitcnt vmcnt(10)
s_cmp_lt_u32 %[so], 1727
s_cbranch_scc0 LP26_%=f
s_waitcnt vmcnt(6)
LP26_%=f:
s_mov_b32 %[so], 110592
buffer_load_dwordx4 v[196:199], %[ko], %[rk], %[so] offen offset:0 nt
s_movk_i32 s94, 4416
v_accvgpr_read_b32 v108, a144
v_accvgpr_read_b32 v109, a145
v_accvgpr_read_b32 v110, a146
v_accvgpr_read_b32 v111, a147
v_perm_b32 v104, v109, v108, s96
v_perm_b32 v105, v111, v110, s96
v_perm_b32 v106, v109, v108, s97
v_perm_b32 v107, v111, v110, s97
v_pk_mad_u16 v108, v108, 2, -1 op_sel_hi:[1,0,0]
v_pk_mad_u16 v109, v109, 2, -1 op_sel_hi:[1,0,0]
v_pk_mad_u16 v110, v110, 2, -1 op_sel_hi:[1,0,0]
v_pk_mad_u16 v111, v111, 2, -1 op_sel_hi:[1,0,0]
v_and_b32 v112, 0x7f7f7f7f, v104
v_and_b32 v113, 0x7f7f7f7f, v105
v_sub_u32 v112, s80, v112
v_sub_u32 v113, s80, v113
v_pk_min_u16 v108, v108, v109
v_pk_min_u16 v110, v110, v111
v_pk_min_u16 v108, v108, v110
v_bitop3_b32 v109, v112, v113, s98 bitop3:0xa8
v_pk_sub_u16 v108, s81, v108 clamp
v_cmp_ne_u32_e64 vcc, v108, v109
v_perm_b32 v112, s99, v115, v112
v_perm_b32 v113, s99, v115, v113
v_bitop3_b32 v112, v112, v104, s98 bitop3:0x8d
v_bitop3_b32 v113, v113, v105, s98 bitop3:0x8d
v_alignbit_b32 v113, v113, v113, 28
v_or_b32 v114, v112, v113
s_getpc_b64 s[92:93]
s_cbranch_vccnz Lexc
v_accvgpr_write_b32 a13, v114
v_accvgpr_write_b32 a14, v106
v_accvgpr_write_b32 a15, v107
s_waitcnt vmcnt(15)
buffer_load_dwordx4 v[200:203], %[ko], %[rk], %[so] offen offset:1024 nt
s_mov_b32 vcc_lo, 0x000f000f
s_mov_b32 vcc_hi, 0x000f000f
v_cndmask_b32 v52, 0, %[q0], vcc
v_cndmask_b32 v53, 0, %[q1], vcc
v_cndmask_b32 v54, 0, %[q2], vcc
v_cndmask_b32 v55, 0, %[q3], vcc
s_nop 1
v_mfma_f32_16x16x32_bf16 v[36:39], v[148:151], v[52:55], 0
s_movk_i32 s94, 4480
v_accvgpr_read_b32 v108, a148
v_accvgpr_read_b32 v109, a149
v_accvgpr_read_b32 v110, a150
v_accvgpr_read_b32 v111, a151
v_perm_b32 v104, v109, v108, s96
v_perm_b32 v105, v111, v110, s96
v_perm_b32 v106, v109, v108, s97
v_perm_b32 v107, v111, v110, s97
v_pk_mad_u16 v108, v108, 2, -1 op_sel_hi:[1,0,0]
v_pk_mad_u16 v109, v109, 2, -1 op_sel_hi:[1,0,0]
v_pk_mad_u16 v110, v110, 2, -1 op_sel_hi:[1,0,0]
v_pk_mad_u16 v111, v111, 2, -1 op_sel_hi:[1,0,0]
v_and_b32 v112, 0x7f7f7f7f, v104
v_and_b32 v113, 0x7f7f7f7f, v105
v_sub_u32 v112, s80, v112
v_sub_u32 v113, s80, v113
v_pk_min_u16 v108, v108, v109
v_pk_min_u16 v110, v110, v111
v_pk_min_u16 v108, v108, v110
v_bitop3_b32 v109, v112, v113, s98 bitop3:0xa8
v_pk_sub_u16 v108, s81, v108 clamp
v_cmp_ne_u32_e64 vcc, v108, v109
v_perm_b32 v112, s99, v115, v112
v_perm_b32 v113, s99, v115, v113
v_bitop3_b32 v112, v112, v104, s98 bitop3:0x8d
v_bitop3_b32 v113, v113, v105, s98 bitop3:0x8d
v_alignbit_b32 v113, v113, v113, 28
v_or_b32 v114, v112, v113
s_getpc_b64 s[92:93]
s_cbranch_vccnz Lexc
v_accvgpr_write_b32 a16, v114
v_accvgpr_write_b32 a17, v106
v_accvgpr_write_b32 a18, v107
s_waitcnt vmcnt(15)
s_mov_b32 %[so], 11008
buffer_load_dword v93, %[go], %[rg], %[so] offen offset:2752
s_waitcnt vmcnt(15)
s_mov_b32 %[so], 110592
buffer_load_dwordx4 v[204:207], %[ko], %[rk], %[so] offen offset:2048 nt
v_mfma_f32_16x16x32_bf16 v[40:43], v[152:155], v[52:55], 0
v_mfma_f32_16x16x32_bf16 v[44:47], v[156:159], v[52:55], 0
s_movk_i32 s94, 4544
v_accvgpr_read_b32 v108, a152
v_accvgpr_read_b32 v109, a153
v_accvgpr_read_b32 v110, a154
v_accvgpr_read_b32 v111, a155
v_perm_b32 v104, v109, v108, s96
v_perm_b32 v105, v111, v110, s96
v_perm_b32 v106, v109, v108, s97
v_perm_b32 v107, v111, v110, s97
v_pk_mad_u16 v108, v108, 2, -1 op_sel_hi:[1,0,0]
v_pk_mad_u16 v109, v109, 2, -1 op_sel_hi:[1,0,0]
v_pk_mad_u16 v110, v110, 2, -1 op_sel_hi:[1,0,0]
v_pk_mad_u16 v111, v111, 2, -1 op_sel_hi:[1,0,0]
v_and_b32 v112, 0x7f7f7f7f, v104
v_and_b32 v113, 0x7f7f7f7f, v105
v_sub_u32 v112, s80, v112
v_sub_u32 v113, s80, v113
v_pk_min_u16 v108, v108, v109
v_pk_min_u16 v110, v110, v111
v_pk_min_u16 v108, v108, v110
v_bitop3_b32 v109, v112, v113, s98 bitop3:0xa8
v_pk_sub_u16 v108, s81, v108 clamp
v_cmp_ne_u32_e64 vcc, v108, v109
v_perm_b32 v112, s99, v115, v112
v_perm_b32 v113, s99, v115, v113
v_bitop3_b32 v112, v112, v104, s98 bitop3:0x8d
v_bitop3_b32 v113, v113, v105, s98 bitop3:0x8d
v_alignbit_b32 v113, v113, v113, 28
v_or_b32 v114, v112, v113
s_getpc_b64 s[92:93]
s_cbranch_vccnz Lexc
v_accvgpr_write_b32 a19, v114
v_accvgpr_write_b32 a20, v106
v_accvgpr_write_b32 a21, v107
s_waitcnt vmcnt(15)
buffer_load_dwordx4 v[208:211], %[ko], %[rk], %[so] offen offset:3072 nt
s_movk_i32 s94, 4608
s_nop 0
v_accvgpr_read_b32 v108, a156
v_accvgpr_read_b32 v109, a157
v_accvgpr_read_b32 v110, a158
v_accvgpr_read_b32 v111, a159
v_perm_b32 v104, v109, v108, s96
v_perm_b32 v105, v111, v110, s96
v_perm_b32 v106, v109, v108, s97
v_perm_b32 v107, v111, v110, s97
v_pk_mad_u16 v108, v108, 2, -1 op_sel_hi:[1,0,0]
v_pk_mad_u16 v109, v109, 2, -1 op_sel_hi:[1,0,0]
v_pk_mad_u16 v110, v110, 2, -1 op_sel_hi:[1,0,0]
v_pk_mad_u16 v111, v111, 2, -1 op_sel_hi:[1,0,0]
v_and_b32 v112, 0x7f7f7f7f, v104
v_and_b32 v113, 0x7f7f7f7f, v105
v_sub_u32 v112, s80, v112
v_sub_u32 v113, s80, v113
v_pk_min_u16 v108, v108, v109
v_pk_min_u16 v110, v110, v111
v_pk_min_u16 v108, v108, v110
v_bitop3_b32 v109, v112, v113, s98 bitop3:0xa8
v_pk_sub_u16 v108, s81, v108 clamp
v_cmp_ne_u32_e64 vcc, v108, v109
v_perm_b32 v112, s99, v115, v112
v_perm_b32 v113, s99, v115, v113
v_bitop3_b32 v112, v112, v104, s98 bitop3:0x8d
v_bitop3_b32 v113, v113, v105, s98 bitop3:0x8d
v_alignbit_b32 v113, v113, v113, 28
v_or_b32 v114, v112, v113
s_getpc_b64 s[92:93]
s_cbranch_vccnz Lexc
v_accvgpr_write_b32 a22, v114
v_accvgpr_write_b32 a23, v106
v_accvgpr_write_b32 a24, v107
s_waitcnt vmcnt(15)
s_branch LP27_%=b
LP27_%=:
s_sleep 1
LP27_%=b:
s_memrealtime vcc
s_waitcnt lgkmcnt(0)
s_sub_u32 %[so], vcc_lo, %[t0]
s_cmp_lt_u32 %[so], 1760
s_cbranch_scc1 LP27_%=
s_cmp_lt_u32 %[so], 1840
s_cbranch_scc0 LP27_%=f
s_waitcnt vmcnt(10)
s_cmp_lt_u32 %[so], 1790
s_cbranch_scc0 LP27_%=f
s_waitcnt vmcnt(6)
LP27_%=f:
s_mov_b32 %[so], 114688
buffer_load_dwordx4 v[212:215], %[ko], %[rk], %[so] offen offset:0 nt
v_mfma_f32_16x16x32_bf16 v[48:51], v[160:163], v[52:55], 0
s_movk_i32 s94, 4672
v_accvgpr_read_b32 v108, a160
v_accvgpr_read_b32 v109, a161
v_accvgpr_read_b32 v110, a162
v_accvgpr_read_b32 v111, a163
v_perm_b32 v104, v109, v108, s96
v_perm_b32 v105, v111, v110, s96
v_perm_b32 v106, v109, v108, s97
v_perm_b32 v107, v111, v110, s97
v_pk_mad_u16 v108, v108, 2, -1 op_sel_hi:[1,0,0]
)" R"(
v_pk_mad_u16 v109, v109, 2, -1 op_sel_hi:[1,0,0]
v_pk_mad_u16 v110, v110, 2, -1 op_sel_hi:[1,0,0]
v_pk_mad_u16 v111, v111, 2, -1 op_sel_hi:[1,0,0]
v_and_b32 v112, 0x7f7f7f7f, v104
v_and_b32 v113, 0x7f7f7f7f, v105
v_sub_u32 v112, s80, v112
v_sub_u32 v113, s80, v113
v_pk_min_u16 v108, v108, v109
v_pk_min_u16 v110, v110, v111
v_pk_min_u16 v108, v108, v110
v_bitop3_b32 v109, v112, v113, s98 bitop3:0xa8
v_pk_sub_u16 v108, s81, v108 clamp
v_cmp_ne_u32_e64 vcc, v108, v109
v_perm_b32 v112, s99, v115, v112
v_perm_b32 v113, s99, v115, v113
v_bitop3_b32 v112, v112, v104, s98 bitop3:0x8d
v_bitop3_b32 v113, v113, v105, s98 bitop3:0x8d
v_alignbit_b32 v113, v113, v113, 28
v_or_b32 v114, v112, v113
s_getpc_b64 s[92:93]
s_cbranch_vccnz Lexc
v_accvgpr_write_b32 a25, v114
v_accvgpr_write_b32 a26, v106
v_accvgpr_write_b32 a27, v107
s_waitcnt vmcnt(15)
s_mov_b32 %[so], 11008
buffer_load_dword v101, %[go], %[ru], %[so] offen offset:2752
s_waitcnt vmcnt(15)
s_mov_b32 %[so], 114688
buffer_load_dwordx4 v[216:219], %[ko], %[rk], %[so] offen offset:1024 nt
s_mov_b32 vcc_lo, 0x00f000f0
s_mov_b32 vcc_hi, 0x00f000f0
v_cndmask_b32 v52, 0, %[q0], vcc
v_cndmask_b32 v53, 0, %[q1], vcc
v_cndmask_b32 v54, 0, %[q2], vcc
v_cndmask_b32 v55, 0, %[q3], vcc
s_nop 1
v_mfma_f32_16x16x32_bf16 v[36:39], v[164:167], v[52:55], v[36:39]
v_mfma_f32_16x16x32_bf16 v[40:43], v[168:171], v[52:55], v[40:43]
s_movk_i32 s94, 4736
s_nop 0
v_accvgpr_read_b32 v108, a164
v_accvgpr_read_b32 v109, a165
v_accvgpr_read_b32 v110, a166
v_accvgpr_read_b32 v111, a167
v_perm_b32 v104, v109, v108, s96
v_perm_b32 v105, v111, v110, s96
v_perm_b32 v106, v109, v108, s97
v_perm_b32 v107, v111, v110, s97
v_pk_mad_u16 v108, v108, 2, -1 op_sel_hi:[1,0,0]
v_pk_mad_u16 v109, v109, 2, -1 op_sel_hi:[1,0,0]
v_pk_mad_u16 v110, v110, 2, -1 op_sel_hi:[1,0,0]
v_pk_mad_u16 v111, v111, 2, -1 op_sel_hi:[1,0,0]
v_and_b32 v112, 0x7f7f7f7f, v104
v_and_b32 v113, 0x7f7f7f7f, v105
v_sub_u32 v112, s80, v112
v_sub_u32 v113, s80, v113
v_pk_min_u16 v108, v108, v109
v_pk_min_u16 v110, v110, v111
v_pk_min_u16 v108, v108, v110
v_bitop3_b32 v109, v112, v113, s98 bitop3:0xa8
v_pk_sub_u16 v108, s81, v108 clamp
v_cmp_ne_u32_e64 vcc, v108, v109
v_perm_b32 v112, s99, v115, v112
v_perm_b32 v113, s99, v115, v113
v_bitop3_b32 v112, v112, v104, s98 bitop3:0x8d
v_bitop3_b32 v113, v113, v105, s98 bitop3:0x8d
v_alignbit_b32 v113, v113, v113, 28
v_or_b32 v114, v112, v113
s_getpc_b64 s[92:93]
s_cbranch_vccnz Lexc
v_accvgpr_write_b32 a28, v114
v_accvgpr_write_b32 a29, v106
v_accvgpr_write_b32 a30, v107
s_waitcnt vmcnt(15)
buffer_load_dwordx4 v[220:223], %[ko], %[rk], %[so] offen offset:2048 nt
v_mfma_f32_16x16x32_bf16 v[44:47], v[172:175], v[52:55], v[44:47]
s_movk_i32 s94, 4800
s_nop 0
v_accvgpr_read_b32 v108, a168
v_accvgpr_read_b32 v109, a169
v_accvgpr_read_b32 v110, a170
v_accvgpr_read_b32 v111, a171
v_perm_b32 v104, v109, v108, s96
v_perm_b32 v105, v111, v110, s96
v_perm_b32 v106, v109, v108, s97
v_perm_b32 v107, v111, v110, s97
v_pk_mad_u16 v108, v108, 2, -1 op_sel_hi:[1,0,0]
v_pk_mad_u16 v109, v109, 2, -1 op_sel_hi:[1,0,0]
v_pk_mad_u16 v110, v110, 2, -1 op_sel_hi:[1,0,0]
v_pk_mad_u16 v111, v111, 2, -1 op_sel_hi:[1,0,0]
v_and_b32 v112, 0x7f7f7f7f, v104
v_and_b32 v113, 0x7f7f7f7f, v105
v_sub_u32 v112, s80, v112
v_sub_u32 v113, s80, v113
v_pk_min_u16 v108, v108, v109
v_pk_min_u16 v110, v110, v111
v_pk_min_u16 v108, v108, v110
v_bitop3_b32 v109, v112, v113, s98 bitop3:0xa8
v_pk_sub_u16 v108, s81, v108 clamp
v_cmp_ne_u32_e64 vcc, v108, v109
v_perm_b32 v112, s99, v115, v112
v_perm_b32 v113, s99, v115, v113
v_bitop3_b32 v112, v112, v104, s98 bitop3:0x8d
v_bitop3_b32 v113, v113, v105, s98 bitop3:0x8d
v_alignbit_b32 v113, v113, v113, 28
v_or_b32 v114, v112, v113
s_getpc_b64 s[92:93]
s_cbranch_vccnz Lexc
v_accvgpr_write_b32 a31, v114
v_accvgpr_write_b32 a32, v106
v_accvgpr_write_b32 a33, v107
s_waitcnt vmcnt(15)
buffer_load_dwordx4 v[224:227], %[ko], %[rk], %[so] offen offset:3072 nt
s_movk_i32 s94, 4864
s_nop 0
v_accvgpr_read_b32 v108, a172
v_accvgpr_read_b32 v109, a173
v_accvgpr_read_b32 v110, a174
v_accvgpr_read_b32 v111, a175
v_perm_b32 v104, v109, v108, s96
v_perm_b32 v105, v111, v110, s96
v_perm_b32 v106, v109, v108, s97
v_perm_b32 v107, v111, v110, s97
v_pk_mad_u16 v108, v108, 2, -1 op_sel_hi:[1,0,0]
v_pk_mad_u16 v109, v109, 2, -1 op_sel_hi:[1,0,0]
v_pk_mad_u16 v110, v110, 2, -1 op_sel_hi:[1,0,0]
v_pk_mad_u16 v111, v111, 2, -1 op_sel_hi:[1,0,0]
v_and_b32 v112, 0x7f7f7f7f, v104
v_and_b32 v113, 0x7f7f7f7f, v105
v_sub_u32 v112, s80, v112
v_sub_u32 v113, s80, v113
v_pk_min_u16 v108, v108, v109
v_pk_min_u16 v110, v110, v111
v_pk_min_u16 v108, v108, v110
v_bitop3_b32 v109, v112, v113, s98 bitop3:0xa8
v_pk_sub_u16 v108, s81, v108 clamp
v_cmp_ne_u32_e64 vcc, v108, v109
v_perm_b32 v112, s99, v115, v112
v_perm_b32 v113, s99, v115, v113
v_bitop3_b32 v112, v112, v104, s98 bitop3:0x8d
v_bitop3_b32 v113, v113, v105, s98 bitop3:0x8d
v_alignbit_b32 v113, v113, v113, 28
v_or_b32 v114, v112, v113
s_getpc_b64 s[92:93]
s_cbranch_vccnz Lexc
v_accvgpr_write_b32 a34, v114
v_accvgpr_write_b32 a35, v106
v_accvgpr_write_b32 a36, v107
s_waitcnt vmcnt(15)
s_branch LP28_%=b
LP28_%=:
s_sleep 1
LP28_%=b:
s_memrealtime vcc
s_waitcnt lgkmcnt(0)
s_sub_u32 %[so], vcc_lo, %[t0]
s_cmp_lt_u32 %[so], 1822
s_cbranch_scc1 LP28_%=
s_cmp_lt_u32 %[so], 1902
s_cbranch_scc0 LP28_%=f
s_waitcnt vmcnt(10)
s_cmp_lt_u32 %[so], 1852
s_cbranch_scc0 LP28_%=f
s_waitcnt vmcnt(6)
LP28_%=f:
s_mov_b32 %[so], 118784
buffer_load_dwordx4 v[228:231], %[ko], %[rk], %[so] offen offset:0 nt
v_mfma_f32_16x16x32_bf16 v[48:51], v[176:179], v[52:55], v[48:51]
s_movk_i32 s94, 4928
v_accvgpr_read_b32 v108, a176
v_accvgpr_read_b32 v109, a177
v_accvgpr_read_b32 v110, a178
v_accvgpr_read_b32 v111, a179
v_perm_b32 v104, v109, v108, s96
v_perm_b32 v105, v111, v110, s96
v_perm_b32 v106, v109, v108, s97
v_perm_b32 v107, v111, v110, s97
v_pk_mad_u16 v108, v108, 2, -1 op_sel_hi:[1,0,0]
v_pk_mad_u16 v109, v109, 2, -1 op_sel_hi:[1,0,0]
v_pk_mad_u16 v110, v110, 2, -1 op_sel_hi:[1,0,0]
v_pk_mad_u16 v111, v111, 2, -1 op_sel_hi:[1,0,0]
v_and_b32 v112, 0x7f7f7f7f, v104
v_and_b32 v113, 0x7f7f7f7f, v105
v_sub_u32 v112, s80, v112
v_sub_u32 v113, s80, v113
v_pk_min_u16 v108, v108, v109
v_pk_min_u16 v110, v110, v111
v_pk_min_u16 v108, v108, v110
v_bitop3_b32 v109, v112, v113, s98 bitop3:0xa8
v_pk_sub_u16 v108, s81, v108 clamp
v_cmp_ne_u32_e64 vcc, v108, v109
v_perm_b32 v112, s99, v115, v112
v_perm_b32 v113, s99, v115, v113
v_bitop3_b32 v112, v112, v104, s98 bitop3:0x8d
v_bitop3_b32 v113, v113, v105, s98 bitop3:0x8d
v_alignbit_b32 v113, v113, v113, 28
v_or_b32 v114, v112, v113
s_getpc_b64 s[92:93]
s_cbranch_vccnz Lexc
v_accvgpr_write_b32 a37, v114
v_accvgpr_write_b32 a38, v106
v_accvgpr_write_b32 a39, v107
s_waitcnt vmcnt(15)
s_mov_b32 %[so], 22016
buffer_load_dword v90, %[go], %[rg], %[so] offen offset:0
s_waitcnt vmcnt(15)
s_mov_b32 %[so], 118784
buffer_load_dwordx4 v[232:235], %[ko], %[rk], %[so] offen offset:1024 nt
s_mov_b32 vcc_lo, 0x0f000f00
s_mov_b32 vcc_hi, 0x0f000f00
v_cndmask_b32 v52, 0, %[q0], vcc
v_cndmask_b32 v53, 0, %[q1], vcc
v_cndmask_b32 v54, 0, %[q2], vcc
v_cndmask_b32 v55, 0, %[q3], vcc
s_nop 1
v_mfma_f32_16x16x32_bf16 v[36:39], v[180:183], v[52:55], v[36:39]
v_mfma_f32_16x16x32_bf16 v[40:43], v[184:187], v[52:55], v[40:43]
s_movk_i32 s94, 4992
s_nop 0
v_accvgpr_read_b32 v108, a180
v_accvgpr_read_b32 v109, a181
v_accvgpr_read_b32 v110, a182
v_accvgpr_read_b32 v111, a183
v_perm_b32 v104, v109, v108, s96
v_perm_b32 v105, v111, v110, s96
v_perm_b32 v106, v109, v108, s97
v_perm_b32 v107, v111, v110, s97
v_pk_mad_u16 v108, v108, 2, -1 op_sel_hi:[1,0,0]
v_pk_mad_u16 v109, v109, 2, -1 op_sel_hi:[1,0,0]
v_pk_mad_u16 v110, v110, 2, -1 op_sel_hi:[1,0,0]
v_pk_mad_u16 v111, v111, 2, -1 op_sel_hi:[1,0,0]
v_and_b32 v112, 0x7f7f7f7f, v104
v_and_b32 v113, 0x7f7f7f7f, v105
v_sub_u32 v112, s80, v112
v_sub_u32 v113, s80, v113
v_pk_min_u16 v108, v108, v109
v_pk_min_u16 v110, v110, v111
v_pk_min_u16 v108, v108, v110
v_bitop3_b32 v109, v112, v113, s98 bitop3:0xa8
v_pk_sub_u16 v108, s81, v108 clamp
v_cmp_ne_u32_e64 vcc, v108, v109
v_perm_b32 v112, s99, v115, v112
v_perm_b32 v113, s99, v115, v113
v_bitop3_b32 v112, v112, v104, s98 bitop3:0x8d
v_bitop3_b32 v113, v113, v105, s98 bitop3:0x8d
v_alignbit_b32 v113, v113, v113, 28
v_or_b32 v114, v112, v113
s_getpc_b64 s[92:93]
s_cbranch_vccnz Lexc
v_accvgpr_write_b32 a40, v114
v_accvgpr_write_b32 a41, v106
v_accvgpr_write_b32 a42, v107
s_waitcnt vmcnt(15)
buffer_load_dwordx4 v[236:239], %[ko], %[rk], %[so] offen offset:2048 nt
s_movk_i32 s94, 5056
s_nop 0
v_accvgpr_read_b32 v108, a184
v_accvgpr_read_b32 v109, a185
v_accvgpr_read_b32 v110, a186
v_accvgpr_read_b32 v111, a187
v_perm_b32 v104, v109, v108, s96
v_perm_b32 v105, v111, v110, s96
v_perm_b32 v106, v109, v108, s97
v_perm_b32 v107, v111, v110, s97
v_pk_mad_u16 v108, v108, 2, -1 op_sel_hi:[1,0,0]
v_pk_mad_u16 v109, v109, 2, -1 op_sel_hi:[1,0,0]
v_pk_mad_u16 v110, v110, 2, -1 op_sel_hi:[1,0,0]
v_pk_mad_u16 v111, v111, 2, -1 op_sel_hi:[1,0,0]
v_and_b32 v112, 0x7f7f7f7f, v104
v_and_b32 v113, 0x7f7f7f7f, v105
v_sub_u32 v112, s80, v112
v_sub_u32 v113, s80, v113
v_pk_min_u16 v108, v108, v109
v_pk_min_u16 v110, v110, v111
v_pk_min_u16 v108, v108, v110
v_bitop3_b32 v109, v112, v113, s98 bitop3:0xa8
v_pk_sub_u16 v108, s81, v108 clamp
v_cmp_ne_u32_e64 vcc, v108, v109
v_perm_b32 v112, s99, v115, v112
v_perm_b32 v113, s99, v115, v113
v_bitop3_b32 v112, v112, v104, s98 bitop3:0x8d
v_bitop3_b32 v113, v113, v105, s98 bitop3:0x8d
v_alignbit_b32 v113, v113, v113, 28
v_or_b32 v114, v112, v113
s_getpc_b64 s[92:93]
s_cbranch_vccnz Lexc
v_accvgpr_write_b32 a43, v114
v_accvgpr_write_b32 a44, v106
v_accvgpr_write_b32 a45, v107
s_waitcnt vmcnt(15)
buffer_load_dwordx4 v[240:243], %[ko], %[rk], %[so] offen offset:3072 nt
v_mfma_f32_16x16x32_bf16 v[44:47], v[188:191], v[52:55], v[44:47]
s_movk_i32 s94, 5120
s_nop 0
v_accvgpr_read_b32 v108, a188
v_accvgpr_read_b32 v109, a189
v_accvgpr_read_b32 v110, a190
v_accvgpr_read_b32 v111, a191
v_perm_b32 v104, v109, v108, s96
v_perm_b32 v105, v111, v110, s96
v_perm_b32 v106, v109, v108, s97
v_perm_b32 v107, v111, v110, s97
v_pk_mad_u16 v108, v108, 2, -1 op_sel_hi:[1,0,0]
v_pk_mad_u16 v109, v109, 2, -1 op_sel_hi:[1,0,0]
v_pk_mad_u16 v110, v110, 2, -1 op_sel_hi:[1,0,0]
v_pk_mad_u16 v111, v111, 2, -1 op_sel_hi:[1,0,0]
v_and_b32 v112, 0x7f7f7f7f, v104
v_and_b32 v113, 0x7f7f7f7f, v105
v_sub_u32 v112, s80, v112
v_sub_u32 v113, s80, v113
v_pk_min_u16 v108, v108, v109
v_pk_min_u16 v110, v110, v111
v_pk_min_u16 v108, v108, v110
v_bitop3_b32 v109, v112, v113, s98 bitop3:0xa8
v_pk_sub_u16 v108, s81, v108 clamp
v_cmp_ne_u32_e64 vcc, v108, v109
v_perm_b32 v112, s99, v115, v112
v_perm_b32 v113, s99, v115, v113
v_bitop3_b32 v112, v112, v104, s98 bitop3:0x8d
v_bitop3_b32 v113, v113, v105, s98 bitop3:0x8d
v_alignbit_b32 v113, v113, v113, 28
v_or_b32 v114, v112, v113
s_getpc_b64 s[92:93]
s_cbranch_vccnz Lexc
v_accvgpr_write_b32 a46, v114
v_accvgpr_write_b32 a47, v106
v_accvgpr_write_b32 a48, v107
s_waitcnt vmcnt(15)
s_mov_b32 %[so], 22016
buffer_load_dword v98, %[go], %[ru], %[so] offen offset:0
s_waitcnt vmcnt(15)
s_branch LP29_%=b
LP29_%=:
s_sleep 1
LP29_%=b:
s_memrealtime vcc
s_waitcnt lgkmcnt(0)
s_sub_u32 %[so], vcc_lo, %[t0]
s_cmp_lt_u32 %[so], 1884
s_cbranch_scc1 LP29_%=
s_cmp_lt_u32 %[so], 1964
s_cbranch_scc0 LP29_%=f
s_waitcnt vmcnt(10)
s_cmp_lt_u32 %[so], 1914
s_cbranch_scc0 LP29_%=f
s_waitcnt vmcnt(6)
LP29_%=f:
s_mov_b32 %[so], 122880
buffer_load_dwordx4 v[244:247], %[ko], %[rk], %[so] offen offset:0 nt
v_mfma_f32_16x16x32_bf16 v[48:51], v[192:195], v[52:55], v[48:51]
s_mov_b32 vcc_lo, 0xf000f000
s_mov_b32 vcc_hi, 0xf000f000
v_cndmask_b32 v52, 0, %[q0], vcc
v_cndmask_b32 v53, 0, %[q1], vcc
v_cndmask_b32 v54, 0, %[q2], vcc
v_cndmask_b32 v55, 0, %[q3], vcc
s_nop 1
v_mfma_f32_16x16x32_bf16 v[36:39], v[196:199], v[52:55], v[36:39]
s_movk_i32 s94, 5184
v_accvgpr_read_b32 v108, a192
v_accvgpr_read_b32 v109, a193
v_accvgpr_read_b32 v110, a194
v_accvgpr_read_b32 v111, a195
v_perm_b32 v104, v109, v108, s96
)" R"(
v_perm_b32 v105, v111, v110, s96
v_perm_b32 v106, v109, v108, s97
v_perm_b32 v107, v111, v110, s97
v_pk_mad_u16 v108, v108, 2, -1 op_sel_hi:[1,0,0]
v_pk_mad_u16 v109, v109, 2, -1 op_sel_hi:[1,0,0]
v_pk_mad_u16 v110, v110, 2, -1 op_sel_hi:[1,0,0]
v_pk_mad_u16 v111, v111, 2, -1 op_sel_hi:[1,0,0]
v_and_b32 v112, 0x7f7f7f7f, v104
v_and_b32 v113, 0x7f7f7f7f, v105
v_sub_u32 v112, s80, v112
v_sub_u32 v113, s80, v113
v_pk_min_u16 v108, v108, v109
v_pk_min_u16 v110, v110, v111
v_pk_min_u16 v108, v108, v110
v_bitop3_b32 v109, v112, v113, s98 bitop3:0xa8
v_pk_sub_u16 v108, s81, v108 clamp
v_cmp_ne_u32_e64 vcc, v108, v109
v_perm_b32 v112, s99, v115, v112
v_perm_b32 v113, s99, v115, v113
v_bitop3_b32 v112, v112, v104, s98 bitop3:0x8d
v_bitop3_b32 v113, v113, v105, s98 bitop3:0x8d
v_alignbit_b32 v113, v113, v113, 28
v_or_b32 v114, v112, v113
s_getpc_b64 s[92:93]
s_cbranch_vccnz Lexc
v_accvgpr_write_b32 a49, v114
v_accvgpr_write_b32 a50, v106
v_accvgpr_write_b32 a51, v107
s_waitcnt vmcnt(15)
buffer_load_dwordx4 a[52:55], %[ko], %[rk], %[so] offen offset:1024 nt
v_mfma_f32_16x16x32_bf16 v[40:43], v[200:203], v[52:55], v[40:43]
s_movk_i32 s94, 5248
s_nop 0
v_accvgpr_read_b32 v108, a196
v_accvgpr_read_b32 v109, a197
v_accvgpr_read_b32 v110, a198
v_accvgpr_read_b32 v111, a199
v_perm_b32 v104, v109, v108, s96
v_perm_b32 v105, v111, v110, s96
v_perm_b32 v106, v109, v108, s97
v_perm_b32 v107, v111, v110, s97
v_pk_mad_u16 v108, v108, 2, -1 op_sel_hi:[1,0,0]
v_pk_mad_u16 v109, v109, 2, -1 op_sel_hi:[1,0,0]
v_pk_mad_u16 v110, v110, 2, -1 op_sel_hi:[1,0,0]
v_pk_mad_u16 v111, v111, 2, -1 op_sel_hi:[1,0,0]
v_and_b32 v112, 0x7f7f7f7f, v104
v_and_b32 v113, 0x7f7f7f7f, v105
v_sub_u32 v112, s80, v112
v_sub_u32 v113, s80, v113
v_pk_min_u16 v108, v108, v109
v_pk_min_u16 v110, v110, v111
v_pk_min_u16 v108, v108, v110
v_bitop3_b32 v109, v112, v113, s98 bitop3:0xa8
v_pk_sub_u16 v108, s81, v108 clamp
v_cmp_ne_u32_e64 vcc, v108, v109
v_perm_b32 v112, s99, v115, v112
v_perm_b32 v113, s99, v115, v113
v_bitop3_b32 v112, v112, v104, s98 bitop3:0x8d
v_bitop3_b32 v113, v113, v105, s98 bitop3:0x8d
v_alignbit_b32 v113, v113, v113, 28
v_or_b32 v114, v112, v113
s_getpc_b64 s[92:93]
s_cbranch_vccnz Lexc
v_accvgpr_write_b32 a56, v114
v_accvgpr_write_b32 a57, v106
v_accvgpr_write_b32 a58, v107
s_movk_i32 s94, 5312
v_accvgpr_read_b32 v108, a200
v_accvgpr_read_b32 v109, a201
v_accvgpr_read_b32 v110, a202
v_accvgpr_read_b32 v111, a203
v_perm_b32 v104, v109, v108, s96
v_perm_b32 v105, v111, v110, s96
v_perm_b32 v106, v109, v108, s97
v_perm_b32 v107, v111, v110, s97
v_pk_mad_u16 v108, v108, 2, -1 op_sel_hi:[1,0,0]
v_pk_mad_u16 v109, v109, 2, -1 op_sel_hi:[1,0,0]
v_pk_mad_u16 v110, v110, 2, -1 op_sel_hi:[1,0,0]
v_pk_mad_u16 v111, v111, 2, -1 op_sel_hi:[1,0,0]
v_and_b32 v112, 0x7f7f7f7f, v104
v_and_b32 v113, 0x7f7f7f7f, v105
v_sub_u32 v112, s80, v112
v_sub_u32 v113, s80, v113
v_pk_min_u16 v108, v108, v109
v_pk_min_u16 v110, v110, v111
v_pk_min_u16 v108, v108, v110
v_bitop3_b32 v109, v112, v113, s98 bitop3:0xa8
v_pk_sub_u16 v108, s81, v108 clamp
v_cmp_ne_u32_e64 vcc, v108, v109
v_perm_b32 v112, s99, v115, v112
v_perm_b32 v113, s99, v115, v113
v_bitop3_b32 v112, v112, v104, s98 bitop3:0x8d
v_bitop3_b32 v113, v113, v105, s98 bitop3:0x8d
v_alignbit_b32 v113, v113, v113, 28
v_or_b32 v114, v112, v113
s_getpc_b64 s[92:93]
s_cbranch_vccnz Lexc
v_accvgpr_write_b32 a59, v114
v_accvgpr_write_b32 a60, v106
v_accvgpr_write_b32 a61, v107
s_waitcnt vmcnt(15)
buffer_load_dwordx4 a[64:67], %[ko], %[rk], %[so] offen offset:2048 nt
s_movk_i32 s94, 5376
s_nop 0
v_accvgpr_read_b32 v108, a204
v_accvgpr_read_b32 v109, a205
v_accvgpr_read_b32 v110, a206
v_accvgpr_read_b32 v111, a207
v_perm_b32 v104, v109, v108, s96
v_perm_b32 v105, v111, v110, s96
v_perm_b32 v106, v109, v108, s97
v_perm_b32 v107, v111, v110, s97
v_pk_mad_u16 v108, v108, 2, -1 op_sel_hi:[1,0,0]
v_pk_mad_u16 v109, v109, 2, -1 op_sel_hi:[1,0,0]
v_pk_mad_u16 v110, v110, 2, -1 op_sel_hi:[1,0,0]
v_pk_mad_u16 v111, v111, 2, -1 op_sel_hi:[1,0,0]
v_and_b32 v112, 0x7f7f7f7f, v104
v_and_b32 v113, 0x7f7f7f7f, v105
v_sub_u32 v112, s80, v112
v_sub_u32 v113, s80, v113
v_pk_min_u16 v108, v108, v109
v_pk_min_u16 v110, v110, v111
v_pk_min_u16 v108, v108, v110
v_bitop3_b32 v109, v112, v113, s98 bitop3:0xa8
v_pk_sub_u16 v108, s81, v108 clamp
v_cmp_ne_u32_e64 vcc, v108, v109
v_perm_b32 v112, s99, v115, v112
v_perm_b32 v113, s99, v115, v113
v_bitop3_b32 v112, v112, v104, s98 bitop3:0x8d
v_bitop3_b32 v113, v113, v105, s98 bitop3:0x8d
v_alignbit_b32 v113, v113, v113, 28
v_or_b32 v114, v112, v113
s_getpc_b64 s[92:93]
s_cbranch_vccnz Lexc
v_accvgpr_write_b32 a62, v114
v_accvgpr_write_b32 a63, v106
v_accvgpr_write_b32 a68, v107
s_waitcnt vmcnt(15)
buffer_load_dwordx4 a[72:75], %[ko], %[rk], %[so] offen offset:3072 nt
v_mfma_f32_16x16x32_bf16 v[44:47], v[204:207], v[52:55], v[44:47]
s_movk_i32 s94, 5440
s_nop 0
v_accvgpr_read_b32 v108, a208
v_accvgpr_read_b32 v109, a209
v_accvgpr_read_b32 v110, a210
v_accvgpr_read_b32 v111, a211
v_perm_b32 v104, v109, v108, s96
v_perm_b32 v105, v111, v110, s96
v_perm_b32 v106, v109, v108, s97
v_perm_b32 v107, v111, v110, s97
v_pk_mad_u16 v108, v108, 2, -1 op_sel_hi:[1,0,0]
v_pk_mad_u16 v109, v109, 2, -1 op_sel_hi:[1,0,0]
v_pk_mad_u16 v110, v110, 2, -1 op_sel_hi:[1,0,0]
v_pk_mad_u16 v111, v111, 2, -1 op_sel_hi:[1,0,0]
v_and_b32 v112, 0x7f7f7f7f, v104
v_and_b32 v113, 0x7f7f7f7f, v105
v_sub_u32 v112, s80, v112
v_sub_u32 v113, s80, v113
v_pk_min_u16 v108, v108, v109
v_pk_min_u16 v110, v110, v111
v_pk_min_u16 v108, v108, v110
v_bitop3_b32 v109, v112, v113, s98 bitop3:0xa8
v_pk_sub_u16 v108, s81, v108 clamp
v_cmp_ne_u32_e64 vcc, v108, v109
v_perm_b32 v112, s99, v115, v112
v_perm_b32 v113, s99, v115, v113
v_bitop3_b32 v112, v112, v104, s98 bitop3:0x8d
v_bitop3_b32 v113, v113, v105, s98 bitop3:0x8d
v_alignbit_b32 v113, v113, v113, 28
v_or_b32 v114, v112, v113
s_getpc_b64 s[92:93]
s_cbranch_vccnz Lexc
v_accvgpr_write_b32 a69, v114
v_accvgpr_write_b32 a70, v106
v_accvgpr_write_b32 a71, v107
s_waitcnt vmcnt(15)
s_mov_b32 %[so], 22016
buffer_load_dword v94, %[go], %[rg], %[so] offen offset:2752
s_waitcnt vmcnt(15)
s_branch LP30_%=b
LP30_%=:
s_sleep 1
LP30_%=b:
s_memrealtime vcc
s_waitcnt lgkmcnt(0)
s_sub_u32 %[so], vcc_lo, %[t0]
s_cmp_lt_u32 %[so], 1947
s_cbranch_scc1 LP30_%=
s_cmp_lt_u32 %[so], 2027
s_cbranch_scc0 LP30_%=f
s_waitcnt vmcnt(10)
s_cmp_lt_u32 %[so], 1977
s_cbranch_scc0 LP30_%=f
s_waitcnt vmcnt(6)
LP30_%=f:
s_mov_b32 %[so], 126976
buffer_load_dwordx4 a[76:79], %[ko], %[rk], %[so] offen offset:0 nt
v_mfma_f32_16x16x32_bf16 v[48:51], v[208:211], v[52:55], v[48:51]
s_movk_i32 s94, 5504
s_nop 0
v_perm_b32 v104, v253, v252, s96
v_perm_b32 v105, v255, v254, s96
v_perm_b32 v106, v253, v252, s97
v_perm_b32 v107, v255, v254, s97
v_pk_mad_u16 v108, v252, 2, -1 op_sel_hi:[1,0,0]
v_pk_mad_u16 v109, v253, 2, -1 op_sel_hi:[1,0,0]
v_pk_mad_u16 v110, v254, 2, -1 op_sel_hi:[1,0,0]
v_pk_mad_u16 v111, v255, 2, -1 op_sel_hi:[1,0,0]
v_and_b32 v112, 0x7f7f7f7f, v104
v_and_b32 v113, 0x7f7f7f7f, v105
v_sub_u32 v112, s80, v112
v_sub_u32 v113, s80, v113
v_pk_min_u16 v108, v108, v109
v_pk_min_u16 v110, v110, v111
v_pk_min_u16 v108, v108, v110
v_bitop3_b32 v109, v112, v113, s98 bitop3:0xa8
v_pk_sub_u16 v108, s81, v108 clamp
v_cmp_ne_u32_e64 vcc, v108, v109
v_perm_b32 v112, s99, v115, v112
v_perm_b32 v113, s99, v115, v113
v_bitop3_b32 v112, v112, v104, s98 bitop3:0x8d
v_bitop3_b32 v113, v113, v105, s98 bitop3:0x8d
v_alignbit_b32 v113, v113, v113, 28
v_or_b32 v114, v112, v113
s_getpc_b64 s[92:93]
s_cbranch_vccnz Lexc
v_mov_b32 v252, v114
v_mov_b32 v253, v106
v_mov_b32 v254, v107
v_cndmask_b32 v36, v36, v38, %[m2]
v_cndmask_b32 v37, v37, v39, %[m2]
v_cndmask_b32 v40, v40, v42, %[m2]
v_cndmask_b32 v41, v41, v43, %[m2]
v_cndmask_b32 v44, v44, v46, %[m2]
v_cndmask_b32 v45, v45, v47, %[m2]
v_cndmask_b32 v48, v48, v50, %[m2]
v_cndmask_b32 v49, v49, v51, %[m2]
v_cndmask_b32 v38, v40, v37, %[m1]
v_cndmask_b32 v39, v36, v41, %[m1]
v_cndmask_b32 v46, v48, v45, %[m1]
v_cndmask_b32 v47, v44, v49, %[m1]
v_add_f32_dpp v39, v38, v39 quad_perm:[1,0,3,2] row_mask:0xf bank_mask:0xf
v_add_f32_dpp v47, v46, v47 quad_perm:[1,0,3,2] row_mask:0xf bank_mask:0xf
v_cndmask_b32 v40, v47, v39, %[m2]
v_cndmask_b32 v41, v39, v47, %[m2]
s_nop 0
v_add_f32_dpp %[sc6], v40, v41 quad_perm:[2,3,0,1] row_mask:0xf bank_mask:0xf
s_waitcnt vmcnt(15)
buffer_load_dwordx4 a[80:83], %[ko], %[rk], %[so] offen offset:1024 nt
s_mov_b32 vcc_lo, 0x000f000f
s_mov_b32 vcc_hi, 0x000f000f
v_cndmask_b32 v52, 0, %[q0], vcc
v_cndmask_b32 v53, 0, %[q1], vcc
v_cndmask_b32 v54, 0, %[q2], vcc
v_cndmask_b32 v55, 0, %[q3], vcc
s_nop 1
v_mfma_f32_16x16x32_bf16 v[36:39], v[212:215], v[52:55], 0
s_movk_i32 s94, 5568
v_perm_b32 v104, v249, v248, s96
v_perm_b32 v105, v251, v250, s96
v_perm_b32 v106, v249, v248, s97
v_perm_b32 v107, v251, v250, s97
v_pk_mad_u16 v108, v248, 2, -1 op_sel_hi:[1,0,0]
v_pk_mad_u16 v109, v249, 2, -1 op_sel_hi:[1,0,0]
v_pk_mad_u16 v110, v250, 2, -1 op_sel_hi:[1,0,0]
v_pk_mad_u16 v111, v251, 2, -1 op_sel_hi:[1,0,0]
v_and_b32 v112, 0x7f7f7f7f, v104
v_and_b32 v113, 0x7f7f7f7f, v105
v_sub_u32 v112, s80, v112
v_sub_u32 v113, s80, v113
v_pk_min_u16 v108, v108, v109
v_pk_min_u16 v110, v110, v111
v_pk_min_u16 v108, v108, v110
v_bitop3_b32 v109, v112, v113, s98 bitop3:0xa8
v_pk_sub_u16 v108, s81, v108 clamp
v_cmp_ne_u32_e64 vcc, v108, v109
v_perm_b32 v112, s99, v115, v112
v_perm_b32 v113, s99, v115, v113
v_bitop3_b32 v112, v112, v104, s98 bitop3:0x8d
v_bitop3_b32 v113, v113, v105, s98 bitop3:0x8d
v_alignbit_b32 v113, v113, v113, 28
v_or_b32 v114, v112, v113
s_getpc_b64 s[92:93]
s_cbranch_vccnz Lexc
v_mov_b32 v255, v114
v_mov_b32 v248, v106
v_mov_b32 v249, v107
s_waitcnt vmcnt(15)
buffer_load_dwordx4 a[84:87], %[ko], %[rk], %[so] offen offset:2048 nt
v_mfma_f32_16x16x32_bf16 v[40:43], v[216:219], v[52:55], 0
s_movk_i32 s94, 5632
v_perm_b32 v104, v117, v116, s96
v_perm_b32 v105, v119, v118, s96
v_perm_b32 v106, v117, v116, s97
v_perm_b32 v107, v119, v118, s97
v_pk_mad_u16 v108, v116, 2, -1 op_sel_hi:[1,0,0]
v_pk_mad_u16 v109, v117, 2, -1 op_sel_hi:[1,0,0]
v_pk_mad_u16 v110, v118, 2, -1 op_sel_hi:[1,0,0]
v_pk_mad_u16 v111, v119, 2, -1 op_sel_hi:[1,0,0]
v_and_b32 v112, 0x7f7f7f7f, v104
v_and_b32 v113, 0x7f7f7f7f, v105
v_sub_u32 v112, s80, v112
v_sub_u32 v113, s80, v113
v_pk_min_u16 v108, v108, v109
v_pk_min_u16 v110, v110, v111
v_pk_min_u16 v108, v108, v110
v_bitop3_b32 v109, v112, v113, s98 bitop3:0xa8
v_pk_sub_u16 v108, s81, v108 clamp
v_cmp_ne_u32_e64 vcc, v108, v109
v_perm_b32 v112, s99, v115, v112
v_perm_b32 v113, s99, v115, v113
v_bitop3_b32 v112, v112, v104, s98 bitop3:0x8d
v_bitop3_b32 v113, v113, v105, s98 bitop3:0x8d
v_alignbit_b32 v113, v113, v113, 28
v_or_b32 v114, v112, v113
s_getpc_b64 s[92:93]
s_cbranch_vccnz Lexc
v_mov_b32 v250, v114
v_mov_b32 v251, v106
v_mov_b32 v116, v107
s_waitcnt vmcnt(15)
s_mov_b32 %[so], 22016
buffer_load_dword v102, %[go], %[ru], %[so] offen offset:2752
s_waitcnt vmcnt(15)
s_mov_b32 %[so], 126976
buffer_load_dwordx4 a[88:91], %[ko], %[rk], %[so] offen offset:3072 nt
v_mfma_f32_16x16x32_bf16 v[44:47], v[220:223], v[52:55], 0
v_mfma_f32_16x16x32_bf16 v[48:51], v[224:227], v[52:55], 0
s_movk_i32 s94, 5696
s_nop 0
v_perm_b32 v104, v121, v120, s96
v_perm_b32 v105, v123, v122, s96
v_perm_b32 v106, v121, v120, s97
v_perm_b32 v107, v123, v122, s97
v_pk_mad_u16 v108, v120, 2, -1 op_sel_hi:[1,0,0]
v_pk_mad_u16 v109, v121, 2, -1 op_sel_hi:[1,0,0]
v_pk_mad_u16 v110, v122, 2, -1 op_sel_hi:[1,0,0]
v_pk_mad_u16 v111, v123, 2, -1 op_sel_hi:[1,0,0]
v_and_b32 v112, 0x7f7f7f7f, v104
v_and_b32 v113, 0x7f7f7f7f, v105
v_sub_u32 v112, s80, v112
v_sub_u32 v113, s80, v113
v_pk_min_u16 v108, v108, v109
v_pk_min_u16 v110, v110, v111
v_pk_min_u16 v108, v108, v110
v_bitop3_b32 v109, v112, v113, s98 bitop3:0xa8
v_pk_sub_u16 v108, s81, v108 clamp
v_cmp_ne_u32_e64 vcc, v108, v109
v_perm_b32 v112, s99, v115, v112
v_perm_b32 v113, s99, v115, v113
v_bitop3_b32 v112, v112, v104, s98 bitop3:0x8d
v_bitop3_b32 v113, v113, v105, s98 bitop3:0x8d
v_alignbit_b32 v113, v113, v113, 28
)" R"(
v_or_b32 v114, v112, v113
s_getpc_b64 s[92:93]
s_cbranch_vccnz Lexc
v_mov_b32 v117, v114
v_mov_b32 v118, v106
v_mov_b32 v119, v107
s_waitcnt vmcnt(15)
s_branch LP31_%=b
LP31_%=:
s_sleep 1
LP31_%=b:
s_memrealtime vcc
s_waitcnt lgkmcnt(0)
s_sub_u32 %[so], vcc_lo, %[t0]
s_cmp_lt_u32 %[so], 2009
s_cbranch_scc1 LP31_%=
s_cmp_lt_u32 %[so], 2089
s_cbranch_scc0 LP31_%=f
s_waitcnt vmcnt(8)
s_cmp_lt_u32 %[so], 2039
s_cbranch_scc0 LP31_%=f
s_waitcnt vmcnt(4)
LP31_%=f:
buffer_load_dwordx4 a[92:95], %[vo], %[rv], 0 offen offset:0
s_mov_b32 vcc_lo, 0x00f000f0
s_mov_b32 vcc_hi, 0x00f000f0
v_cndmask_b32 v52, 0, %[q0], vcc
v_cndmask_b32 v53, 0, %[q1], vcc
v_cndmask_b32 v54, 0, %[q2], vcc
v_cndmask_b32 v55, 0, %[q3], vcc
s_nop 1
v_mfma_f32_16x16x32_bf16 v[36:39], v[228:231], v[52:55], v[36:39]
s_movk_i32 s94, 5760
v_perm_b32 v104, v125, v124, s96
v_perm_b32 v105, v127, v126, s96
v_perm_b32 v106, v125, v124, s97
v_perm_b32 v107, v127, v126, s97
v_pk_mad_u16 v108, v124, 2, -1 op_sel_hi:[1,0,0]
v_pk_mad_u16 v109, v125, 2, -1 op_sel_hi:[1,0,0]
v_pk_mad_u16 v110, v126, 2, -1 op_sel_hi:[1,0,0]
v_pk_mad_u16 v111, v127, 2, -1 op_sel_hi:[1,0,0]
v_and_b32 v112, 0x7f7f7f7f, v104
v_and_b32 v113, 0x7f7f7f7f, v105
v_sub_u32 v112, s80, v112
v_sub_u32 v113, s80, v113
v_pk_min_u16 v108, v108, v109
v_pk_min_u16 v110, v110, v111
v_pk_min_u16 v108, v108, v110
v_bitop3_b32 v109, v112, v113, s98 bitop3:0xa8
v_pk_sub_u16 v108, s81, v108 clamp
v_cmp_ne_u32_e64 vcc, v108, v109
v_perm_b32 v112, s99, v115, v112
v_perm_b32 v113, s99, v115, v113
v_bitop3_b32 v112, v112, v104, s98 bitop3:0x8d
v_bitop3_b32 v113, v113, v105, s98 bitop3:0x8d
v_alignbit_b32 v113, v113, v113, 28
v_or_b32 v114, v112, v113
s_getpc_b64 s[92:93]
s_cbranch_vccnz Lexc
v_mov_b32 v120, v114
v_mov_b32 v121, v106
v_mov_b32 v122, v107
s_waitcnt vmcnt(15)
buffer_load_dwordx4 a[96:99], %[vo], %[rv], 0 offen offset:1024
s_movk_i32 s94, 5824
v_perm_b32 v104, v129, v128, s96
v_perm_b32 v105, v131, v130, s96
v_perm_b32 v106, v129, v128, s97
v_perm_b32 v107, v131, v130, s97
v_pk_mad_u16 v108, v128, 2, -1 op_sel_hi:[1,0,0]
v_pk_mad_u16 v109, v129, 2, -1 op_sel_hi:[1,0,0]
v_pk_mad_u16 v110, v130, 2, -1 op_sel_hi:[1,0,0]
v_pk_mad_u16 v111, v131, 2, -1 op_sel_hi:[1,0,0]
v_and_b32 v112, 0x7f7f7f7f, v104
v_and_b32 v113, 0x7f7f7f7f, v105
v_sub_u32 v112, s80, v112
v_sub_u32 v113, s80, v113
v_pk_min_u16 v108, v108, v109
v_pk_min_u16 v110, v110, v111
v_pk_min_u16 v108, v108, v110
v_bitop3_b32 v109, v112, v113, s98 bitop3:0xa8
v_pk_sub_u16 v108, s81, v108 clamp
v_cmp_ne_u32_e64 vcc, v108, v109
v_perm_b32 v112, s99, v115, v112
v_perm_b32 v113, s99, v115, v113
v_bitop3_b32 v112, v112, v104, s98 bitop3:0x8d
v_bitop3_b32 v113, v113, v105, s98 bitop3:0x8d
v_alignbit_b32 v113, v113, v113, 28
v_or_b32 v114, v112, v113
s_getpc_b64 s[92:93]
s_cbranch_vccnz Lexc
v_mov_b32 v123, v114
v_mov_b32 v124, v106
v_mov_b32 v125, v107
s_waitcnt vmcnt(15)
buffer_load_dwordx4 a[100:103], %[vo], %[rv], 0 offen offset:2048
v_mfma_f32_16x16x32_bf16 v[40:43], v[232:235], v[52:55], v[40:43]
s_movk_i32 s94, 5888
v_perm_b32 v104, v133, v132, s96
v_perm_b32 v105, v135, v134, s96
v_perm_b32 v106, v133, v132, s97
v_perm_b32 v107, v135, v134, s97
v_pk_mad_u16 v108, v132, 2, -1 op_sel_hi:[1,0,0]
v_pk_mad_u16 v109, v133, 2, -1 op_sel_hi:[1,0,0]
v_pk_mad_u16 v110, v134, 2, -1 op_sel_hi:[1,0,0]
v_pk_mad_u16 v111, v135, 2, -1 op_sel_hi:[1,0,0]
v_and_b32 v112, 0x7f7f7f7f, v104
v_and_b32 v113, 0x7f7f7f7f, v105
v_sub_u32 v112, s80, v112
v_sub_u32 v113, s80, v113
v_pk_min_u16 v108, v108, v109
v_pk_min_u16 v110, v110, v111
v_pk_min_u16 v108, v108, v110
v_bitop3_b32 v109, v112, v113, s98 bitop3:0xa8
v_pk_sub_u16 v108, s81, v108 clamp
v_cmp_ne_u32_e64 vcc, v108, v109
v_perm_b32 v112, s99, v115, v112
v_perm_b32 v113, s99, v115, v113
v_bitop3_b32 v112, v112, v104, s98 bitop3:0x8d
v_bitop3_b32 v113, v113, v105, s98 bitop3:0x8d
v_alignbit_b32 v113, v113, v113, 28
v_or_b32 v114, v112, v113
s_getpc_b64 s[92:93]
s_cbranch_vccnz Lexc
v_mov_b32 v126, v114
v_mov_b32 v127, v106
v_mov_b32 v128, v107
s_waitcnt vmcnt(15)
s_mov_b32 %[so], 33024
buffer_load_dword v91, %[go], %[rg], %[so] offen offset:0
s_waitcnt vmcnt(15)
buffer_load_dwordx4 a[104:107], %[vo], %[rv], 0 offen offset:3072
v_mfma_f32_16x16x32_bf16 v[44:47], v[236:239], v[52:55], v[44:47]
v_mfma_f32_16x16x32_bf16 v[48:51], v[240:243], v[52:55], v[48:51]
s_movk_i32 s94, 5952
s_nop 0
v_perm_b32 v104, v137, v136, s96
v_perm_b32 v105, v139, v138, s96
v_perm_b32 v106, v137, v136, s97
v_perm_b32 v107, v139, v138, s97
v_pk_mad_u16 v108, v136, 2, -1 op_sel_hi:[1,0,0]
v_pk_mad_u16 v109, v137, 2, -1 op_sel_hi:[1,0,0]
v_pk_mad_u16 v110, v138, 2, -1 op_sel_hi:[1,0,0]
v_pk_mad_u16 v111, v139, 2, -1 op_sel_hi:[1,0,0]
v_and_b32 v112, 0x7f7f7f7f, v104
v_and_b32 v113, 0x7f7f7f7f, v105
v_sub_u32 v112, s80, v112
v_sub_u32 v113, s80, v113
v_pk_min_u16 v108, v108, v109
v_pk_min_u16 v110, v110, v111
v_pk_min_u16 v108, v108, v110
v_bitop3_b32 v109, v112, v113, s98 bitop3:0xa8
v_pk_sub_u16 v108, s81, v108 clamp
v_cmp_ne_u32_e64 vcc, v108, v109
v_perm_b32 v112, s99, v115, v112
v_perm_b32 v113, s99, v115, v113
v_bitop3_b32 v112, v112, v104, s98 bitop3:0x8d
v_bitop3_b32 v113, v113, v105, s98 bitop3:0x8d
v_alignbit_b32 v113, v113, v113, 28
v_or_b32 v114, v112, v113
s_getpc_b64 s[92:93]
s_cbranch_vccnz Lexc
v_mov_b32 v129, v114
v_mov_b32 v130, v106
v_mov_b32 v131, v107
s_waitcnt vmcnt(15)
s_branch LP32_%=b
LP32_%=:
s_sleep 1
LP32_%=b:
s_memrealtime vcc
s_waitcnt lgkmcnt(0)
s_sub_u32 %[so], vcc_lo, %[t0]
s_cmp_lt_u32 %[so], 2076
s_cbranch_scc1 LP32_%=
s_cmp_lt_u32 %[so], 2156
s_cbranch_scc0 LP32_%=f
s_waitcnt vmcnt(8)
s_cmp_lt_u32 %[so], 2106
s_cbranch_scc0 LP32_%=f
s_waitcnt vmcnt(4)
LP32_%=f:
s_mov_b32 %[so], 4096
buffer_load_dwordx4 a[108:111], %[vo], %[rv], %[so] offen offset:0
s_movk_i32 s94, 6016
s_nop 0
v_perm_b32 v104, v141, v140, s96
v_perm_b32 v105, v143, v142, s96
v_perm_b32 v106, v141, v140, s97
v_perm_b32 v107, v143, v142, s97
v_pk_mad_u16 v108, v140, 2, -1 op_sel_hi:[1,0,0]
v_pk_mad_u16 v109, v141, 2, -1 op_sel_hi:[1,0,0]
v_pk_mad_u16 v110, v142, 2, -1 op_sel_hi:[1,0,0]
v_pk_mad_u16 v111, v143, 2, -1 op_sel_hi:[1,0,0]
v_and_b32 v112, 0x7f7f7f7f, v104
v_and_b32 v113, 0x7f7f7f7f, v105
v_sub_u32 v112, s80, v112
v_sub_u32 v113, s80, v113
v_pk_min_u16 v108, v108, v109
v_pk_min_u16 v110, v110, v111
v_pk_min_u16 v108, v108, v110
v_bitop3_b32 v109, v112, v113, s98 bitop3:0xa8
v_pk_sub_u16 v108, s81, v108 clamp
v_cmp_ne_u32_e64 vcc, v108, v109
v_perm_b32 v112, s99, v115, v112
v_perm_b32 v113, s99, v115, v113
v_bitop3_b32 v112, v112, v104, s98 bitop3:0x8d
v_bitop3_b32 v113, v113, v105, s98 bitop3:0x8d
v_alignbit_b32 v113, v113, v113, 28
v_or_b32 v114, v112, v113
s_getpc_b64 s[92:93]
s_cbranch_vccnz Lexc
v_accvgpr_write_b32 a112, v114
v_accvgpr_write_b32 a113, v106
v_accvgpr_write_b32 a114, v107
s_waitcnt vmcnt(15)
buffer_load_dwordx4 a[116:119], %[vo], %[rv], %[so] offen offset:1024
s_mov_b32 vcc_lo, 0x0f000f00
s_mov_b32 vcc_hi, 0x0f000f00
v_cndmask_b32 v52, 0, %[q0], vcc
v_cndmask_b32 v53, 0, %[q1], vcc
v_cndmask_b32 v54, 0, %[q2], vcc
v_cndmask_b32 v55, 0, %[q3], vcc
s_nop 1
v_mfma_f32_16x16x32_bf16 v[36:39], v[244:247], v[52:55], v[36:39]
s_movk_i32 s94, 6080
v_perm_b32 v104, v145, v144, s96
v_perm_b32 v105, v147, v146, s96
v_perm_b32 v106, v145, v144, s97
v_perm_b32 v107, v147, v146, s97
v_pk_mad_u16 v108, v144, 2, -1 op_sel_hi:[1,0,0]
v_pk_mad_u16 v109, v145, 2, -1 op_sel_hi:[1,0,0]
v_pk_mad_u16 v110, v146, 2, -1 op_sel_hi:[1,0,0]
v_pk_mad_u16 v111, v147, 2, -1 op_sel_hi:[1,0,0]
v_and_b32 v112, 0x7f7f7f7f, v104
v_and_b32 v113, 0x7f7f7f7f, v105
v_sub_u32 v112, s80, v112
v_sub_u32 v113, s80, v113
v_pk_min_u16 v108, v108, v109
v_pk_min_u16 v110, v110, v111
v_pk_min_u16 v108, v108, v110
v_bitop3_b32 v109, v112, v113, s98 bitop3:0xa8
v_pk_sub_u16 v108, s81, v108 clamp
v_cmp_ne_u32_e64 vcc, v108, v109
v_perm_b32 v112, s99, v115, v112
v_perm_b32 v113, s99, v115, v113
v_bitop3_b32 v112, v112, v104, s98 bitop3:0x8d
v_bitop3_b32 v113, v113, v105, s98 bitop3:0x8d
v_alignbit_b32 v113, v113, v113, 28
v_or_b32 v114, v112, v113
s_getpc_b64 s[92:93]
s_cbranch_vccnz Lexc
v_accvgpr_write_b32 a115, v114
v_accvgpr_write_b32 a120, v106
v_accvgpr_write_b32 a121, v107
s_waitcnt vmcnt(15)
s_mov_b32 %[so], 33024
buffer_load_dword v99, %[go], %[ru], %[so] offen offset:0
s_waitcnt vmcnt(15)
s_mov_b32 %[so], 4096
buffer_load_dwordx4 a[124:127], %[vo], %[rv], %[so] offen offset:2048
v_mfma_f32_16x16x32_bf16 v[40:43], a[52:55], v[52:55], v[40:43]
v_mfma_f32_16x16x32_bf16 v[44:47], a[64:67], v[52:55], v[44:47]
s_movk_i32 s94, 6144
v_perm_b32 v104, v149, v148, s96
v_perm_b32 v105, v151, v150, s96
v_perm_b32 v106, v149, v148, s97
v_perm_b32 v107, v151, v150, s97
v_pk_mad_u16 v108, v148, 2, -1 op_sel_hi:[1,0,0]
v_pk_mad_u16 v109, v149, 2, -1 op_sel_hi:[1,0,0]
v_pk_mad_u16 v110, v150, 2, -1 op_sel_hi:[1,0,0]
v_pk_mad_u16 v111, v151, 2, -1 op_sel_hi:[1,0,0]
v_and_b32 v112, 0x7f7f7f7f, v104
v_and_b32 v113, 0x7f7f7f7f, v105
v_sub_u32 v112, s80, v112
v_sub_u32 v113, s80, v113
v_pk_min_u16 v108, v108, v109
v_pk_min_u16 v110, v110, v111
v_pk_min_u16 v108, v108, v110
v_bitop3_b32 v109, v112, v113, s98 bitop3:0xa8
v_pk_sub_u16 v108, s81, v108 clamp
v_cmp_ne_u32_e64 vcc, v108, v109
v_perm_b32 v112, s99, v115, v112
v_perm_b32 v113, s99, v115, v113
v_bitop3_b32 v112, v112, v104, s98 bitop3:0x8d
v_bitop3_b32 v113, v113, v105, s98 bitop3:0x8d
v_alignbit_b32 v113, v113, v113, 28
v_or_b32 v114, v112, v113
s_getpc_b64 s[92:93]
s_cbranch_vccnz Lexc
v_accvgpr_write_b32 a122, v114
v_accvgpr_write_b32 a123, v106
v_accvgpr_write_b32 a128, v107
s_waitcnt vmcnt(15)
buffer_load_dwordx4 a[132:135], %[vo], %[rv], %[so] offen offset:3072
v_mfma_f32_16x16x32_bf16 v[48:51], a[72:75], v[52:55], v[48:51]
s_movk_i32 s94, 6208
s_nop 0
v_perm_b32 v104, v153, v152, s96
v_perm_b32 v105, v155, v154, s96
v_perm_b32 v106, v153, v152, s97
v_perm_b32 v107, v155, v154, s97
v_pk_mad_u16 v108, v152, 2, -1 op_sel_hi:[1,0,0]
v_pk_mad_u16 v109, v153, 2, -1 op_sel_hi:[1,0,0]
v_pk_mad_u16 v110, v154, 2, -1 op_sel_hi:[1,0,0]
v_pk_mad_u16 v111, v155, 2, -1 op_sel_hi:[1,0,0]
v_and_b32 v112, 0x7f7f7f7f, v104
v_and_b32 v113, 0x7f7f7f7f, v105
v_sub_u32 v112, s80, v112
v_sub_u32 v113, s80, v113
v_pk_min_u16 v108, v108, v109
v_pk_min_u16 v110, v110, v111
v_pk_min_u16 v108, v108, v110
v_bitop3_b32 v109, v112, v113, s98 bitop3:0xa8
v_pk_sub_u16 v108, s81, v108 clamp
v_cmp_ne_u32_e64 vcc, v108, v109
v_perm_b32 v112, s99, v115, v112
v_perm_b32 v113, s99, v115, v113
v_bitop3_b32 v112, v112, v104, s98 bitop3:0x8d
v_bitop3_b32 v113, v113, v105, s98 bitop3:0x8d
v_alignbit_b32 v113, v113, v113, 28
v_or_b32 v114, v112, v113
s_getpc_b64 s[92:93]
s_cbranch_vccnz Lexc
v_accvgpr_write_b32 a129, v114
v_accvgpr_write_b32 a130, v106
v_accvgpr_write_b32 a131, v107
s_waitcnt vmcnt(15)
s_branch LP33_%=b
LP33_%=:
s_sleep 1
LP33_%=b:
s_memrealtime vcc
s_waitcnt lgkmcnt(0)
s_sub_u32 %[so], vcc_lo, %[t0]
s_cmp_lt_u32 %[so], 2143
s_cbranch_scc1 LP33_%=
s_cmp_lt_u32 %[so], 2223
s_cbranch_scc0 LP33_%=f
s_waitcnt vmcnt(8)
s_cmp_lt_u32 %[so], 2173
s_cbranch_scc0 LP33_%=f
s_waitcnt vmcnt(4)
LP33_%=f:
s_mov_b32 %[so], 8192
buffer_load_dwordx4 a[136:139], %[vo], %[rv], %[so] offen offset:0
s_movk_i32 s94, 6272
v_perm_b32 v104, v157, v156, s96
v_perm_b32 v105, v159, v158, s96
v_perm_b32 v106, v157, v156, s97
v_perm_b32 v107, v159, v158, s97
v_pk_mad_u16 v108, v156, 2, -1 op_sel_hi:[1,0,0]
v_pk_mad_u16 v109, v157, 2, -1 op_sel_hi:[1,0,0]
v_pk_mad_u16 v110, v158, 2, -1 op_sel_hi:[1,0,0]
v_pk_mad_u16 v111, v159, 2, -1 op_sel_hi:[1,0,0]
v_and_b32 v112, 0x7f7f7f7f, v104
v_and_b32 v113, 0x7f7f7f7f, v105
v_sub_u32 v112, s80, v112
v_sub_u32 v113, s80, v113
v_pk_min_u16 v108, v108, v109
v_pk_min_u16 v110, v110, v111
v_pk_min_u16 v108, v108, v110
v_bitop3_b32 v109, v112, v113, s98 bitop3:0xa8
v_pk_sub_u16 v108, s81, v108 clamp
v_cmp_ne_u32_e64 vcc, v108, v109
v_perm_b32 v112, s99, v115, v112
v_perm_b32 v113, s99, v115, v113
v_bitop3_b32 v112, v112, v104, s98 bitop3:0x8d
v_bitop3_b32 v113, v113, v105, s98 bitop3:0x8d
v_alignbit_b32 v113, v113, v113, 28
)" R"(
v_or_b32 v114, v112, v113
s_getpc_b64 s[92:93]
s_cbranch_vccnz Lexc
v_accvgpr_write_b32 a140, v114
v_accvgpr_write_b32 a141, v106
v_accvgpr_write_b32 a142, v107
s_waitcnt vmcnt(15)
buffer_load_dwordx4 a[144:147], %[vo], %[rv], %[so] offen offset:1024
s_mov_b32 vcc_lo, 0xf000f000
s_mov_b32 vcc_hi, 0xf000f000
v_cndmask_b32 v52, 0, %[q0], vcc
v_cndmask_b32 v53, 0, %[q1], vcc
v_cndmask_b32 v54, 0, %[q2], vcc
v_cndmask_b32 v55, 0, %[q3], vcc
s_nop 1
v_mfma_f32_16x16x32_bf16 v[36:39], a[76:79], v[52:55], v[36:39]
s_movk_i32 s94, 6336
v_perm_b32 v104, v161, v160, s96
v_perm_b32 v105, v163, v162, s96
v_perm_b32 v106, v161, v160, s97
v_perm_b32 v107, v163, v162, s97
v_pk_mad_u16 v108, v160, 2, -1 op_sel_hi:[1,0,0]
v_pk_mad_u16 v109, v161, 2, -1 op_sel_hi:[1,0,0]
v_pk_mad_u16 v110, v162, 2, -1 op_sel_hi:[1,0,0]
v_pk_mad_u16 v111, v163, 2, -1 op_sel_hi:[1,0,0]
v_and_b32 v112, 0x7f7f7f7f, v104
v_and_b32 v113, 0x7f7f7f7f, v105
v_sub_u32 v112, s80, v112
v_sub_u32 v113, s80, v113
v_pk_min_u16 v108, v108, v109
v_pk_min_u16 v110, v110, v111
v_pk_min_u16 v108, v108, v110
v_bitop3_b32 v109, v112, v113, s98 bitop3:0xa8
v_pk_sub_u16 v108, s81, v108 clamp
v_cmp_ne_u32_e64 vcc, v108, v109
v_perm_b32 v112, s99, v115, v112
v_perm_b32 v113, s99, v115, v113
v_bitop3_b32 v112, v112, v104, s98 bitop3:0x8d
v_bitop3_b32 v113, v113, v105, s98 bitop3:0x8d
v_alignbit_b32 v113, v113, v113, 28
v_or_b32 v114, v112, v113
s_getpc_b64 s[92:93]
s_cbranch_vccnz Lexc
v_accvgpr_write_b32 a143, v114
v_accvgpr_write_b32 a148, v106
v_accvgpr_write_b32 a149, v107
s_waitcnt vmcnt(15)
s_mov_b32 %[so], 33024
buffer_load_dword v95, %[go], %[rg], %[so] offen offset:2752
s_waitcnt vmcnt(15)
s_mov_b32 %[so], 8192
buffer_load_dwordx4 a[152:155], %[vo], %[rv], %[so] offen offset:2048
v_mfma_f32_16x16x32_bf16 v[40:43], a[80:83], v[52:55], v[40:43]
v_mfma_f32_16x16x32_bf16 v[44:47], a[84:87], v[52:55], v[44:47]
s_movk_i32 s94, 6400
v_perm_b32 v104, v165, v164, s96
v_perm_b32 v105, v167, v166, s96
v_perm_b32 v106, v165, v164, s97
v_perm_b32 v107, v167, v166, s97
v_pk_mad_u16 v108, v164, 2, -1 op_sel_hi:[1,0,0]
v_pk_mad_u16 v109, v165, 2, -1 op_sel_hi:[1,0,0]
v_pk_mad_u16 v110, v166, 2, -1 op_sel_hi:[1,0,0]
v_pk_mad_u16 v111, v167, 2, -1 op_sel_hi:[1,0,0]
v_and_b32 v112, 0x7f7f7f7f, v104
v_and_b32 v113, 0x7f7f7f7f, v105
v_sub_u32 v112, s80, v112
v_sub_u32 v113, s80, v113
v_pk_min_u16 v108, v108, v109
v_pk_min_u16 v110, v110, v111
v_pk_min_u16 v108, v108, v110
v_bitop3_b32 v109, v112, v113, s98 bitop3:0xa8
v_pk_sub_u16 v108, s81, v108 clamp
v_cmp_ne_u32_e64 vcc, v108, v109
v_perm_b32 v112, s99, v115, v112
v_perm_b32 v113, s99, v115, v113
v_bitop3_b32 v112, v112, v104, s98 bitop3:0x8d
v_bitop3_b32 v113, v113, v105, s98 bitop3:0x8d
v_alignbit_b32 v113, v113, v113, 28
v_or_b32 v114, v112, v113
s_getpc_b64 s[92:93]
s_cbranch_vccnz Lexc
v_accvgpr_write_b32 a150, v114
v_accvgpr_write_b32 a151, v106
v_accvgpr_write_b32 a156, v107
s_waitcnt vmcnt(15)
buffer_load_dwordx4 a[160:163], %[vo], %[rv], %[so] offen offset:3072
s_movk_i32 s94, 6464
s_nop 0
v_perm_b32 v104, v169, v168, s96
v_perm_b32 v105, v171, v170, s96
v_perm_b32 v106, v169, v168, s97
v_perm_b32 v107, v171, v170, s97
v_pk_mad_u16 v108, v168, 2, -1 op_sel_hi:[1,0,0]
v_pk_mad_u16 v109, v169, 2, -1 op_sel_hi:[1,0,0]
v_pk_mad_u16 v110, v170, 2, -1 op_sel_hi:[1,0,0]
v_pk_mad_u16 v111, v171, 2, -1 op_sel_hi:[1,0,0]
v_and_b32 v112, 0x7f7f7f7f, v104
v_and_b32 v113, 0x7f7f7f7f, v105
v_sub_u32 v112, s80, v112
v_sub_u32 v113, s80, v113
v_pk_min_u16 v108, v108, v109
v_pk_min_u16 v110, v110, v111
v_pk_min_u16 v108, v108, v110
v_bitop3_b32 v109, v112, v113, s98 bitop3:0xa8
v_pk_sub_u16 v108, s81, v108 clamp
v_cmp_ne_u32_e64 vcc, v108, v109
v_perm_b32 v112, s99, v115, v112
v_perm_b32 v113, s99, v115, v113
v_bitop3_b32 v112, v112, v104, s98 bitop3:0x8d
v_bitop3_b32 v113, v113, v105, s98 bitop3:0x8d
v_alignbit_b32 v113, v113, v113, 28
v_or_b32 v114, v112, v113
s_getpc_b64 s[92:93]
s_cbranch_vccnz Lexc
v_accvgpr_write_b32 a157, v114
v_accvgpr_write_b32 a158, v106
v_accvgpr_write_b32 a159, v107
s_waitcnt vmcnt(15)
s_branch LP34_%=b
LP34_%=:
s_sleep 1
LP34_%=b:
s_memrealtime vcc
s_waitcnt lgkmcnt(0)
s_sub_u32 %[so], vcc_lo, %[t0]
s_cmp_lt_u32 %[so], 2210
s_cbranch_scc1 LP34_%=
s_cmp_lt_u32 %[so], 2290
s_cbranch_scc0 LP34_%=f
s_waitcnt vmcnt(8)
s_cmp_lt_u32 %[so], 2240
s_cbranch_scc0 LP34_%=f
s_waitcnt vmcnt(4)
LP34_%=f:
buffer_load_dword a164, %[vo], %[rc], 0 offen
s_mov_b32 %[so], 12288
buffer_load_dwordx4 a[164:167], %[vo], %[rv], %[so] offen offset:0
v_mfma_f32_16x16x32_bf16 v[48:51], a[88:91], v[52:55], v[48:51]
s_movk_i32 s94, 6528
v_perm_b32 v104, v173, v172, s96
v_perm_b32 v105, v175, v174, s96
v_perm_b32 v106, v173, v172, s97
v_perm_b32 v107, v175, v174, s97
v_pk_mad_u16 v108, v172, 2, -1 op_sel_hi:[1,0,0]
v_pk_mad_u16 v109, v173, 2, -1 op_sel_hi:[1,0,0]
v_pk_mad_u16 v110, v174, 2, -1 op_sel_hi:[1,0,0]
v_pk_mad_u16 v111, v175, 2, -1 op_sel_hi:[1,0,0]
v_and_b32 v112, 0x7f7f7f7f, v104
v_and_b32 v113, 0x7f7f7f7f, v105
v_sub_u32 v112, s80, v112
v_sub_u32 v113, s80, v113
v_pk_min_u16 v108, v108, v109
v_pk_min_u16 v110, v110, v111
v_pk_min_u16 v108, v108, v110
v_bitop3_b32 v109, v112, v113, s98 bitop3:0xa8
v_pk_sub_u16 v108, s81, v108 clamp
v_cmp_ne_u32_e64 vcc, v108, v109
v_perm_b32 v112, s99, v115, v112
v_perm_b32 v113, s99, v115, v113
v_bitop3_b32 v112, v112, v104, s98 bitop3:0x8d
v_bitop3_b32 v113, v113, v105, s98 bitop3:0x8d
v_alignbit_b32 v113, v113, v113, 28
v_or_b32 v114, v112, v113
s_getpc_b64 s[92:93]
s_cbranch_vccnz Lexc
s_nop 0
v_accvgpr_write_b32 a168, v114
v_accvgpr_write_b32 a169, v106
v_accvgpr_write_b32 a170, v107
v_cndmask_b32 v36, v36, v38, %[m2]
v_cndmask_b32 v37, v37, v39, %[m2]
v_cndmask_b32 v40, v40, v42, %[m2]
v_cndmask_b32 v41, v41, v43, %[m2]
v_cndmask_b32 v44, v44, v46, %[m2]
v_cndmask_b32 v45, v45, v47, %[m2]
v_cndmask_b32 v48, v48, v50, %[m2]
v_cndmask_b32 v49, v49, v51, %[m2]
v_cndmask_b32 v38, v40, v37, %[m1]
v_cndmask_b32 v39, v36, v41, %[m1]
v_cndmask_b32 v46, v48, v45, %[m1]
v_cndmask_b32 v47, v44, v49, %[m1]
v_add_f32_dpp v39, v38, v39 quad_perm:[1,0,3,2] row_mask:0xf bank_mask:0xf
v_add_f32_dpp v47, v46, v47 quad_perm:[1,0,3,2] row_mask:0xf bank_mask:0xf
v_cndmask_b32 v40, v47, v39, %[m2]
v_cndmask_b32 v41, v39, v47, %[m2]
s_nop 0
v_add_f32_dpp %[sc7], v40, v41 quad_perm:[2,3,0,1] row_mask:0xf bank_mask:0xf
s_waitcnt vmcnt(15)
s_mov_b32 %[so], 33024
buffer_load_dword v103, %[go], %[ru], %[so] offen offset:2752
s_waitcnt vmcnt(15)
s_mov_b32 %[so], 12288
buffer_load_dwordx4 a[172:175], %[vo], %[rv], %[so] offen offset:1024
)"
      : [sc0] "=&v"(a.sc[0]), [sc1] "=&v"(a.sc[1]), [sc2] "=&v"(a.sc[2]), [sc3] "=&v"(a.sc[3]), [sc4] "=&v"(a.sc[4]), [sc5] "=&v"(a.sc[5]), [sc6] "=&v"(a.sc[6]), [sc7] "=&v"(a.sc[7]), [so] "=&s"(so), "+{s78}"(rcn)
      : [rk] "{s[64:67]}"(c.rsK), [rv] "{s[60:63]}"(c.rsV), [rc] "{s[52:55]}"(c.rsC), [t0] "s"(t0), [ko] "v"(k_off(c)), [vo] "v"(c.lane * 16), [rg] "s"(mkrs(p.wg, DM * FF * 2)), [ru] "s"(mkrs(p.wu, DM * FF * 2)), [go] "v"(wgu_off(c)), [m1] "s"(0xAAAAAAAAAAAAAAAAull), [m2] "s"(0xCCCCCCCCCCCCCCCCull), [q0] "v"(q4[0]), [q1] "v"(q4[1]), [q2] "v"(q4[2]), [q3] "v"(q4[3]), "{s79}"(r_base(c)), "{s80}"(t4k), "{s81}"(thk)
      : "memory", "vcc", "scc", "s84", "s85", "s86", "s87", "s88", "s89", "s90", "s91", "s92", "s93", "s94", "s95", "s96", "s97", "s98", "s99");

    if (2) { t0 -= (tb & 1) * (max((int)(t0 - tb) + (30) - (20), 0) >> (2 == 1)); asm volatile("" : "+s"(t0)); }
    STAMP(2);
    softmax_local(a);
#pragma unroll
    for (int k = 0; k < 8; ++k) a.pp[k] = pk2(a.sc[k], a.sc[k]);
    const u32 tag = c.epoch + 1;
    st128(c.rsW, c.lane == 0 ? 0 : OOB, WS_LAYER0 + O_ML + (c.h * 32 + c.s * 4 + c.w) * 16, u32x4{asu(a.m), tag, asu(a.l), tag});
    u32x4 ml, acc; u32 ak;
    asm volatile(
R"(
.p2align 3
s_nop 4
s_mov_b32 s96, 0x07050301
s_mov_b32 s97, 0x06040200
s_mov_b32 s98, 0x80808080
s_mov_b32 s99, 0xfffefdfc
v_mov_b32 v52, 0
v_mov_b32 v53, 0
v_mov_b32 v54, 0
v_mov_b32 v55, 0
s_waitcnt vmcnt(14)
s_waitcnt vmcnt(13)
s_waitcnt vmcnt(12)
s_waitcnt vmcnt(10)
s_waitcnt vmcnt(9)
v_mov_b32_e64 v108, 0
v_accvgpr_read_b32 v109, a92
v_pk_add_u16 v109, v109, v109
v_pk_max_u16 v108, v108, v109
v_accvgpr_read_b32 v109, a93
v_pk_add_u16 v109, v109, v109
v_pk_max_u16 v108, v108, v109
v_accvgpr_read_b32 v109, a94
v_pk_add_u16 v109, v109, v109
v_pk_max_u16 v108, v108, v109
v_accvgpr_read_b32 v109, a95
v_pk_add_u16 v109, v109, v109
v_pk_max_u16 v108, v108, v109
v_accvgpr_read_b32 v109, a96
v_pk_add_u16 v109, v109, v109
v_pk_max_u16 v108, v108, v109
v_accvgpr_read_b32 v109, a97
v_pk_add_u16 v109, v109, v109
v_pk_max_u16 v108, v108, v109
v_accvgpr_read_b32 v109, a98
v_pk_add_u16 v109, v109, v109
v_pk_max_u16 v108, v108, v109
v_accvgpr_read_b32 v109, a99
v_pk_add_u16 v109, v109, v109
v_pk_max_u16 v108, v108, v109
v_accvgpr_read_b32 v109, a100
v_pk_add_u16 v109, v109, v109
v_pk_max_u16 v108, v108, v109
v_accvgpr_read_b32 v109, a101
v_pk_add_u16 v109, v109, v109
v_pk_max_u16 v108, v108, v109
v_accvgpr_read_b32 v109, a102
v_pk_add_u16 v109, v109, v109
v_pk_max_u16 v108, v108, v109
v_accvgpr_read_b32 v109, a103
v_pk_add_u16 v109, v109, v109
v_pk_max_u16 v108, v108, v109
v_accvgpr_read_b32 v109, a104
v_pk_add_u16 v109, v109, v109
v_pk_max_u16 v108, v108, v109
v_accvgpr_read_b32 v109, a105
v_pk_add_u16 v109, v109, v109
v_pk_max_u16 v108, v108, v109
v_accvgpr_read_b32 v109, a106
v_pk_add_u16 v109, v109, v109
v_pk_max_u16 v108, v108, v109
v_accvgpr_read_b32 v109, a107
v_pk_add_u16 v109, v109, v109
v_pk_max_u16 v108, v108, v109
v_accvgpr_read_b32 v109, a108
v_pk_add_u16 v109, v109, v109
v_pk_max_u16 v108, v108, v109
v_accvgpr_read_b32 v109, a109
v_pk_add_u16 v109, v109, v109
v_pk_max_u16 v108, v108, v109
v_accvgpr_read_b32 v109, a110
v_pk_add_u16 v109, v109, v109
v_pk_max_u16 v108, v108, v109
v_accvgpr_read_b32 v109, a111
v_pk_add_u16 v109, v109, v109
v_pk_max_u16 v108, v108, v109
v_accvgpr_read_b32 v109, a116
v_pk_add_u16 v109, v109, v109
v_pk_max_u16 v108, v108, v109
v_accvgpr_read_b32 v109, a117
v_pk_add_u16 v109, v109, v109
v_pk_max_u16 v108, v108, v109
v_accvgpr_read_b32 v109, a118
v_pk_add_u16 v109, v109, v109
v_pk_max_u16 v108, v108, v109
v_accvgpr_read_b32 v109, a119
v_pk_add_u16 v109, v109, v109
v_pk_max_u16 v108, v108, v109
v_accvgpr_read_b32 v109, a124
v_pk_add_u16 v109, v109, v109
v_pk_max_u16 v108, v108, v109
v_accvgpr_read_b32 v109, a125
v_pk_add_u16 v109, v109, v109
v_pk_max_u16 v108, v108, v109
v_accvgpr_read_b32 v109, a126
v_pk_add_u16 v109, v109, v109
v_pk_max_u16 v108, v108, v109
v_accvgpr_read_b32 v109, a127
v_pk_add_u16 v109, v109, v109
v_pk_max_u16 v108, v108, v109
v_accvgpr_read_b32 v109, a132
v_pk_add_u16 v109, v109, v109
v_pk_max_u16 v108, v108, v109
v_accvgpr_read_b32 v109, a133
v_pk_add_u16 v109, v109, v109
v_pk_max_u16 v108, v108, v109
v_accvgpr_read_b32 v109, a134
v_pk_add_u16 v109, v109, v109
v_pk_max_u16 v108, v108, v109
v_accvgpr_read_b32 v109, a135
v_pk_add_u16 v109, v109, v109
v_pk_max_u16 v108, v108, v109
v_pk_max_u16 v108, v108, v108 op_sel:[0,1] op_sel_hi:[1,0]
s_nop 1
v_max_u32_dpp v108, v108, v108 quad_perm:[1,0,3,2] row_mask:0xf bank_mask:0xf
s_nop 1
v_max_u32_dpp v108, v108, v108 quad_perm:[2,3,0,1] row_mask:0xf bank_mask:0xf
s_nop 1
v_max_u32_dpp v108, v108, v108 row_half_mirror row_mask:0xf bank_mask:0xf
s_nop 1
v_max_u32_dpp v108, v108, v108 row_ror:8 row_mask:0xf bank_mask:0xf
s_nop 0
v_readlane_b32 s82, v108, 0
v_readlane_b32 s83, v108, 16
v_readlane_b32 s94, v108, 32
v_readlane_b32 s95, v108, 48
s_nop 1
s_max_u32 s82, s82, s83
s_max_u32 s94, s94, s95
s_max_u32 s82, s82, s94
s_lshr_b32 s82, s82, 25
s_max_u32 s82, s82, 13
s_nop 0
s_min_u32 s82, s82, 70
s_mul_i32 s83, s82, 0x02000200
s_sub_u32 s83, s83, 0x0d020d02
s_mul_i32 s82, s82, 0x01010101
v_and_b32_dpp v52, %[p0], %[hm] row_newbcast:0 row_mask:0xf bank_mask:0x1
v_and_b32_dpp v53, %[p0], %[hm] row_newbcast:0 row_mask:0xf bank_mask:0x2
v_and_b32_dpp v54, %[p0], %[hm] row_newbcast:0 row_mask:0xf bank_mask:0x4
v_and_b32_dpp v55, %[p0], %[hm] row_newbcast:0 row_mask:0xf bank_mask:0x8
s_nop 1
v_mfma_f32_16x16x32_bf16 %[acc], a[92:95], v[52:55], 0
s_movk_i32 s94, 6592
v_perm_b32 v104, v177, v176, s96
v_perm_b32 v105, v179, v178, s96
v_perm_b32 v106, v177, v176, s97
v_perm_b32 v107, v179, v178, s97
v_pk_mad_u16 v108, v176, 2, -1 op_sel_hi:[1,0,0]
v_pk_mad_u16 v109, v177, 2, -1 op_sel_hi:[1,0,0]
v_pk_mad_u16 v110, v178, 2, -1 op_sel_hi:[1,0,0]
v_pk_mad_u16 v111, v179, 2, -1 op_sel_hi:[1,0,0]
v_and_b32 v112, 0x7f7f7f7f, v104
v_and_b32 v113, 0x7f7f7f7f, v105
v_sub_u32 v112, s80, v112
v_sub_u32 v113, s80, v113
v_pk_min_u16 v108, v108, v109
v_pk_min_u16 v110, v110, v111
v_pk_min_u16 v108, v108, v110
v_bitop3_b32 v109, v112, v113, s98 bitop3:0xa8
v_pk_sub_u16 v108, s81, v108 clamp
v_cmp_ne_u32_e64 vcc, v108, v109
v_perm_b32 v112, s99, v115, v112
v_perm_b32 v113, s99, v115, v113
v_bitop3_b32 v112, v112, v104, s98 bitop3:0x8d
v_bitop3_b32 v113, v113, v105, s98 bitop3:0x8d
v_alignbit_b32 v113, v113, v113, 28
v_or_b32 v114, v112, v113
s_getpc_b64 s[92:93]
s_cbranch_vccnz Lexc
v_accvgpr_write_b32 a171, v114
v_accvgpr_write_b32 a176, v106
v_accvgpr_write_b32 a177, v107
s_movk_i32 s94, 6656
v_perm_b32 v104, v181, v180, s96
v_perm_b32 v105, v183, v182, s96
v_perm_b32 v106, v181, v180, s97
v_perm_b32 v107, v183, v182, s97
v_pk_mad_u16 v108, v180, 2, -1 op_sel_hi:[1,0,0]
v_pk_mad_u16 v109, v181, 2, -1 op_sel_hi:[1,0,0]
v_pk_mad_u16 v110, v182, 2, -1 op_sel_hi:[1,0,0]
v_pk_mad_u16 v111, v183, 2, -1 op_sel_hi:[1,0,0]
v_and_b32 v112, 0x7f7f7f7f, v104
v_and_b32 v113, 0x7f7f7f7f, v105
v_sub_u32 v112, s80, v112
v_sub_u32 v113, s80, v113
v_pk_min_u16 v108, v108, v109
v_pk_min_u16 v110, v110, v111
v_pk_min_u16 v108, v108, v110
v_bitop3_b32 v109, v112, v113, s98 bitop3:0xa8
v_pk_sub_u16 v108, s81, v108 clamp
v_cmp_ne_u32_e64 vcc, v108, v109
v_perm_b32 v112, s99, v115, v112
v_perm_b32 v113, s99, v115, v113
v_bitop3_b32 v112, v112, v104, s98 bitop3:0x8d
v_bitop3_b32 v113, v113, v105, s98 bitop3:0x8d
v_alignbit_b32 v113, v113, v113, 28
v_or_b32 v114, v112, v113
s_getpc_b64 s[92:93]
s_cbranch_vccnz Lexc
s_nop 0
v_accvgpr_write_b32 a178, v114
v_accvgpr_write_b32 a179, v106
v_accvgpr_write_b32 a180, v107
s_mov_b32 %[so], 12288
buffer_load_dwordx4 v[36:39], %[vo], %[rv], %[so] offen offset:2048
buffer_load_dwordx4 v[40:43], %[vo], %[rv], %[so] offen offset:3072
s_branch LP35_%=b
LP35_%=:
s_sleep 1
LP35_%=b:
s_memrealtime vcc
s_waitcnt lgkmcnt(0)
s_sub_u32 %[so], vcc_lo, %[t0]
s_cmp_lt_u32 %[so], 2277
s_cbranch_scc1 LP35_%=
s_cmp_lt_u32 %[so], 2357
s_cbranch_scc0 LP35_%=f
s_waitcnt vmcnt(8)
s_cmp_lt_u32 %[so], 2307
s_cbranch_scc0 LP35_%=f
s_waitcnt vmcnt(4)
s_nop 0
LP35_%=f:
s_mov_b32 %[so], 16384
buffer_load_dwordx4 v[44:47], %[vo], %[rv], %[so] offen offset:0
v_and_b32_dpp v52, %[p0], %[hm] row_newbcast:1 row_mask:0xf bank_mask:0x1
v_and_b32_dpp v53, %[p0], %[hm] row_newbcast:1 row_mask:0xf bank_mask:0x2
v_and_b32_dpp v54, %[p0], %[hm] row_newbcast:1 row_mask:0xf bank_mask:0x4
v_and_b32_dpp v55, %[p0], %[hm] row_newbcast:1 row_mask:0xf bank_mask:0x8
s_nop 1
v_mfma_f32_16x16x32_bf16 %[acc], a[96:99], v[52:55], %[acc]
s_movk_i32 s94, 6720
v_perm_b32 v104, v185, v184, s96
v_perm_b32 v105, v187, v186, s96
v_perm_b32 v106, v185, v184, s97
v_perm_b32 v107, v187, v186, s97
v_pk_mad_u16 v108, v184, 2, -1 op_sel_hi:[1,0,0]
v_pk_mad_u16 v109, v185, 2, -1 op_sel_hi:[1,0,0]
v_pk_mad_u16 v110, v186, 2, -1 op_sel_hi:[1,0,0]
v_pk_mad_u16 v111, v187, 2, -1 op_sel_hi:[1,0,0]
v_and_b32 v112, 0x7f7f7f7f, v104
v_and_b32 v113, 0x7f7f7f7f, v105
v_sub_u32 v112, s80, v112
v_sub_u32 v113, s80, v113
v_pk_min_u16 v108, v108, v109
v_pk_min_u16 v110, v110, v111
v_pk_min_u16 v108, v108, v110
v_bitop3_b32 v109, v112, v113, s98 bitop3:0xa8
v_pk_sub_u16 v108, s81, v108 clamp
v_cmp_ne_u32_e64 vcc, v108, v109
v_perm_b32 v112, s99, v115, v112
v_perm_b32 v113, s99, v115, v113
v_bitop3_b32 v112, v112, v104, s98 bitop3:0x8d
v_bitop3_b32 v113, v113, v105, s98 bitop3:0x8d
v_alignbit_b32 v113, v113, v113, 28
v_or_b32 v114, v112, v113
s_getpc_b64 s[92:93]
s_cbranch_vccnz Lexc
s_nop 0
v_accvgpr_write_b32 a181, v114
v_accvgpr_write_b32 a182, v106
v_accvgpr_write_b32 a183, v107
buffer_load_dwordx4 v[48:51], %[vo], %[rv], %[so] offen offset:1024
v_and_b32_dpp v52, %[p0], %[hm] row_newbcast:2 row_mask:0xf bank_mask:0x1
v_and_b32_dpp v53, %[p0], %[hm] row_newbcast:2 row_mask:0xf bank_mask:0x2
v_and_b32_dpp v54, %[p0], %[hm] row_newbcast:2 row_mask:0xf bank_mask:0x4
v_and_b32_dpp v55, %[p0], %[hm] row_newbcast:2 row_mask:0xf bank_mask:0x8
s_nop 1
v_mfma_f32_16x16x32_bf16 %[acc], a[100:103], v[52:55], %[acc]
s_movk_i32 s94, 6784
v_perm_b32 v104, v189, v188, s96
v_perm_b32 v105, v191, v190, s96
v_perm_b32 v106, v189, v188, s97
v_perm_b32 v107, v191, v190, s97
v_pk_mad_u16 v108, v188, 2, -1 op_sel_hi:[1,0,0]
v_pk_mad_u16 v109, v189, 2, -1 op_sel_hi:[1,0,0]
v_pk_mad_u16 v110, v190, 2, -1 op_sel_hi:[1,0,0]
v_pk_mad_u16 v111, v191, 2, -1 op_sel_hi:[1,0,0]
v_and_b32 v112, 0x7f7f7f7f, v104
v_and_b32 v113, 0x7f7f7f7f, v105
v_sub_u32 v112, s80, v112
v_sub_u32 v113, s80, v113
v_pk_min_u16 v108, v108, v109
v_pk_min_u16 v110, v110, v111
v_pk_min_u16 v108, v108, v110
v_bitop3_b32 v109, v112, v113, s98 bitop3:0xa8
v_pk_sub_u16 v108, s81, v108 clamp
v_cmp_ne_u32_e64 vcc, v108, v109
v_perm_b32 v112, s99, v115, v112
v_perm_b32 v113, s99, v115, v113
v_bitop3_b32 v112, v112, v104, s98 bitop3:0x8d
v_bitop3_b32 v113, v113, v105, s98 bitop3:0x8d
v_alignbit_b32 v113, v113, v113, 28
v_or_b32 v114, v112, v113
s_getpc_b64 s[92:93]
s_cbranch_vccnz Lexc
s_nop 0
v_accvgpr_write_b32 a184, v114
v_accvgpr_write_b32 a185, v106
v_accvgpr_write_b32 a186, v107
buffer_load_dwordx4 a[188:191], %[vo], %[rv], %[so] offen offset:2048
v_and_b32_dpp v52, %[p0], %[hm] row_newbcast:3 row_mask:0xf bank_mask:0x1
v_and_b32_dpp v53, %[p0], %[hm] row_newbcast:3 row_mask:0xf bank_mask:0x2
v_and_b32_dpp v54, %[p0], %[hm] row_newbcast:3 row_mask:0xf bank_mask:0x4
v_and_b32_dpp v55, %[p0], %[hm] row_newbcast:3 row_mask:0xf bank_mask:0x8
s_nop 1
v_mfma_f32_16x16x32_bf16 %[acc], a[104:107], v[52:55], %[acc]
s_movk_i32 s94, 6848
v_perm_b32 v104, v193, v192, s96
v_perm_b32 v105, v195, v194, s96
v_perm_b32 v106, v193, v192, s97
v_perm_b32 v107, v195, v194, s97
v_pk_mad_u16 v108, v192, 2, -1 op_sel_hi:[1,0,0]
v_pk_mad_u16 v109, v193, 2, -1 op_sel_hi:[1,0,0]
v_pk_mad_u16 v110, v194, 2, -1 op_sel_hi:[1,0,0]
v_pk_mad_u16 v111, v195, 2, -1 op_sel_hi:[1,0,0]
v_and_b32 v112, 0x7f7f7f7f, v104
v_and_b32 v113, 0x7f7f7f7f, v105
v_sub_u32 v112, s80, v112
v_sub_u32 v113, s80, v113
v_pk_min_u16 v108, v108, v109
v_pk_min_u16 v110, v110, v111
v_pk_min_u16 v108, v108, v110
v_bitop3_b32 v109, v112, v113, s98 bitop3:0xa8
v_pk_sub_u16 v108, s81, v108 clamp
v_cmp_ne_u32_e64 vcc, v108, v109
v_perm_b32 v112, s99, v115, v112
v_perm_b32 v113, s99, v115, v113
v_bitop3_b32 v112, v112, v104, s98 bitop3:0x8d
v_bitop3_b32 v113, v113, v105, s98 bitop3:0x8d
v_alignbit_b32 v113, v113, v113, 28
v_or_b32 v114, v112, v113
s_getpc_b64 s[92:93]
s_cbranch_vccnz Lexc
s_nop 0
v_accvgpr_write_b32 a187, v114
v_accvgpr_write_b32 a192, v106
v_accvgpr_write_b32 a193, v107
buffer_load_dwordx4 a[196:199], %[vo], %[rv], %[so] offen offset:3072
v_and_b32_dpp v52, %[p0], %[hm] row_newbcast:4 row_mask:0xf bank_mask:0x1
v_and_b32_dpp v53, %[p0], %[hm] row_newbcast:4 row_mask:0xf bank_mask:0x2
v_and_b32_dpp v54, %[p0], %[hm] row_newbcast:4 row_mask:0xf bank_mask:0x4
v_and_b32_dpp v55, %[p0], %[hm] row_newbcast:4 row_mask:0xf bank_mask:0x8
s_nop 1
v_mfma_f32_16x16x32_bf16 %[acc], a[108:111], v[52:55], %[acc]
s_movk_i32 s94, 6912
v_perm_b32 v104, v197, v196, s96
v_perm_b32 v105, v199, v198, s96
v_perm_b32 v106, v197, v196, s97
v_perm_b32 v107, v199, v198, s97
v_pk_mad_u16 v108, v196, 2, -1 op_sel_hi:[1,0,0]
v_pk_mad_u16 v109, v197, 2, -1 op_sel_hi:[1,0,0]
v_pk_mad_u16 v110, v198, 2, -1 op_sel_hi:[1,0,0]
)" R"(
v_pk_mad_u16 v111, v199, 2, -1 op_sel_hi:[1,0,0]
v_and_b32 v112, 0x7f7f7f7f, v104
v_and_b32 v113, 0x7f7f7f7f, v105
v_sub_u32 v112, s80, v112
v_sub_u32 v113, s80, v113
v_pk_min_u16 v108, v108, v109
v_pk_min_u16 v110, v110, v111
v_pk_min_u16 v108, v108, v110
v_bitop3_b32 v109, v112, v113, s98 bitop3:0xa8
v_pk_sub_u16 v108, s81, v108 clamp
v_cmp_ne_u32_e64 vcc, v108, v109
v_perm_b32 v112, s99, v115, v112
v_perm_b32 v113, s99, v115, v113
v_bitop3_b32 v112, v112, v104, s98 bitop3:0x8d
v_bitop3_b32 v113, v113, v105, s98 bitop3:0x8d
v_alignbit_b32 v113, v113, v113, 28
v_or_b32 v114, v112, v113
s_getpc_b64 s[92:93]
s_cbranch_vccnz Lexc
v_accvgpr_write_b32 a194, v114
v_accvgpr_write_b32 a195, v106
v_accvgpr_write_b32 a200, v107
s_branch LP36_%=b
LP36_%=:
s_sleep 1
LP36_%=b:
s_memrealtime vcc
s_waitcnt lgkmcnt(0)
s_sub_u32 %[so], vcc_lo, %[t0]
s_cmp_lt_u32 %[so], 2343
s_cbranch_scc1 LP36_%=
s_cmp_lt_u32 %[so], 2423
s_cbranch_scc0 LP36_%=f
s_waitcnt vmcnt(8)
s_cmp_lt_u32 %[so], 2373
s_cbranch_scc0 LP36_%=f
s_waitcnt vmcnt(4)
LP36_%=f:
s_mov_b32 %[so], 20480
buffer_load_dwordx4 a[204:207], %[vo], %[rv], %[so] offen offset:0
v_and_b32_dpp v52, %[p0], %[hm] row_newbcast:5 row_mask:0xf bank_mask:0x1
v_and_b32_dpp v53, %[p0], %[hm] row_newbcast:5 row_mask:0xf bank_mask:0x2
v_and_b32_dpp v54, %[p0], %[hm] row_newbcast:5 row_mask:0xf bank_mask:0x4
v_and_b32_dpp v55, %[p0], %[hm] row_newbcast:5 row_mask:0xf bank_mask:0x8
s_nop 1
v_mfma_f32_16x16x32_bf16 %[acc], a[116:119], v[52:55], %[acc]
s_movk_i32 s94, 6976
v_perm_b32 v104, v201, v200, s96
v_perm_b32 v105, v203, v202, s96
v_perm_b32 v106, v201, v200, s97
v_perm_b32 v107, v203, v202, s97
v_pk_mad_u16 v108, v200, 2, -1 op_sel_hi:[1,0,0]
v_pk_mad_u16 v109, v201, 2, -1 op_sel_hi:[1,0,0]
v_pk_mad_u16 v110, v202, 2, -1 op_sel_hi:[1,0,0]
v_pk_mad_u16 v111, v203, 2, -1 op_sel_hi:[1,0,0]
v_and_b32 v112, 0x7f7f7f7f, v104
v_and_b32 v113, 0x7f7f7f7f, v105
v_sub_u32 v112, s80, v112
v_sub_u32 v113, s80, v113
v_pk_min_u16 v108, v108, v109
v_pk_min_u16 v110, v110, v111
v_pk_min_u16 v108, v108, v110
v_bitop3_b32 v109, v112, v113, s98 bitop3:0xa8
v_pk_sub_u16 v108, s81, v108 clamp
v_cmp_ne_u32_e64 vcc, v108, v109
v_perm_b32 v112, s99, v115, v112
v_perm_b32 v113, s99, v115, v113
v_bitop3_b32 v112, v112, v104, s98 bitop3:0x8d
v_bitop3_b32 v113, v113, v105, s98 bitop3:0x8d
v_alignbit_b32 v113, v113, v113, 28
v_or_b32 v114, v112, v113
s_getpc_b64 s[92:93]
s_cbranch_vccnz Lexc
s_nop 0
v_accvgpr_write_b32 a201, v114
v_accvgpr_write_b32 a202, v106
v_accvgpr_write_b32 a203, v107
buffer_load_dwordx4 a[208:211], %[vo], %[rv], %[so] offen offset:1024
v_and_b32_dpp v52, %[p0], %[hm] row_newbcast:6 row_mask:0xf bank_mask:0x1
v_and_b32_dpp v53, %[p0], %[hm] row_newbcast:6 row_mask:0xf bank_mask:0x2
v_and_b32_dpp v54, %[p0], %[hm] row_newbcast:6 row_mask:0xf bank_mask:0x4
v_and_b32_dpp v55, %[p0], %[hm] row_newbcast:6 row_mask:0xf bank_mask:0x8
s_nop 1
v_mfma_f32_16x16x32_bf16 %[acc], a[124:127], v[52:55], %[acc]
s_movk_i32 s94, 7040
v_perm_b32 v104, v205, v204, s96
v_perm_b32 v105, v207, v206, s96
v_perm_b32 v106, v205, v204, s97
v_perm_b32 v107, v207, v206, s97
v_pk_mad_u16 v108, v204, 2, -1 op_sel_hi:[1,0,0]
v_pk_mad_u16 v109, v205, 2, -1 op_sel_hi:[1,0,0]
v_pk_mad_u16 v110, v206, 2, -1 op_sel_hi:[1,0,0]
v_pk_mad_u16 v111, v207, 2, -1 op_sel_hi:[1,0,0]
v_and_b32 v112, 0x7f7f7f7f, v104
v_and_b32 v113, 0x7f7f7f7f, v105
v_sub_u32 v112, s80, v112
v_sub_u32 v113, s80, v113
v_pk_min_u16 v108, v108, v109
v_pk_min_u16 v110, v110, v111
v_pk_min_u16 v108, v108, v110
v_bitop3_b32 v109, v112, v113, s98 bitop3:0xa8
v_pk_sub_u16 v108, s81, v108 clamp
v_cmp_ne_u32_e64 vcc, v108, v109
v_perm_b32 v112, s99, v115, v112
v_perm_b32 v113, s99, v115, v113
v_bitop3_b32 v112, v112, v104, s98 bitop3:0x8d
v_bitop3_b32 v113, v113, v105, s98 bitop3:0x8d
v_alignbit_b32 v113, v113, v113, 28
v_or_b32 v114, v112, v113
s_getpc_b64 s[92:93]
s_cbranch_vccnz Lexc
s_nop 0
v_accvgpr_write_b32 a92, v114
v_accvgpr_write_b32 a93, v106
v_accvgpr_write_b32 a94, v107
buffer_load_dwordx4 a[96:99], %[vo], %[rv], %[so] offen offset:2048
v_and_b32_dpp v52, %[p0], %[hm] row_newbcast:7 row_mask:0xf bank_mask:0x1
v_and_b32_dpp v53, %[p0], %[hm] row_newbcast:7 row_mask:0xf bank_mask:0x2
v_and_b32_dpp v54, %[p0], %[hm] row_newbcast:7 row_mask:0xf bank_mask:0x4
v_and_b32_dpp v55, %[p0], %[hm] row_newbcast:7 row_mask:0xf bank_mask:0x8
s_nop 1
v_mfma_f32_16x16x32_bf16 %[acc], a[132:135], v[52:55], %[acc]
s_movk_i32 s94, 7104
v_perm_b32 v104, v209, v208, s96
v_perm_b32 v105, v211, v210, s96
v_perm_b32 v106, v209, v208, s97
v_perm_b32 v107, v211, v210, s97
v_pk_mad_u16 v108, v208, 2, -1 op_sel_hi:[1,0,0]
v_pk_mad_u16 v109, v209, 2, -1 op_sel_hi:[1,0,0]
v_pk_mad_u16 v110, v210, 2, -1 op_sel_hi:[1,0,0]
v_pk_mad_u16 v111, v211, 2, -1 op_sel_hi:[1,0,0]
v_and_b32 v112, 0x7f7f7f7f, v104
v_and_b32 v113, 0x7f7f7f7f, v105
v_sub_u32 v112, s80, v112
v_sub_u32 v113, s80, v113
v_pk_min_u16 v108, v108, v109
v_pk_min_u16 v110, v110, v111
v_pk_min_u16 v108, v108, v110
v_bitop3_b32 v109, v112, v113, s98 bitop3:0xa8
v_pk_sub_u16 v108, s81, v108 clamp
v_cmp_ne_u32_e64 vcc, v108, v109
v_perm_b32 v112, s99, v115, v112
v_perm_b32 v113, s99, v115, v113
v_bitop3_b32 v112, v112, v104, s98 bitop3:0x8d
v_bitop3_b32 v113, v113, v105, s98 bitop3:0x8d
v_alignbit_b32 v113, v113, v113, 28
v_or_b32 v114, v112, v113
s_getpc_b64 s[92:93]
s_cbranch_vccnz Lexc
s_nop 0
v_accvgpr_write_b32 a95, v114
v_accvgpr_write_b32 a100, v106
v_accvgpr_write_b32 a101, v107
buffer_load_dwordx4 a[104:107], %[vo], %[rv], %[so] offen offset:3072
v_and_b32_dpp v52, %[p0], %[hm] row_newbcast:8 row_mask:0xf bank_mask:0x1
v_and_b32_dpp v53, %[p0], %[hm] row_newbcast:8 row_mask:0xf bank_mask:0x2
v_and_b32_dpp v54, %[p0], %[hm] row_newbcast:8 row_mask:0xf bank_mask:0x4
v_and_b32_dpp v55, %[p0], %[hm] row_newbcast:8 row_mask:0xf bank_mask:0x8
s_waitcnt vmcnt(18)
s_nop 0
v_mfma_f32_16x16x32_bf16 %[acc], a[136:139], v[52:55], %[acc]
s_movk_i32 s94, 7168
s_nop 0
v_perm_b32 v104, v213, v212, s96
v_perm_b32 v105, v215, v214, s96
v_perm_b32 v106, v213, v212, s97
v_perm_b32 v107, v215, v214, s97
v_pk_mad_u16 v108, v212, 2, -1 op_sel_hi:[1,0,0]
v_pk_mad_u16 v109, v213, 2, -1 op_sel_hi:[1,0,0]
v_pk_mad_u16 v110, v214, 2, -1 op_sel_hi:[1,0,0]
v_pk_mad_u16 v111, v215, 2, -1 op_sel_hi:[1,0,0]
v_and_b32 v112, 0x7f7f7f7f, v104
v_and_b32 v113, 0x7f7f7f7f, v105
v_sub_u32 v112, s80, v112
v_sub_u32 v113, s80, v113
v_pk_min_u16 v108, v108, v109
v_pk_min_u16 v110, v110, v111
v_pk_min_u16 v108, v108, v110
v_bitop3_b32 v109, v112, v113, s98 bitop3:0xa8
v_pk_sub_u16 v108, s81, v108 clamp
v_cmp_ne_u32_e64 vcc, v108, v109
v_perm_b32 v112, s99, v115, v112
v_perm_b32 v113, s99, v115, v113
v_bitop3_b32 v112, v112, v104, s98 bitop3:0x8d
v_bitop3_b32 v113, v113, v105, s98 bitop3:0x8d
v_alignbit_b32 v113, v113, v113, 28
v_or_b32 v114, v112, v113
s_getpc_b64 s[92:93]
s_cbranch_vccnz Lexc
v_accvgpr_write_b32 a102, v114
v_accvgpr_write_b32 a103, v106
v_accvgpr_write_b32 a108, v107
s_branch LP37_%=b
LP37_%=:
s_sleep 1
LP37_%=b:
s_memrealtime vcc
s_waitcnt lgkmcnt(0)
s_sub_u32 %[so], vcc_lo, %[t0]
s_cmp_lt_u32 %[so], 2410
s_cbranch_scc1 LP37_%=
s_cmp_lt_u32 %[so], 2490
s_cbranch_scc0 LP37_%=f
s_waitcnt vmcnt(8)
s_cmp_lt_u32 %[so], 2440
s_cbranch_scc0 LP37_%=f
s_waitcnt vmcnt(4)
LP37_%=f:
s_mov_b32 %[so], 24576
buffer_load_dwordx4 a[116:119], %[vo], %[rv], %[so] offen offset:0
v_and_b32_dpp v52, %[p0], %[hm] row_newbcast:9 row_mask:0xf bank_mask:0x1
v_and_b32_dpp v53, %[p0], %[hm] row_newbcast:9 row_mask:0xf bank_mask:0x2
v_and_b32_dpp v54, %[p0], %[hm] row_newbcast:9 row_mask:0xf bank_mask:0x4
v_and_b32_dpp v55, %[p0], %[hm] row_newbcast:9 row_mask:0xf bank_mask:0x8
s_waitcnt vmcnt(18)
s_nop 0
v_mfma_f32_16x16x32_bf16 %[acc], a[144:147], v[52:55], %[acc]
s_movk_i32 s94, 7232
s_nop 0
v_perm_b32 v104, v217, v216, s96
v_perm_b32 v105, v219, v218, s96
v_perm_b32 v106, v217, v216, s97
v_perm_b32 v107, v219, v218, s97
v_pk_mad_u16 v108, v216, 2, -1 op_sel_hi:[1,0,0]
v_pk_mad_u16 v109, v217, 2, -1 op_sel_hi:[1,0,0]
v_pk_mad_u16 v110, v218, 2, -1 op_sel_hi:[1,0,0]
v_pk_mad_u16 v111, v219, 2, -1 op_sel_hi:[1,0,0]
v_and_b32 v112, 0x7f7f7f7f, v104
v_and_b32 v113, 0x7f7f7f7f, v105
v_sub_u32 v112, s80, v112
v_sub_u32 v113, s80, v113
v_pk_min_u16 v108, v108, v109
v_pk_min_u16 v110, v110, v111
v_pk_min_u16 v108, v108, v110
v_bitop3_b32 v109, v112, v113, s98 bitop3:0xa8
v_pk_sub_u16 v108, s81, v108 clamp
v_cmp_ne_u32_e64 vcc, v108, v109
v_perm_b32 v112, s99, v115, v112
v_perm_b32 v113, s99, v115, v113
v_bitop3_b32 v112, v112, v104, s98 bitop3:0x8d
v_bitop3_b32 v113, v113, v105, s98 bitop3:0x8d
v_alignbit_b32 v113, v113, v113, 28
v_or_b32 v114, v112, v113
s_getpc_b64 s[92:93]
s_cbranch_vccnz Lexc
s_nop 0
v_accvgpr_write_b32 a109, v114
v_accvgpr_write_b32 a110, v106
v_accvgpr_write_b32 a111, v107
buffer_load_dwordx4 a[124:127], %[vo], %[rv], %[so] offen offset:1024
v_and_b32_dpp v52, %[p0], %[hm] row_newbcast:10 row_mask:0xf bank_mask:0x1
v_and_b32_dpp v53, %[p0], %[hm] row_newbcast:10 row_mask:0xf bank_mask:0x2
v_and_b32_dpp v54, %[p0], %[hm] row_newbcast:10 row_mask:0xf bank_mask:0x4
v_and_b32_dpp v55, %[p0], %[hm] row_newbcast:10 row_mask:0xf bank_mask:0x8
s_waitcnt vmcnt(17)
s_nop 0
v_mfma_f32_16x16x32_bf16 %[acc], a[152:155], v[52:55], %[acc]
s_movk_i32 s94, 7296
s_nop 0
v_perm_b32 v104, v221, v220, s96
v_perm_b32 v105, v223, v222, s96
v_perm_b32 v106, v221, v220, s97
v_perm_b32 v107, v223, v222, s97
v_pk_mad_u16 v108, v220, 2, -1 op_sel_hi:[1,0,0]
v_pk_mad_u16 v109, v221, 2, -1 op_sel_hi:[1,0,0]
v_pk_mad_u16 v110, v222, 2, -1 op_sel_hi:[1,0,0]
v_pk_mad_u16 v111, v223, 2, -1 op_sel_hi:[1,0,0]
v_and_b32 v112, 0x7f7f7f7f, v104
v_and_b32 v113, 0x7f7f7f7f, v105
v_sub_u32 v112, s80, v112
v_sub_u32 v113, s80, v113
v_pk_min_u16 v108, v108, v109
v_pk_min_u16 v110, v110, v111
v_pk_min_u16 v108, v108, v110
v_bitop3_b32 v109, v112, v113, s98 bitop3:0xa8
v_pk_sub_u16 v108, s81, v108 clamp
v_cmp_ne_u32_e64 vcc, v108, v109
v_perm_b32 v112, s99, v115, v112
v_perm_b32 v113, s99, v115, v113
v_bitop3_b32 v112, v112, v104, s98 bitop3:0x8d
v_bitop3_b32 v113, v113, v105, s98 bitop3:0x8d
v_alignbit_b32 v113, v113, v113, 28
v_or_b32 v114, v112, v113
s_getpc_b64 s[92:93]
s_cbranch_vccnz Lexc
v_accvgpr_write_b32 a132, v114
v_accvgpr_write_b32 a133, v106
v_accvgpr_write_b32 a134, v107
s_movk_i32 s94, 7360
v_perm_b32 v104, v225, v224, s96
v_perm_b32 v105, v227, v226, s96
v_perm_b32 v106, v225, v224, s97
v_perm_b32 v107, v227, v226, s97
v_pk_mad_u16 v108, v224, 2, -1 op_sel_hi:[1,0,0]
v_pk_mad_u16 v109, v225, 2, -1 op_sel_hi:[1,0,0]
v_pk_mad_u16 v110, v226, 2, -1 op_sel_hi:[1,0,0]
v_pk_mad_u16 v111, v227, 2, -1 op_sel_hi:[1,0,0]
v_and_b32 v112, 0x7f7f7f7f, v104
v_and_b32 v113, 0x7f7f7f7f, v105
v_sub_u32 v112, s80, v112
v_sub_u32 v113, s80, v113
v_pk_min_u16 v108, v108, v109
v_pk_min_u16 v110, v110, v111
v_pk_min_u16 v108, v108, v110
v_bitop3_b32 v109, v112, v113, s98 bitop3:0xa8
v_pk_sub_u16 v108, s81, v108 clamp
v_cmp_ne_u32_e64 vcc, v108, v109
v_perm_b32 v112, s99, v115, v112
v_perm_b32 v113, s99, v115, v113
v_bitop3_b32 v112, v112, v104, s98 bitop3:0x8d
v_bitop3_b32 v113, v113, v105, s98 bitop3:0x8d
v_alignbit_b32 v113, v113, v113, 28
v_or_b32 v114, v112, v113
s_getpc_b64 s[92:93]
s_cbranch_vccnz Lexc
s_nop 0
v_accvgpr_write_b32 a135, v114
v_accvgpr_write_b32 a136, v106
v_accvgpr_write_b32 a137, v107
buffer_load_dwordx4 a[144:147], %[vo], %[rv], %[so] offen offset:2048
v_and_b32_dpp v52, %[p0], %[hm] row_newbcast:11 row_mask:0xf bank_mask:0x1
v_and_b32_dpp v53, %[p0], %[hm] row_newbcast:11 row_mask:0xf bank_mask:0x2
v_and_b32_dpp v54, %[p0], %[hm] row_newbcast:11 row_mask:0xf bank_mask:0x4
v_and_b32_dpp v55, %[p0], %[hm] row_newbcast:11 row_mask:0xf bank_mask:0x8
s_waitcnt vmcnt(17)
s_nop 0
v_mfma_f32_16x16x32_bf16 %[acc], a[160:163], v[52:55], %[acc]
s_movk_i32 s94, 7424
s_nop 0
v_perm_b32 v104, v229, v228, s96
v_perm_b32 v105, v231, v230, s96
v_perm_b32 v106, v229, v228, s97
v_perm_b32 v107, v231, v230, s97
v_pk_mad_u16 v108, v228, 2, -1 op_sel_hi:[1,0,0]
)" R"(
v_pk_mad_u16 v109, v229, 2, -1 op_sel_hi:[1,0,0]
v_pk_mad_u16 v110, v230, 2, -1 op_sel_hi:[1,0,0]
v_pk_mad_u16 v111, v231, 2, -1 op_sel_hi:[1,0,0]
v_and_b32 v112, 0x7f7f7f7f, v104
v_and_b32 v113, 0x7f7f7f7f, v105
v_sub_u32 v112, s80, v112
v_sub_u32 v113, s80, v113
v_pk_min_u16 v108, v108, v109
v_pk_min_u16 v110, v110, v111
v_pk_min_u16 v108, v108, v110
v_bitop3_b32 v109, v112, v113, s98 bitop3:0xa8
v_pk_sub_u16 v108, s81, v108 clamp
v_cmp_ne_u32_e64 vcc, v108, v109
v_perm_b32 v112, s99, v115, v112
v_perm_b32 v113, s99, v115, v113
v_bitop3_b32 v112, v112, v104, s98 bitop3:0x8d
v_bitop3_b32 v113, v113, v105, s98 bitop3:0x8d
v_alignbit_b32 v113, v113, v113, 28
v_or_b32 v114, v112, v113
s_getpc_b64 s[92:93]
s_cbranch_vccnz Lexc
s_nop 0
v_accvgpr_write_b32 a138, v114
v_accvgpr_write_b32 a139, v106
v_accvgpr_write_b32 a152, v107
buffer_load_dwordx4 a[160:163], %[vo], %[rv], %[so] offen offset:3072
v_and_b32_dpp v52, %[p0], %[hm] row_newbcast:12 row_mask:0xf bank_mask:0x1
v_and_b32_dpp v53, %[p0], %[hm] row_newbcast:12 row_mask:0xf bank_mask:0x2
v_and_b32_dpp v54, %[p0], %[hm] row_newbcast:12 row_mask:0xf bank_mask:0x4
v_and_b32_dpp v55, %[p0], %[hm] row_newbcast:12 row_mask:0xf bank_mask:0x8
s_waitcnt vmcnt(16)
s_nop 0
v_mfma_f32_16x16x32_bf16 %[acc], a[164:167], v[52:55], %[acc]
s_movk_i32 s94, 7488
s_nop 0
v_perm_b32 v104, v233, v232, s96
v_perm_b32 v105, v235, v234, s96
v_perm_b32 v106, v233, v232, s97
v_perm_b32 v107, v235, v234, s97
v_pk_mad_u16 v108, v232, 2, -1 op_sel_hi:[1,0,0]
v_pk_mad_u16 v109, v233, 2, -1 op_sel_hi:[1,0,0]
v_pk_mad_u16 v110, v234, 2, -1 op_sel_hi:[1,0,0]
v_pk_mad_u16 v111, v235, 2, -1 op_sel_hi:[1,0,0]
v_and_b32 v112, 0x7f7f7f7f, v104
v_and_b32 v113, 0x7f7f7f7f, v105
v_sub_u32 v112, s80, v112
v_sub_u32 v113, s80, v113
v_pk_min_u16 v108, v108, v109
v_pk_min_u16 v110, v110, v111
v_pk_min_u16 v108, v108, v110
v_bitop3_b32 v109, v112, v113, s98 bitop3:0xa8
v_pk_sub_u16 v108, s81, v108 clamp
v_cmp_ne_u32_e64 vcc, v108, v109
v_perm_b32 v112, s99, v115, v112
v_perm_b32 v113, s99, v115, v113
v_bitop3_b32 v112, v112, v104, s98 bitop3:0x8d
v_bitop3_b32 v113, v113, v105, s98 bitop3:0x8d
v_alignbit_b32 v113, v113, v113, 28
v_or_b32 v114, v112, v113
s_getpc_b64 s[92:93]
s_cbranch_vccnz Lexc
v_accvgpr_write_b32 a153, v114
v_accvgpr_write_b32 a154, v106
v_accvgpr_write_b32 a155, v107
s_branch LP38_%=b
LP38_%=:
s_sleep 1
LP38_%=b:
s_memrealtime vcc
s_waitcnt lgkmcnt(0)
s_sub_u32 %[so], vcc_lo, %[t0]
s_cmp_lt_u32 %[so], 2477
s_cbranch_scc1 LP38_%=
s_cmp_lt_u32 %[so], 2557
s_cbranch_scc0 LP38_%=f
s_waitcnt vmcnt(8)
s_cmp_lt_u32 %[so], 2507
s_cbranch_scc0 LP38_%=f
s_waitcnt vmcnt(4)
LP38_%=f:
s_mov_b32 %[so], 28672
buffer_load_dwordx4 v[132:135], %[vo], %[rv], %[so] offen offset:0 nt
v_and_b32_dpp v52, %[p0], %[hm] row_newbcast:13 row_mask:0xf bank_mask:0x1
v_and_b32_dpp v53, %[p0], %[hm] row_newbcast:13 row_mask:0xf bank_mask:0x2
v_and_b32_dpp v54, %[p0], %[hm] row_newbcast:13 row_mask:0xf bank_mask:0x4
v_and_b32_dpp v55, %[p0], %[hm] row_newbcast:13 row_mask:0xf bank_mask:0x8
s_waitcnt vmcnt(15)
s_nop 0
v_mfma_f32_16x16x32_bf16 %[acc], a[172:175], v[52:55], %[acc]
s_movk_i32 s94, 7552
s_nop 0
v_perm_b32 v104, v237, v236, s96
v_perm_b32 v105, v239, v238, s96
v_perm_b32 v106, v237, v236, s97
v_perm_b32 v107, v239, v238, s97
v_pk_mad_u16 v108, v236, 2, -1 op_sel_hi:[1,0,0]
v_pk_mad_u16 v109, v237, 2, -1 op_sel_hi:[1,0,0]
v_pk_mad_u16 v110, v238, 2, -1 op_sel_hi:[1,0,0]
v_pk_mad_u16 v111, v239, 2, -1 op_sel_hi:[1,0,0]
v_and_b32 v112, 0x7f7f7f7f, v104
v_and_b32 v113, 0x7f7f7f7f, v105
v_sub_u32 v112, s80, v112
v_sub_u32 v113, s80, v113
v_pk_min_u16 v108, v108, v109
v_pk_min_u16 v110, v110, v111
v_pk_min_u16 v108, v108, v110
v_bitop3_b32 v109, v112, v113, s98 bitop3:0xa8
v_pk_sub_u16 v108, s81, v108 clamp
v_cmp_ne_u32_e64 vcc, v108, v109
v_perm_b32 v112, s99, v115, v112
v_perm_b32 v113, s99, v115, v113
v_bitop3_b32 v112, v112, v104, s98 bitop3:0x8d
v_bitop3_b32 v113, v113, v105, s98 bitop3:0x8d
v_alignbit_b32 v113, v113, v113, 28
v_or_b32 v114, v112, v113
s_getpc_b64 s[92:93]
s_cbranch_vccnz Lexc
s_nop 0
v_accvgpr_write_b32 a164, v114
v_accvgpr_write_b32 a165, v106
v_accvgpr_write_b32 a166, v107
buffer_load_dwordx4 v[136:139], %[vo], %[rv], %[so] offen offset:1024 nt
v_and_b32_dpp v52, %[p0], %[hm] row_newbcast:14 row_mask:0xf bank_mask:0x1
v_and_b32_dpp v53, %[p0], %[hm] row_newbcast:14 row_mask:0xf bank_mask:0x2
v_and_b32_dpp v54, %[p0], %[hm] row_newbcast:14 row_mask:0xf bank_mask:0x4
v_and_b32_dpp v55, %[p0], %[hm] row_newbcast:14 row_mask:0xf bank_mask:0x8
s_waitcnt vmcnt(15)
s_nop 0
v_mfma_f32_16x16x32_bf16 %[acc], v[36:39], v[52:55], %[acc]
s_movk_i32 s94, 7616
s_nop 0
v_perm_b32 v104, v241, v240, s96
v_perm_b32 v105, v243, v242, s96
v_perm_b32 v106, v241, v240, s97
v_perm_b32 v107, v243, v242, s97
v_pk_mad_u16 v108, v240, 2, -1 op_sel_hi:[1,0,0]
v_pk_mad_u16 v109, v241, 2, -1 op_sel_hi:[1,0,0]
v_pk_mad_u16 v110, v242, 2, -1 op_sel_hi:[1,0,0]
v_pk_mad_u16 v111, v243, 2, -1 op_sel_hi:[1,0,0]
v_and_b32 v112, 0x7f7f7f7f, v104
v_and_b32 v113, 0x7f7f7f7f, v105
v_sub_u32 v112, s80, v112
v_sub_u32 v113, s80, v113
v_pk_min_u16 v108, v108, v109
v_pk_min_u16 v110, v110, v111
v_pk_min_u16 v108, v108, v110
v_bitop3_b32 v109, v112, v113, s98 bitop3:0xa8
v_pk_sub_u16 v108, s81, v108 clamp
v_cmp_ne_u32_e64 vcc, v108, v109
v_perm_b32 v112, s99, v115, v112
v_perm_b32 v113, s99, v115, v113
v_bitop3_b32 v112, v112, v104, s98 bitop3:0x8d
v_bitop3_b32 v113, v113, v105, s98 bitop3:0x8d
v_alignbit_b32 v113, v113, v113, 28
v_or_b32 v114, v112, v113
s_getpc_b64 s[92:93]
s_cbranch_vccnz Lexc
s_nop 0
v_accvgpr_write_b32 a167, v114
v_accvgpr_write_b32 a172, v106
v_accvgpr_write_b32 a173, v107
buffer_load_dwordx4 v[140:143], %[vo], %[rv], %[so] offen offset:2048 nt
v_and_b32_dpp v52, %[p0], %[hm] row_newbcast:15 row_mask:0xf bank_mask:0x1
v_and_b32_dpp v53, %[p0], %[hm] row_newbcast:15 row_mask:0xf bank_mask:0x2
v_and_b32_dpp v54, %[p0], %[hm] row_newbcast:15 row_mask:0xf bank_mask:0x4
v_and_b32_dpp v55, %[p0], %[hm] row_newbcast:15 row_mask:0xf bank_mask:0x8
s_waitcnt vmcnt(15)
s_nop 0
v_mfma_f32_16x16x32_bf16 %[acc], v[40:43], v[52:55], %[acc]
s_movk_i32 s94, 7680
s_nop 0
v_perm_b32 v104, v245, v244, s96
v_perm_b32 v105, v247, v246, s96
v_perm_b32 v106, v245, v244, s97
v_perm_b32 v107, v247, v246, s97
v_pk_mad_u16 v108, v244, 2, -1 op_sel_hi:[1,0,0]
v_pk_mad_u16 v109, v245, 2, -1 op_sel_hi:[1,0,0]
v_pk_mad_u16 v110, v246, 2, -1 op_sel_hi:[1,0,0]
v_pk_mad_u16 v111, v247, 2, -1 op_sel_hi:[1,0,0]
v_and_b32 v112, 0x7f7f7f7f, v104
v_and_b32 v113, 0x7f7f7f7f, v105
v_sub_u32 v112, s80, v112
v_sub_u32 v113, s80, v113
v_pk_min_u16 v108, v108, v109
v_pk_min_u16 v110, v110, v111
v_pk_min_u16 v108, v108, v110
v_bitop3_b32 v109, v112, v113, s98 bitop3:0xa8
v_pk_sub_u16 v108, s81, v108 clamp
v_cmp_ne_u32_e64 vcc, v108, v109
v_perm_b32 v112, s99, v115, v112
v_perm_b32 v113, s99, v115, v113
v_bitop3_b32 v112, v112, v104, s98 bitop3:0x8d
v_bitop3_b32 v113, v113, v105, s98 bitop3:0x8d
v_alignbit_b32 v113, v113, v113, 28
v_or_b32 v114, v112, v113
s_getpc_b64 s[92:93]
s_cbranch_vccnz Lexc
v_accvgpr_write_b32 a174, v114
v_accvgpr_write_b32 a175, v106
v_mov_b32 v144, v107
buffer_load_dwordx4 v[148:151], %[vo], %[rv], %[so] offen offset:3072 nt
v_and_b32_dpp v52, %[p1], %[hm] row_newbcast:0 row_mask:0xf bank_mask:0x1
v_and_b32_dpp v53, %[p1], %[hm] row_newbcast:0 row_mask:0xf bank_mask:0x2
v_and_b32_dpp v54, %[p1], %[hm] row_newbcast:0 row_mask:0xf bank_mask:0x4
v_and_b32_dpp v55, %[p1], %[hm] row_newbcast:0 row_mask:0xf bank_mask:0x8
s_waitcnt vmcnt(15)
s_nop 0
v_mfma_f32_16x16x32_bf16 %[acc], v[44:47], v[52:55], %[acc]
s_movk_i32 s94, 7744
s_nop 0
v_accvgpr_read_b32 v108, a52
v_accvgpr_read_b32 v109, a53
v_accvgpr_read_b32 v110, a54
v_accvgpr_read_b32 v111, a55
v_perm_b32 v104, v109, v108, s96
v_perm_b32 v105, v111, v110, s96
v_perm_b32 v106, v109, v108, s97
v_perm_b32 v107, v111, v110, s97
v_pk_mad_u16 v108, v108, 2, -1 op_sel_hi:[1,0,0]
v_pk_mad_u16 v109, v109, 2, -1 op_sel_hi:[1,0,0]
v_pk_mad_u16 v110, v110, 2, -1 op_sel_hi:[1,0,0]
v_pk_mad_u16 v111, v111, 2, -1 op_sel_hi:[1,0,0]
v_and_b32 v112, 0x7f7f7f7f, v104
v_and_b32 v113, 0x7f7f7f7f, v105
v_sub_u32 v112, s80, v112
v_sub_u32 v113, s80, v113
v_pk_min_u16 v108, v108, v109
v_pk_min_u16 v110, v110, v111
v_pk_min_u16 v108, v108, v110
v_bitop3_b32 v109, v112, v113, s98 bitop3:0xa8
v_pk_sub_u16 v108, s81, v108 clamp
v_cmp_ne_u32_e64 vcc, v108, v109
v_perm_b32 v112, s99, v115, v112
v_perm_b32 v113, s99, v115, v113
v_bitop3_b32 v112, v112, v104, s98 bitop3:0x8d
v_bitop3_b32 v113, v113, v105, s98 bitop3:0x8d
v_alignbit_b32 v113, v113, v113, 28
v_or_b32 v114, v112, v113
s_getpc_b64 s[92:93]
s_cbranch_vccnz Lexc
v_accvgpr_write_b32 a52, v114
v_accvgpr_write_b32 a53, v106
v_accvgpr_write_b32 a54, v107
s_branch LP39_%=b
LP39_%=:
s_sleep 1
LP39_%=b:
s_memrealtime vcc
s_waitcnt lgkmcnt(0)
s_sub_u32 %[so], vcc_lo, %[t0]
s_cmp_lt_u32 %[so], 2539
s_cbranch_scc1 LP39_%=
s_cmp_lt_u32 %[so], 2619
s_cbranch_scc0 LP39_%=f
s_waitcnt vmcnt(8)
s_cmp_lt_u32 %[so], 2569
s_cbranch_scc0 LP39_%=f
s_waitcnt vmcnt(4)
LP39_%=f:
s_mov_b32 %[so], 32768
buffer_load_dwordx4 v[152:155], %[vo], %[rv], %[so] offen offset:0 nt
v_and_b32_dpp v52, %[p1], %[hm] row_newbcast:1 row_mask:0xf bank_mask:0x1
v_and_b32_dpp v53, %[p1], %[hm] row_newbcast:1 row_mask:0xf bank_mask:0x2
v_and_b32_dpp v54, %[p1], %[hm] row_newbcast:1 row_mask:0xf bank_mask:0x4
v_and_b32_dpp v55, %[p1], %[hm] row_newbcast:1 row_mask:0xf bank_mask:0x8
s_waitcnt vmcnt(15)
s_nop 0
v_mfma_f32_16x16x32_bf16 %[acc], v[48:51], v[52:55], %[acc]
s_movk_i32 s94, 7808
s_nop 0
v_accvgpr_read_b32 v108, a64
v_accvgpr_read_b32 v109, a65
v_accvgpr_read_b32 v110, a66
v_accvgpr_read_b32 v111, a67
v_perm_b32 v104, v109, v108, s96
v_perm_b32 v105, v111, v110, s96
v_perm_b32 v106, v109, v108, s97
v_perm_b32 v107, v111, v110, s97
v_pk_mad_u16 v108, v108, 2, -1 op_sel_hi:[1,0,0]
v_pk_mad_u16 v109, v109, 2, -1 op_sel_hi:[1,0,0]
v_pk_mad_u16 v110, v110, 2, -1 op_sel_hi:[1,0,0]
v_pk_mad_u16 v111, v111, 2, -1 op_sel_hi:[1,0,0]
v_and_b32 v112, 0x7f7f7f7f, v104
v_and_b32 v113, 0x7f7f7f7f, v105
v_sub_u32 v112, s80, v112
v_sub_u32 v113, s80, v113
v_pk_min_u16 v108, v108, v109
v_pk_min_u16 v110, v110, v111
v_pk_min_u16 v108, v108, v110
v_bitop3_b32 v109, v112, v113, s98 bitop3:0xa8
v_pk_sub_u16 v108, s81, v108 clamp
v_cmp_ne_u32_e64 vcc, v108, v109
v_perm_b32 v112, s99, v115, v112
v_perm_b32 v113, s99, v115, v113
v_bitop3_b32 v112, v112, v104, s98 bitop3:0x8d
v_bitop3_b32 v113, v113, v105, s98 bitop3:0x8d
v_alignbit_b32 v113, v113, v113, 28
v_or_b32 v114, v112, v113
s_getpc_b64 s[92:93]
s_cbranch_vccnz Lexc
s_nop 0
v_accvgpr_write_b32 a55, v114
v_accvgpr_write_b32 a64, v106
v_accvgpr_write_b32 a65, v107
buffer_load_dwordx4 v[156:159], %[vo], %[rv], %[so] offen offset:1024 nt
v_and_b32_dpp v52, %[p1], %[hm] row_newbcast:2 row_mask:0xf bank_mask:0x1
v_and_b32_dpp v53, %[p1], %[hm] row_newbcast:2 row_mask:0xf bank_mask:0x2
v_and_b32_dpp v54, %[p1], %[hm] row_newbcast:2 row_mask:0xf bank_mask:0x4
v_and_b32_dpp v55, %[p1], %[hm] row_newbcast:2 row_mask:0xf bank_mask:0x8
s_waitcnt vmcnt(15)
s_nop 0
v_mfma_f32_16x16x32_bf16 %[acc], a[188:191], v[52:55], %[acc]
s_movk_i32 s94, 7872
s_nop 0
v_accvgpr_read_b32 v108, a72
v_accvgpr_read_b32 v109, a73
v_accvgpr_read_b32 v110, a74
v_accvgpr_read_b32 v111, a75
v_perm_b32 v104, v109, v108, s96
v_perm_b32 v105, v111, v110, s96
v_perm_b32 v106, v109, v108, s97
v_perm_b32 v107, v111, v110, s97
v_pk_mad_u16 v108, v108, 2, -1 op_sel_hi:[1,0,0]
v_pk_mad_u16 v109, v109, 2, -1 op_sel_hi:[1,0,0]
v_pk_mad_u16 v110, v110, 2, -1 op_sel_hi:[1,0,0]
v_pk_mad_u16 v111, v111, 2, -1 op_sel_hi:[1,0,0]
v_and_b32 v112, 0x7f7f7f7f, v104
v_and_b32 v113, 0x7f7f7f7f, v105
v_sub_u32 v112, s80, v112
v_sub_u32 v113, s80, v113
v_pk_min_u16 v108, v108, v109
v_pk_min_u16 v110, v110, v111
v_pk_min_u16 v108, v108, v110
v_bitop3_b32 v109, v112, v113, s98 bitop3:0xa8
v_pk_sub_u16 v108, s81, v108 clamp
v_cmp_ne_u32_e64 vcc, v108, v109
v_perm_b32 v112, s99, v115, v112
)" R"(
v_perm_b32 v113, s99, v115, v113
v_bitop3_b32 v112, v112, v104, s98 bitop3:0x8d
v_bitop3_b32 v113, v113, v105, s98 bitop3:0x8d
v_alignbit_b32 v113, v113, v113, 28
v_or_b32 v114, v112, v113
s_getpc_b64 s[92:93]
s_cbranch_vccnz Lexc
s_nop 0
v_accvgpr_write_b32 a66, v114
v_accvgpr_write_b32 a67, v106
v_accvgpr_write_b32 a188, v107
buffer_load_dwordx4 v[160:163], %[vo], %[rv], %[so] offen offset:2048 nt
v_and_b32_dpp v52, %[p1], %[hm] row_newbcast:3 row_mask:0xf bank_mask:0x1
v_and_b32_dpp v53, %[p1], %[hm] row_newbcast:3 row_mask:0xf bank_mask:0x2
v_and_b32_dpp v54, %[p1], %[hm] row_newbcast:3 row_mask:0xf bank_mask:0x4
v_and_b32_dpp v55, %[p1], %[hm] row_newbcast:3 row_mask:0xf bank_mask:0x8
s_waitcnt vmcnt(15)
s_nop 0
v_mfma_f32_16x16x32_bf16 %[acc], a[196:199], v[52:55], %[acc]
s_movk_i32 s94, 7936
s_nop 0
v_accvgpr_read_b32 v108, a76
v_accvgpr_read_b32 v109, a77
v_accvgpr_read_b32 v110, a78
v_accvgpr_read_b32 v111, a79
v_perm_b32 v104, v109, v108, s96
v_perm_b32 v105, v111, v110, s96
v_perm_b32 v106, v109, v108, s97
v_perm_b32 v107, v111, v110, s97
v_pk_mad_u16 v108, v108, 2, -1 op_sel_hi:[1,0,0]
v_pk_mad_u16 v109, v109, 2, -1 op_sel_hi:[1,0,0]
v_pk_mad_u16 v110, v110, 2, -1 op_sel_hi:[1,0,0]
v_pk_mad_u16 v111, v111, 2, -1 op_sel_hi:[1,0,0]
v_and_b32 v112, 0x7f7f7f7f, v104
v_and_b32 v113, 0x7f7f7f7f, v105
v_sub_u32 v112, s80, v112
v_sub_u32 v113, s80, v113
v_pk_min_u16 v108, v108, v109
v_pk_min_u16 v110, v110, v111
v_pk_min_u16 v108, v108, v110
v_bitop3_b32 v109, v112, v113, s98 bitop3:0xa8
v_pk_sub_u16 v108, s81, v108 clamp
v_cmp_ne_u32_e64 vcc, v108, v109
v_perm_b32 v112, s99, v115, v112
v_perm_b32 v113, s99, v115, v113
v_bitop3_b32 v112, v112, v104, s98 bitop3:0x8d
v_bitop3_b32 v113, v113, v105, s98 bitop3:0x8d
v_alignbit_b32 v113, v113, v113, 28
v_or_b32 v114, v112, v113
s_getpc_b64 s[92:93]
s_cbranch_vccnz Lexc
s_nop 0
v_accvgpr_write_b32 a189, v114
v_accvgpr_write_b32 a190, v106
v_accvgpr_write_b32 a191, v107
buffer_load_dwordx4 v[164:167], %[vo], %[rv], %[so] offen offset:3072 nt
v_and_b32_dpp v52, %[p1], %[hm] row_newbcast:4 row_mask:0xf bank_mask:0x1
v_and_b32_dpp v53, %[p1], %[hm] row_newbcast:4 row_mask:0xf bank_mask:0x2
v_and_b32_dpp v54, %[p1], %[hm] row_newbcast:4 row_mask:0xf bank_mask:0x4
v_and_b32_dpp v55, %[p1], %[hm] row_newbcast:4 row_mask:0xf bank_mask:0x8
s_waitcnt vmcnt(15)
s_nop 0
v_mfma_f32_16x16x32_bf16 %[acc], a[204:207], v[52:55], %[acc]
s_movk_i32 s94, 8000
s_nop 0
v_accvgpr_read_b32 v108, a80
v_accvgpr_read_b32 v109, a81
v_accvgpr_read_b32 v110, a82
v_accvgpr_read_b32 v111, a83
v_perm_b32 v104, v109, v108, s96
v_perm_b32 v105, v111, v110, s96
v_perm_b32 v106, v109, v108, s97
v_perm_b32 v107, v111, v110, s97
v_pk_mad_u16 v108, v108, 2, -1 op_sel_hi:[1,0,0]
v_pk_mad_u16 v109, v109, 2, -1 op_sel_hi:[1,0,0]
v_pk_mad_u16 v110, v110, 2, -1 op_sel_hi:[1,0,0]
v_pk_mad_u16 v111, v111, 2, -1 op_sel_hi:[1,0,0]
v_and_b32 v112, 0x7f7f7f7f, v104
v_and_b32 v113, 0x7f7f7f7f, v105
v_sub_u32 v112, s80, v112
v_sub_u32 v113, s80, v113
v_pk_min_u16 v108, v108, v109
v_pk_min_u16 v110, v110, v111
v_pk_min_u16 v108, v108, v110
v_bitop3_b32 v109, v112, v113, s98 bitop3:0xa8
v_pk_sub_u16 v108, s81, v108 clamp
v_cmp_ne_u32_e64 vcc, v108, v109
v_perm_b32 v112, s99, v115, v112
v_perm_b32 v113, s99, v115, v113
v_bitop3_b32 v112, v112, v104, s98 bitop3:0x8d
v_bitop3_b32 v113, v113, v105, s98 bitop3:0x8d
v_alignbit_b32 v113, v113, v113, 28
v_or_b32 v114, v112, v113
s_getpc_b64 s[92:93]
s_cbranch_vccnz Lexc
v_accvgpr_write_b32 a72, v114
v_accvgpr_write_b32 a73, v106
v_accvgpr_write_b32 a74, v107
s_movk_i32 s94, 8064
v_accvgpr_read_b32 v108, a84
v_accvgpr_read_b32 v109, a85
v_accvgpr_read_b32 v110, a86
v_accvgpr_read_b32 v111, a87
v_perm_b32 v104, v109, v108, s96
v_perm_b32 v105, v111, v110, s96
v_perm_b32 v106, v109, v108, s97
v_perm_b32 v107, v111, v110, s97
v_pk_mad_u16 v108, v108, 2, -1 op_sel_hi:[1,0,0]
v_pk_mad_u16 v109, v109, 2, -1 op_sel_hi:[1,0,0]
v_pk_mad_u16 v110, v110, 2, -1 op_sel_hi:[1,0,0]
v_pk_mad_u16 v111, v111, 2, -1 op_sel_hi:[1,0,0]
v_and_b32 v112, 0x7f7f7f7f, v104
v_and_b32 v113, 0x7f7f7f7f, v105
v_sub_u32 v112, s80, v112
v_sub_u32 v113, s80, v113
v_pk_min_u16 v108, v108, v109
v_pk_min_u16 v110, v110, v111
v_pk_min_u16 v108, v108, v110
v_bitop3_b32 v109, v112, v113, s98 bitop3:0xa8
v_pk_sub_u16 v108, s81, v108 clamp
v_cmp_ne_u32_e64 vcc, v108, v109
v_perm_b32 v112, s99, v115, v112
v_perm_b32 v113, s99, v115, v113
v_bitop3_b32 v112, v112, v104, s98 bitop3:0x8d
v_bitop3_b32 v113, v113, v105, s98 bitop3:0x8d
v_alignbit_b32 v113, v113, v113, 28
v_or_b32 v114, v112, v113
s_getpc_b64 s[92:93]
s_cbranch_vccnz Lexc
v_accvgpr_write_b32 a75, v114
v_accvgpr_write_b32 a196, v106
v_accvgpr_write_b32 a197, v107
s_branch LP40_%=b
LP40_%=:
s_sleep 1
LP40_%=b:
s_memrealtime vcc
s_waitcnt lgkmcnt(0)
s_sub_u32 %[so], vcc_lo, %[t0]
s_cmp_lt_u32 %[so], 2602
s_cbranch_scc1 LP40_%=
s_cmp_lt_u32 %[so], 2682
s_cbranch_scc0 LP40_%=f
s_waitcnt vmcnt(8)
s_cmp_lt_u32 %[so], 2632
s_cbranch_scc0 LP40_%=f
s_waitcnt vmcnt(4)
LP40_%=f:
s_mov_b32 %[so], 36864
buffer_load_dwordx4 v[168:171], %[vo], %[rv], %[so] offen offset:0 nt
v_and_b32_dpp v52, %[p1], %[hm] row_newbcast:5 row_mask:0xf bank_mask:0x1
v_and_b32_dpp v53, %[p1], %[hm] row_newbcast:5 row_mask:0xf bank_mask:0x2
v_and_b32_dpp v54, %[p1], %[hm] row_newbcast:5 row_mask:0xf bank_mask:0x4
v_and_b32_dpp v55, %[p1], %[hm] row_newbcast:5 row_mask:0xf bank_mask:0x8
s_waitcnt vmcnt(15)
s_nop 0
v_mfma_f32_16x16x32_bf16 %[acc], a[208:211], v[52:55], %[acc]
s_movk_i32 s94, 8128
s_nop 0
v_accvgpr_read_b32 v108, a88
v_accvgpr_read_b32 v109, a89
v_accvgpr_read_b32 v110, a90
v_accvgpr_read_b32 v111, a91
v_perm_b32 v104, v109, v108, s96
v_perm_b32 v105, v111, v110, s96
v_perm_b32 v106, v109, v108, s97
v_perm_b32 v107, v111, v110, s97
v_pk_mad_u16 v108, v108, 2, -1 op_sel_hi:[1,0,0]
v_pk_mad_u16 v109, v109, 2, -1 op_sel_hi:[1,0,0]
v_pk_mad_u16 v110, v110, 2, -1 op_sel_hi:[1,0,0]
v_pk_mad_u16 v111, v111, 2, -1 op_sel_hi:[1,0,0]
v_and_b32 v112, 0x7f7f7f7f, v104
v_and_b32 v113, 0x7f7f7f7f, v105
v_sub_u32 v112, s80, v112
v_sub_u32 v113, s80, v113
v_pk_min_u16 v108, v108, v109
v_pk_min_u16 v110, v110, v111
v_pk_min_u16 v108, v108, v110
v_bitop3_b32 v109, v112, v113, s98 bitop3:0xa8
v_pk_sub_u16 v108, s81, v108 clamp
v_cmp_ne_u32_e64 vcc, v108, v109
v_perm_b32 v112, s99, v115, v112
v_perm_b32 v113, s99, v115, v113
v_bitop3_b32 v112, v112, v104, s98 bitop3:0x8d
v_bitop3_b32 v113, v113, v105, s98 bitop3:0x8d
v_alignbit_b32 v113, v113, v113, 28
v_or_b32 v114, v112, v113
s_getpc_b64 s[92:93]
s_cbranch_vccnz Lexc
s_nop 0
v_accvgpr_write_b32 a198, v114
v_accvgpr_write_b32 a199, v106
v_accvgpr_write_b32 a76, v107
buffer_load_dwordx4 v[172:175], %[vo], %[rv], %[so] offen offset:1024 nt
v_and_b32_dpp v52, %[p1], %[hm] row_newbcast:6 row_mask:0xf bank_mask:0x1
v_and_b32_dpp v53, %[p1], %[hm] row_newbcast:6 row_mask:0xf bank_mask:0x2
v_and_b32_dpp v54, %[p1], %[hm] row_newbcast:6 row_mask:0xf bank_mask:0x4
v_and_b32_dpp v55, %[p1], %[hm] row_newbcast:6 row_mask:0xf bank_mask:0x8
s_waitcnt vmcnt(15)
s_nop 0
v_mfma_f32_16x16x32_bf16 %[acc], a[96:99], v[52:55], %[acc]
buffer_load_dwordx4 v[176:179], %[vo], %[rv], %[so] offen offset:2048 nt
v_and_b32_dpp v52, %[p1], %[hm] row_newbcast:7 row_mask:0xf bank_mask:0x1
v_and_b32_dpp v53, %[p1], %[hm] row_newbcast:7 row_mask:0xf bank_mask:0x2
v_and_b32_dpp v54, %[p1], %[hm] row_newbcast:7 row_mask:0xf bank_mask:0x4
v_and_b32_dpp v55, %[p1], %[hm] row_newbcast:7 row_mask:0xf bank_mask:0x8
s_waitcnt vmcnt(15)
s_nop 0
v_mfma_f32_16x16x32_bf16 %[acc], a[104:107], v[52:55], %[acc]
buffer_load_dwordx4 v[180:183], %[vo], %[rv], %[so] offen offset:3072 nt
v_and_b32_dpp v52, %[p1], %[hm] row_newbcast:8 row_mask:0xf bank_mask:0x1
v_and_b32_dpp v53, %[p1], %[hm] row_newbcast:8 row_mask:0xf bank_mask:0x2
v_and_b32_dpp v54, %[p1], %[hm] row_newbcast:8 row_mask:0xf bank_mask:0x4
v_and_b32_dpp v55, %[p1], %[hm] row_newbcast:8 row_mask:0xf bank_mask:0x8
s_waitcnt vmcnt(15)
s_nop 0
v_mfma_f32_16x16x32_bf16 %[acc], a[116:119], v[52:55], %[acc]
s_branch LP41_%=b
LP41_%=:
s_sleep 1
LP41_%=b:
s_memrealtime vcc
s_waitcnt lgkmcnt(0)
s_sub_u32 %[so], vcc_lo, %[t0]
s_cmp_lt_u32 %[so], 2664
s_cbranch_scc1 LP41_%=
s_cmp_lt_u32 %[so], 2744
s_cbranch_scc0 LP41_%=f
s_waitcnt vmcnt(8)
s_cmp_lt_u32 %[so], 2694
s_cbranch_scc0 LP41_%=f
s_waitcnt vmcnt(4)
s_nop 0
LP41_%=f:
s_mov_b32 %[so], 40960
buffer_load_dwordx4 v[184:187], %[vo], %[rv], %[so] offen offset:0 nt
v_and_b32_dpp v52, %[p1], %[hm] row_newbcast:9 row_mask:0xf bank_mask:0x1
v_and_b32_dpp v53, %[p1], %[hm] row_newbcast:9 row_mask:0xf bank_mask:0x2
v_and_b32_dpp v54, %[p1], %[hm] row_newbcast:9 row_mask:0xf bank_mask:0x4
v_and_b32_dpp v55, %[p1], %[hm] row_newbcast:9 row_mask:0xf bank_mask:0x8
s_waitcnt vmcnt(15)
s_nop 0
v_mfma_f32_16x16x32_bf16 %[acc], a[124:127], v[52:55], %[acc]
buffer_load_dwordx4 v[188:191], %[vo], %[rv], %[so] offen offset:1024 nt
v_and_b32_dpp v52, %[p1], %[hm] row_newbcast:10 row_mask:0xf bank_mask:0x1
v_and_b32_dpp v53, %[p1], %[hm] row_newbcast:10 row_mask:0xf bank_mask:0x2
v_and_b32_dpp v54, %[p1], %[hm] row_newbcast:10 row_mask:0xf bank_mask:0x4
v_and_b32_dpp v55, %[p1], %[hm] row_newbcast:10 row_mask:0xf bank_mask:0x8
s_waitcnt vmcnt(15)
s_nop 0
v_mfma_f32_16x16x32_bf16 %[acc], a[144:147], v[52:55], %[acc]
buffer_load_dwordx4 v[192:195], %[vo], %[rv], %[so] offen offset:2048 nt
v_and_b32_dpp v52, %[p1], %[hm] row_newbcast:11 row_mask:0xf bank_mask:0x1
v_and_b32_dpp v53, %[p1], %[hm] row_newbcast:11 row_mask:0xf bank_mask:0x2
v_and_b32_dpp v54, %[p1], %[hm] row_newbcast:11 row_mask:0xf bank_mask:0x4
v_and_b32_dpp v55, %[p1], %[hm] row_newbcast:11 row_mask:0xf bank_mask:0x8
s_waitcnt vmcnt(15)
s_nop 0
v_mfma_f32_16x16x32_bf16 %[acc], a[160:163], v[52:55], %[acc]
buffer_load_dwordx4 v[196:199], %[vo], %[rv], %[so] offen offset:3072 nt
v_and_b32_dpp v52, %[p1], %[hm] row_newbcast:12 row_mask:0xf bank_mask:0x1
v_and_b32_dpp v53, %[p1], %[hm] row_newbcast:12 row_mask:0xf bank_mask:0x2
v_and_b32_dpp v54, %[p1], %[hm] row_newbcast:12 row_mask:0xf bank_mask:0x4
v_and_b32_dpp v55, %[p1], %[hm] row_newbcast:12 row_mask:0xf bank_mask:0x8
s_waitcnt vmcnt(15)
s_nop 0
v_mfma_f32_16x16x32_bf16 %[acc], v[132:135], v[52:55], %[acc]
s_movk_i32 s94, 9984
s_nop 0
v_perm_b32 v104, v133, v132, s96
v_perm_b32 v105, v135, v134, s96
v_perm_b32 v106, v133, v132, s97
v_perm_b32 v107, v135, v134, s97
v_pk_mad_u16 v108, v132, 2, -1 op_sel_hi:[1,0,0]
v_pk_mad_u16 v109, v133, 2, -1 op_sel_hi:[1,0,0]
v_pk_mad_u16 v110, v134, 2, -1 op_sel_hi:[1,0,0]
v_pk_mad_u16 v111, v135, 2, -1 op_sel_hi:[1,0,0]
v_and_b32 v112, 0x7f7f7f7f, v104
v_and_b32 v113, 0x7f7f7f7f, v105
v_sub_u32 v112, s82, v112
v_sub_u32 v113, s82, v113
v_pk_min_u16 v108, v108, v109
v_pk_min_u16 v110, v110, v111
v_pk_min_u16 v108, v108, v110
v_bitop3_b32 v109, v112, v113, s98 bitop3:0xa8
v_pk_sub_u16 v108, s83, v108 clamp
v_cmp_ne_u32_e64 vcc, v108, v109
v_perm_b32 v112, s99, v115, v112
v_perm_b32 v113, s99, v115, v113
v_bitop3_b32 v112, v112, v104, s98 bitop3:0x8d
v_bitop3_b32 v113, v113, v105, s98 bitop3:0x8d
v_alignbit_b32 v113, v113, v113, 28
v_or_b32 v114, v112, v113
s_getpc_b64 s[92:93]
s_cbranch_vccnz Lexc
v_accvgpr_write_b32 a77, v114
v_accvgpr_write_b32 a78, v106
v_accvgpr_write_b32 a79, v107
s_branch LP42_%=b
LP42_%=:
s_sleep 1
LP42_%=b:
s_memrealtime vcc
s_waitcnt lgkmcnt(0)
s_sub_u32 %[so], vcc_lo, %[t0]
s_cmp_lt_u32 %[so], 2727
s_cbranch_scc1 LP42_%=
s_cmp_lt_u32 %[so], 2807
s_cbranch_scc0 LP42_%=f
s_waitcnt vmcnt(8)
s_cmp_lt_u32 %[so], 2757
s_cbranch_scc0 LP42_%=f
s_waitcnt vmcnt(4)
LP42_%=f:
s_mov_b32 %[so], 45056
buffer_load_dwordx4 v[200:203], %[vo], %[rv], %[so] offen offset:0 nt
v_and_b32_dpp v52, %[p1], %[hm] row_newbcast:13 row_mask:0xf bank_mask:0x1
v_and_b32_dpp v53, %[p1], %[hm] row_newbcast:13 row_mask:0xf bank_mask:0x2
v_and_b32_dpp v54, %[p1], %[hm] row_newbcast:13 row_mask:0xf bank_mask:0x4
v_and_b32_dpp v55, %[p1], %[hm] row_newbcast:13 row_mask:0xf bank_mask:0x8
s_waitcnt vmcnt(15)
s_nop 0
v_mfma_f32_16x16x32_bf16 %[acc], v[136:139], v[52:55], %[acc]
)" R"(
s_movk_i32 s94, 10048
s_nop 0
v_perm_b32 v104, v137, v136, s96
v_perm_b32 v105, v139, v138, s96
v_perm_b32 v106, v137, v136, s97
v_perm_b32 v107, v139, v138, s97
v_pk_mad_u16 v108, v136, 2, -1 op_sel_hi:[1,0,0]
v_pk_mad_u16 v109, v137, 2, -1 op_sel_hi:[1,0,0]
v_pk_mad_u16 v110, v138, 2, -1 op_sel_hi:[1,0,0]
v_pk_mad_u16 v111, v139, 2, -1 op_sel_hi:[1,0,0]
v_and_b32 v112, 0x7f7f7f7f, v104
v_and_b32 v113, 0x7f7f7f7f, v105
v_sub_u32 v112, s82, v112
v_sub_u32 v113, s82, v113
v_pk_min_u16 v108, v108, v109
v_pk_min_u16 v110, v110, v111
v_pk_min_u16 v108, v108, v110
v_bitop3_b32 v109, v112, v113, s98 bitop3:0xa8
v_pk_sub_u16 v108, s83, v108 clamp
v_cmp_ne_u32_e64 vcc, v108, v109
v_perm_b32 v112, s99, v115, v112
v_perm_b32 v113, s99, v115, v113
v_bitop3_b32 v112, v112, v104, s98 bitop3:0x8d
v_bitop3_b32 v113, v113, v105, s98 bitop3:0x8d
v_alignbit_b32 v113, v113, v113, 28
v_or_b32 v114, v112, v113
s_getpc_b64 s[92:93]
s_cbranch_vccnz Lexc
s_nop 0
v_accvgpr_write_b32 a204, v114
v_accvgpr_write_b32 a205, v106
v_accvgpr_write_b32 a206, v107
buffer_load_dwordx4 v[204:207], %[vo], %[rv], %[so] offen offset:1024 nt
v_and_b32_dpp v52, %[p1], %[hm] row_newbcast:14 row_mask:0xf bank_mask:0x1
v_and_b32_dpp v53, %[p1], %[hm] row_newbcast:14 row_mask:0xf bank_mask:0x2
v_and_b32_dpp v54, %[p1], %[hm] row_newbcast:14 row_mask:0xf bank_mask:0x4
v_and_b32_dpp v55, %[p1], %[hm] row_newbcast:14 row_mask:0xf bank_mask:0x8
s_waitcnt vmcnt(15)
s_nop 0
v_mfma_f32_16x16x32_bf16 %[acc], v[140:143], v[52:55], %[acc]
s_movk_i32 s94, 10112
s_nop 0
v_perm_b32 v104, v141, v140, s96
v_perm_b32 v105, v143, v142, s96
v_perm_b32 v106, v141, v140, s97
v_perm_b32 v107, v143, v142, s97
v_pk_mad_u16 v108, v140, 2, -1 op_sel_hi:[1,0,0]
v_pk_mad_u16 v109, v141, 2, -1 op_sel_hi:[1,0,0]
v_pk_mad_u16 v110, v142, 2, -1 op_sel_hi:[1,0,0]
v_pk_mad_u16 v111, v143, 2, -1 op_sel_hi:[1,0,0]
v_and_b32 v112, 0x7f7f7f7f, v104
v_and_b32 v113, 0x7f7f7f7f, v105
v_sub_u32 v112, s82, v112
v_sub_u32 v113, s82, v113
v_pk_min_u16 v108, v108, v109
v_pk_min_u16 v110, v110, v111
v_pk_min_u16 v108, v108, v110
v_bitop3_b32 v109, v112, v113, s98 bitop3:0xa8
v_pk_sub_u16 v108, s83, v108 clamp
v_cmp_ne_u32_e64 vcc, v108, v109
v_perm_b32 v112, s99, v115, v112
v_perm_b32 v113, s99, v115, v113
v_bitop3_b32 v112, v112, v104, s98 bitop3:0x8d
v_bitop3_b32 v113, v113, v105, s98 bitop3:0x8d
v_alignbit_b32 v113, v113, v113, 28
v_or_b32 v114, v112, v113
s_getpc_b64 s[92:93]
s_cbranch_vccnz Lexc
s_nop 0
v_accvgpr_write_b32 a207, v114
v_accvgpr_write_b32 a80, v106
v_accvgpr_write_b32 a81, v107
buffer_load_dwordx4 v[208:211], %[vo], %[rv], %[so] offen offset:2048 nt
v_and_b32_dpp v52, %[p1], %[hm] row_newbcast:15 row_mask:0xf bank_mask:0x1
v_and_b32_dpp v53, %[p1], %[hm] row_newbcast:15 row_mask:0xf bank_mask:0x2
v_and_b32_dpp v54, %[p1], %[hm] row_newbcast:15 row_mask:0xf bank_mask:0x4
v_and_b32_dpp v55, %[p1], %[hm] row_newbcast:15 row_mask:0xf bank_mask:0x8
s_waitcnt vmcnt(15)
s_nop 0
v_mfma_f32_16x16x32_bf16 %[acc], v[148:151], v[52:55], %[acc]
s_movk_i32 s94, 10176
s_nop 0
v_perm_b32 v104, v149, v148, s96
v_perm_b32 v105, v151, v150, s96
v_perm_b32 v106, v149, v148, s97
v_perm_b32 v107, v151, v150, s97
v_pk_mad_u16 v108, v148, 2, -1 op_sel_hi:[1,0,0]
v_pk_mad_u16 v109, v149, 2, -1 op_sel_hi:[1,0,0]
v_pk_mad_u16 v110, v150, 2, -1 op_sel_hi:[1,0,0]
v_pk_mad_u16 v111, v151, 2, -1 op_sel_hi:[1,0,0]
v_and_b32 v112, 0x7f7f7f7f, v104
v_and_b32 v113, 0x7f7f7f7f, v105
v_sub_u32 v112, s82, v112
v_sub_u32 v113, s82, v113
v_pk_min_u16 v108, v108, v109
v_pk_min_u16 v110, v110, v111
v_pk_min_u16 v108, v108, v110
v_bitop3_b32 v109, v112, v113, s98 bitop3:0xa8
v_pk_sub_u16 v108, s83, v108 clamp
v_cmp_ne_u32_e64 vcc, v108, v109
v_perm_b32 v112, s99, v115, v112
v_perm_b32 v113, s99, v115, v113
v_bitop3_b32 v112, v112, v104, s98 bitop3:0x8d
v_bitop3_b32 v113, v113, v105, s98 bitop3:0x8d
v_alignbit_b32 v113, v113, v113, 28
v_or_b32 v114, v112, v113
s_getpc_b64 s[92:93]
s_cbranch_vccnz Lexc
s_nop 0
v_accvgpr_write_b32 a82, v114
v_accvgpr_write_b32 a83, v106
v_accvgpr_write_b32 a84, v107
buffer_load_dwordx4 v[212:215], %[vo], %[rv], %[so] offen offset:3072 nt
v_and_b32_dpp v52, %[p2], %[hm] row_newbcast:0 row_mask:0xf bank_mask:0x1
v_and_b32_dpp v53, %[p2], %[hm] row_newbcast:0 row_mask:0xf bank_mask:0x2
v_and_b32_dpp v54, %[p2], %[hm] row_newbcast:0 row_mask:0xf bank_mask:0x4
v_and_b32_dpp v55, %[p2], %[hm] row_newbcast:0 row_mask:0xf bank_mask:0x8
s_waitcnt vmcnt(15)
s_nop 0
v_mfma_f32_16x16x32_bf16 %[acc], v[152:155], v[52:55], %[acc]
s_movk_i32 s94, 10240
s_nop 0
v_perm_b32 v104, v153, v152, s96
v_perm_b32 v105, v155, v154, s96
v_perm_b32 v106, v153, v152, s97
v_perm_b32 v107, v155, v154, s97
v_pk_mad_u16 v108, v152, 2, -1 op_sel_hi:[1,0,0]
v_pk_mad_u16 v109, v153, 2, -1 op_sel_hi:[1,0,0]
v_pk_mad_u16 v110, v154, 2, -1 op_sel_hi:[1,0,0]
v_pk_mad_u16 v111, v155, 2, -1 op_sel_hi:[1,0,0]
v_and_b32 v112, 0x7f7f7f7f, v104
v_and_b32 v113, 0x7f7f7f7f, v105
v_sub_u32 v112, s82, v112
v_sub_u32 v113, s82, v113
v_pk_min_u16 v108, v108, v109
v_pk_min_u16 v110, v110, v111
v_pk_min_u16 v108, v108, v110
v_bitop3_b32 v109, v112, v113, s98 bitop3:0xa8
v_pk_sub_u16 v108, s83, v108 clamp
v_cmp_ne_u32_e64 vcc, v108, v109
v_perm_b32 v112, s99, v115, v112
v_perm_b32 v113, s99, v115, v113
v_bitop3_b32 v112, v112, v104, s98 bitop3:0x8d
v_bitop3_b32 v113, v113, v105, s98 bitop3:0x8d
v_alignbit_b32 v113, v113, v113, 28
v_or_b32 v114, v112, v113
s_getpc_b64 s[92:93]
s_cbranch_vccnz Lexc
v_accvgpr_write_b32 a85, v114
v_accvgpr_write_b32 a86, v106
v_accvgpr_write_b32 a87, v107
s_branch LP43_%=b
LP43_%=:
s_sleep 1
LP43_%=b:
s_memrealtime vcc
s_waitcnt lgkmcnt(0)
s_sub_u32 %[so], vcc_lo, %[t0]
s_cmp_lt_u32 %[so], 2789
s_cbranch_scc1 LP43_%=
s_cmp_lt_u32 %[so], 2869
s_cbranch_scc0 LP43_%=f
s_waitcnt vmcnt(8)
s_cmp_lt_u32 %[so], 2819
s_cbranch_scc0 LP43_%=f
s_waitcnt vmcnt(4)
LP43_%=f:
s_mov_b32 %[so], 49152
buffer_load_dwordx4 v[216:219], %[vo], %[rv], %[so] offen offset:0 nt
v_and_b32_dpp v52, %[p2], %[hm] row_newbcast:1 row_mask:0xf bank_mask:0x1
v_and_b32_dpp v53, %[p2], %[hm] row_newbcast:1 row_mask:0xf bank_mask:0x2
v_and_b32_dpp v54, %[p2], %[hm] row_newbcast:1 row_mask:0xf bank_mask:0x4
v_and_b32_dpp v55, %[p2], %[hm] row_newbcast:1 row_mask:0xf bank_mask:0x8
s_waitcnt vmcnt(15)
s_nop 0
v_mfma_f32_16x16x32_bf16 %[acc], v[156:159], v[52:55], %[acc]
s_movk_i32 s94, 10304
s_nop 0
v_perm_b32 v104, v157, v156, s96
v_perm_b32 v105, v159, v158, s96
v_perm_b32 v106, v157, v156, s97
v_perm_b32 v107, v159, v158, s97
v_pk_mad_u16 v108, v156, 2, -1 op_sel_hi:[1,0,0]
v_pk_mad_u16 v109, v157, 2, -1 op_sel_hi:[1,0,0]
v_pk_mad_u16 v110, v158, 2, -1 op_sel_hi:[1,0,0]
v_pk_mad_u16 v111, v159, 2, -1 op_sel_hi:[1,0,0]
v_and_b32 v112, 0x7f7f7f7f, v104
v_and_b32 v113, 0x7f7f7f7f, v105
v_sub_u32 v112, s82, v112
v_sub_u32 v113, s82, v113
v_pk_min_u16 v108, v108, v109
v_pk_min_u16 v110, v110, v111
v_pk_min_u16 v108, v108, v110
v_bitop3_b32 v109, v112, v113, s98 bitop3:0xa8
v_pk_sub_u16 v108, s83, v108 clamp
v_cmp_ne_u32_e64 vcc, v108, v109
v_perm_b32 v112, s99, v115, v112
v_perm_b32 v113, s99, v115, v113
v_bitop3_b32 v112, v112, v104, s98 bitop3:0x8d
v_bitop3_b32 v113, v113, v105, s98 bitop3:0x8d
v_alignbit_b32 v113, v113, v113, 28
v_or_b32 v114, v112, v113
s_getpc_b64 s[92:93]
s_cbranch_vccnz Lexc
s_nop 0
v_accvgpr_write_b32 a208, v114
v_accvgpr_write_b32 a209, v106
v_accvgpr_write_b32 a210, v107
buffer_load_dwordx4 v[220:223], %[vo], %[rv], %[so] offen offset:1024 nt
v_and_b32_dpp v52, %[p2], %[hm] row_newbcast:2 row_mask:0xf bank_mask:0x1
v_and_b32_dpp v53, %[p2], %[hm] row_newbcast:2 row_mask:0xf bank_mask:0x2
v_and_b32_dpp v54, %[p2], %[hm] row_newbcast:2 row_mask:0xf bank_mask:0x4
v_and_b32_dpp v55, %[p2], %[hm] row_newbcast:2 row_mask:0xf bank_mask:0x8
s_waitcnt vmcnt(15)
s_nop 0
v_mfma_f32_16x16x32_bf16 %[acc], v[160:163], v[52:55], %[acc]
s_movk_i32 s94, 10368
s_nop 0
v_perm_b32 v104, v161, v160, s96
v_perm_b32 v105, v163, v162, s96
v_perm_b32 v106, v161, v160, s97
v_perm_b32 v107, v163, v162, s97
v_pk_mad_u16 v108, v160, 2, -1 op_sel_hi:[1,0,0]
v_pk_mad_u16 v109, v161, 2, -1 op_sel_hi:[1,0,0]
v_pk_mad_u16 v110, v162, 2, -1 op_sel_hi:[1,0,0]
v_pk_mad_u16 v111, v163, 2, -1 op_sel_hi:[1,0,0]
v_and_b32 v112, 0x7f7f7f7f, v104
v_and_b32 v113, 0x7f7f7f7f, v105
v_sub_u32 v112, s82, v112
v_sub_u32 v113, s82, v113
v_pk_min_u16 v108, v108, v109
v_pk_min_u16 v110, v110, v111
v_pk_min_u16 v108, v108, v110
v_bitop3_b32 v109, v112, v113, s98 bitop3:0xa8
v_pk_sub_u16 v108, s83, v108 clamp
v_cmp_ne_u32_e64 vcc, v108, v109
v_perm_b32 v112, s99, v115, v112
v_perm_b32 v113, s99, v115, v113
v_bitop3_b32 v112, v112, v104, s98 bitop3:0x8d
v_bitop3_b32 v113, v113, v105, s98 bitop3:0x8d
v_alignbit_b32 v113, v113, v113, 28
v_or_b32 v114, v112, v113
s_getpc_b64 s[92:93]
s_cbranch_vccnz Lexc
s_nop 0
v_accvgpr_write_b32 a211, v114
v_accvgpr_write_b32 a88, v106
v_accvgpr_write_b32 a89, v107
buffer_load_dwordx4 v[224:227], %[vo], %[rv], %[so] offen offset:2048 nt
v_and_b32_dpp v52, %[p2], %[hm] row_newbcast:3 row_mask:0xf bank_mask:0x1
v_and_b32_dpp v53, %[p2], %[hm] row_newbcast:3 row_mask:0xf bank_mask:0x2
v_and_b32_dpp v54, %[p2], %[hm] row_newbcast:3 row_mask:0xf bank_mask:0x4
v_and_b32_dpp v55, %[p2], %[hm] row_newbcast:3 row_mask:0xf bank_mask:0x8
s_waitcnt vmcnt(15)
s_nop 0
v_mfma_f32_16x16x32_bf16 %[acc], v[164:167], v[52:55], %[acc]
s_movk_i32 s94, 10432
s_nop 0
v_perm_b32 v104, v165, v164, s96
v_perm_b32 v105, v167, v166, s96
v_perm_b32 v106, v165, v164, s97
v_perm_b32 v107, v167, v166, s97
v_pk_mad_u16 v108, v164, 2, -1 op_sel_hi:[1,0,0]
v_pk_mad_u16 v109, v165, 2, -1 op_sel_hi:[1,0,0]
v_pk_mad_u16 v110, v166, 2, -1 op_sel_hi:[1,0,0]
v_pk_mad_u16 v111, v167, 2, -1 op_sel_hi:[1,0,0]
v_and_b32 v112, 0x7f7f7f7f, v104
v_and_b32 v113, 0x7f7f7f7f, v105
v_sub_u32 v112, s82, v112
v_sub_u32 v113, s82, v113
v_pk_min_u16 v108, v108, v109
v_pk_min_u16 v110, v110, v111
v_pk_min_u16 v108, v108, v110
v_bitop3_b32 v109, v112, v113, s98 bitop3:0xa8
v_pk_sub_u16 v108, s83, v108 clamp
v_cmp_ne_u32_e64 vcc, v108, v109
v_perm_b32 v112, s99, v115, v112
v_perm_b32 v113, s99, v115, v113
v_bitop3_b32 v112, v112, v104, s98 bitop3:0x8d
v_bitop3_b32 v113, v113, v105, s98 bitop3:0x8d
v_alignbit_b32 v113, v113, v113, 28
v_or_b32 v114, v112, v113
s_getpc_b64 s[92:93]
s_cbranch_vccnz Lexc
s_nop 0
v_accvgpr_write_b32 a90, v114
v_accvgpr_write_b32 a91, v106
v_accvgpr_write_b32 a96, v107
buffer_load_dwordx4 v[228:231], %[vo], %[rv], %[so] offen offset:3072 nt
v_and_b32_dpp v52, %[p2], %[hm] row_newbcast:4 row_mask:0xf bank_mask:0x1
v_and_b32_dpp v53, %[p2], %[hm] row_newbcast:4 row_mask:0xf bank_mask:0x2
v_and_b32_dpp v54, %[p2], %[hm] row_newbcast:4 row_mask:0xf bank_mask:0x4
v_and_b32_dpp v55, %[p2], %[hm] row_newbcast:4 row_mask:0xf bank_mask:0x8
s_waitcnt vmcnt(15)
s_nop 0
v_mfma_f32_16x16x32_bf16 %[acc], v[168:171], v[52:55], %[acc]
s_movk_i32 s94, 10496
s_nop 0
v_perm_b32 v104, v169, v168, s96
v_perm_b32 v105, v171, v170, s96
v_perm_b32 v106, v169, v168, s97
v_perm_b32 v107, v171, v170, s97
v_pk_mad_u16 v108, v168, 2, -1 op_sel_hi:[1,0,0]
v_pk_mad_u16 v109, v169, 2, -1 op_sel_hi:[1,0,0]
v_pk_mad_u16 v110, v170, 2, -1 op_sel_hi:[1,0,0]
v_pk_mad_u16 v111, v171, 2, -1 op_sel_hi:[1,0,0]
v_and_b32 v112, 0x7f7f7f7f, v104
v_and_b32 v113, 0x7f7f7f7f, v105
v_sub_u32 v112, s82, v112
v_sub_u32 v113, s82, v113
v_pk_min_u16 v108, v108, v109
v_pk_min_u16 v110, v110, v111
v_pk_min_u16 v108, v108, v110
v_bitop3_b32 v109, v112, v113, s98 bitop3:0xa8
v_pk_sub_u16 v108, s83, v108 clamp
v_cmp_ne_u32_e64 vcc, v108, v109
v_perm_b32 v112, s99, v115, v112
v_perm_b32 v113, s99, v115, v113
v_bitop3_b32 v112, v112, v104, s98 bitop3:0x8d
v_bitop3_b32 v113, v113, v105, s98 bitop3:0x8d
v_alignbit_b32 v113, v113, v113, 28
v_or_b32 v114, v112, v113
s_getpc_b64 s[92:93]
s_cbranch_vccnz Lexc
v_accvgpr_write_b32 a97, v114
v_accvgpr_write_b32 a98, v106
v_accvgpr_write_b32 a99, v107
s_branch LP44_%=b
LP44_%=:
s_sleep 1
LP44_%=b:
s_memrealtime vcc
s_waitcnt lgkmcnt(0)
s_sub_u32 %[so], vcc_lo, %[t0]
)" R"(
s_cmp_lt_u32 %[so], 2852
s_cbranch_scc1 LP44_%=
s_cmp_lt_u32 %[so], 2932
s_cbranch_scc0 LP44_%=f
s_waitcnt vmcnt(8)
s_cmp_lt_u32 %[so], 2882
s_cbranch_scc0 LP44_%=f
s_waitcnt vmcnt(4)
LP44_%=f:
s_mov_b32 %[so], 53248
buffer_load_dwordx4 v[232:235], %[vo], %[rv], %[so] offen offset:0 nt
v_and_b32_dpp v52, %[p2], %[hm] row_newbcast:5 row_mask:0xf bank_mask:0x1
v_and_b32_dpp v53, %[p2], %[hm] row_newbcast:5 row_mask:0xf bank_mask:0x2
v_and_b32_dpp v54, %[p2], %[hm] row_newbcast:5 row_mask:0xf bank_mask:0x4
v_and_b32_dpp v55, %[p2], %[hm] row_newbcast:5 row_mask:0xf bank_mask:0x8
s_waitcnt vmcnt(15)
s_nop 0
v_mfma_f32_16x16x32_bf16 %[acc], v[172:175], v[52:55], %[acc]
s_movk_i32 s94, 10560
s_nop 0
v_perm_b32 v104, v173, v172, s96
v_perm_b32 v105, v175, v174, s96
v_perm_b32 v106, v173, v172, s97
v_perm_b32 v107, v175, v174, s97
v_pk_mad_u16 v108, v172, 2, -1 op_sel_hi:[1,0,0]
v_pk_mad_u16 v109, v173, 2, -1 op_sel_hi:[1,0,0]
v_pk_mad_u16 v110, v174, 2, -1 op_sel_hi:[1,0,0]
v_pk_mad_u16 v111, v175, 2, -1 op_sel_hi:[1,0,0]
v_and_b32 v112, 0x7f7f7f7f, v104
v_and_b32 v113, 0x7f7f7f7f, v105
v_sub_u32 v112, s82, v112
v_sub_u32 v113, s82, v113
v_pk_min_u16 v108, v108, v109
v_pk_min_u16 v110, v110, v111
v_pk_min_u16 v108, v108, v110
v_bitop3_b32 v109, v112, v113, s98 bitop3:0xa8
v_pk_sub_u16 v108, s83, v108 clamp
v_cmp_ne_u32_e64 vcc, v108, v109
v_perm_b32 v112, s99, v115, v112
v_perm_b32 v113, s99, v115, v113
v_bitop3_b32 v112, v112, v104, s98 bitop3:0x8d
v_bitop3_b32 v113, v113, v105, s98 bitop3:0x8d
v_alignbit_b32 v113, v113, v113, 28
v_or_b32 v114, v112, v113
s_getpc_b64 s[92:93]
s_cbranch_vccnz Lexc
s_nop 0
v_accvgpr_write_b32 a104, v114
v_accvgpr_write_b32 a105, v106
v_accvgpr_write_b32 a106, v107
buffer_load_dwordx4 v[236:239], %[vo], %[rv], %[so] offen offset:1024 nt
v_and_b32_dpp v52, %[p2], %[hm] row_newbcast:6 row_mask:0xf bank_mask:0x1
v_and_b32_dpp v53, %[p2], %[hm] row_newbcast:6 row_mask:0xf bank_mask:0x2
v_and_b32_dpp v54, %[p2], %[hm] row_newbcast:6 row_mask:0xf bank_mask:0x4
v_and_b32_dpp v55, %[p2], %[hm] row_newbcast:6 row_mask:0xf bank_mask:0x8
s_waitcnt vmcnt(15)
s_nop 0
v_mfma_f32_16x16x32_bf16 %[acc], v[176:179], v[52:55], %[acc]
s_movk_i32 s94, 10624
s_nop 0
v_perm_b32 v104, v177, v176, s96
v_perm_b32 v105, v179, v178, s96
v_perm_b32 v106, v177, v176, s97
v_perm_b32 v107, v179, v178, s97
v_pk_mad_u16 v108, v176, 2, -1 op_sel_hi:[1,0,0]
v_pk_mad_u16 v109, v177, 2, -1 op_sel_hi:[1,0,0]
v_pk_mad_u16 v110, v178, 2, -1 op_sel_hi:[1,0,0]
v_pk_mad_u16 v111, v179, 2, -1 op_sel_hi:[1,0,0]
v_and_b32 v112, 0x7f7f7f7f, v104
v_and_b32 v113, 0x7f7f7f7f, v105
v_sub_u32 v112, s82, v112
v_sub_u32 v113, s82, v113
v_pk_min_u16 v108, v108, v109
v_pk_min_u16 v110, v110, v111
v_pk_min_u16 v108, v108, v110
v_bitop3_b32 v109, v112, v113, s98 bitop3:0xa8
v_pk_sub_u16 v108, s83, v108 clamp
v_cmp_ne_u32_e64 vcc, v108, v109
v_perm_b32 v112, s99, v115, v112
v_perm_b32 v113, s99, v115, v113
v_bitop3_b32 v112, v112, v104, s98 bitop3:0x8d
v_bitop3_b32 v113, v113, v105, s98 bitop3:0x8d
v_alignbit_b32 v113, v113, v113, 28
v_or_b32 v114, v112, v113
s_getpc_b64 s[92:93]
s_cbranch_vccnz Lexc
s_nop 0
v_accvgpr_write_b32 a107, v114
v_accvgpr_write_b32 a116, v106
v_accvgpr_write_b32 a117, v107
buffer_load_dwordx4 v[240:243], %[vo], %[rv], %[so] offen offset:2048 nt
v_and_b32_dpp v52, %[p2], %[hm] row_newbcast:7 row_mask:0xf bank_mask:0x1
v_and_b32_dpp v53, %[p2], %[hm] row_newbcast:7 row_mask:0xf bank_mask:0x2
v_and_b32_dpp v54, %[p2], %[hm] row_newbcast:7 row_mask:0xf bank_mask:0x4
v_and_b32_dpp v55, %[p2], %[hm] row_newbcast:7 row_mask:0xf bank_mask:0x8
s_waitcnt vmcnt(15)
s_nop 0
v_mfma_f32_16x16x32_bf16 %[acc], v[180:183], v[52:55], %[acc]
s_movk_i32 s94, 10688
s_nop 0
v_perm_b32 v104, v181, v180, s96
v_perm_b32 v105, v183, v182, s96
v_perm_b32 v106, v181, v180, s97
v_perm_b32 v107, v183, v182, s97
v_pk_mad_u16 v108, v180, 2, -1 op_sel_hi:[1,0,0]
v_pk_mad_u16 v109, v181, 2, -1 op_sel_hi:[1,0,0]
v_pk_mad_u16 v110, v182, 2, -1 op_sel_hi:[1,0,0]
v_pk_mad_u16 v111, v183, 2, -1 op_sel_hi:[1,0,0]
v_and_b32 v112, 0x7f7f7f7f, v104
v_and_b32 v113, 0x7f7f7f7f, v105
v_sub_u32 v112, s82, v112
v_sub_u32 v113, s82, v113
v_pk_min_u16 v108, v108, v109
v_pk_min_u16 v110, v110, v111
v_pk_min_u16 v108, v108, v110
v_bitop3_b32 v109, v112, v113, s98 bitop3:0xa8
v_pk_sub_u16 v108, s83, v108 clamp
v_cmp_ne_u32_e64 vcc, v108, v109
v_perm_b32 v112, s99, v115, v112
v_perm_b32 v113, s99, v115, v113
v_bitop3_b32 v112, v112, v104, s98 bitop3:0x8d
v_bitop3_b32 v113, v113, v105, s98 bitop3:0x8d
v_alignbit_b32 v113, v113, v113, 28
v_or_b32 v114, v112, v113
s_getpc_b64 s[92:93]
s_cbranch_vccnz Lexc
s_nop 0
v_accvgpr_write_b32 a118, v114
v_accvgpr_write_b32 a119, v106
v_accvgpr_write_b32 a124, v107
buffer_load_dwordx4 v[244:247], %[vo], %[rv], %[so] offen offset:3072 nt
v_and_b32_dpp v52, %[p2], %[hm] row_newbcast:8 row_mask:0xf bank_mask:0x1
v_and_b32_dpp v53, %[p2], %[hm] row_newbcast:8 row_mask:0xf bank_mask:0x2
v_and_b32_dpp v54, %[p2], %[hm] row_newbcast:8 row_mask:0xf bank_mask:0x4
v_and_b32_dpp v55, %[p2], %[hm] row_newbcast:8 row_mask:0xf bank_mask:0x8
s_waitcnt vmcnt(15)
s_nop 0
v_mfma_f32_16x16x32_bf16 %[acc], v[184:187], v[52:55], %[acc]
s_movk_i32 s94, 10752
s_nop 0
v_perm_b32 v104, v185, v184, s96
v_perm_b32 v105, v187, v186, s96
v_perm_b32 v106, v185, v184, s97
v_perm_b32 v107, v187, v186, s97
v_pk_mad_u16 v108, v184, 2, -1 op_sel_hi:[1,0,0]
v_pk_mad_u16 v109, v185, 2, -1 op_sel_hi:[1,0,0]
v_pk_mad_u16 v110, v186, 2, -1 op_sel_hi:[1,0,0]
v_pk_mad_u16 v111, v187, 2, -1 op_sel_hi:[1,0,0]
v_and_b32 v112, 0x7f7f7f7f, v104
v_and_b32 v113, 0x7f7f7f7f, v105
v_sub_u32 v112, s82, v112
v_sub_u32 v113, s82, v113
v_pk_min_u16 v108, v108, v109
v_pk_min_u16 v110, v110, v111
v_pk_min_u16 v108, v108, v110
v_bitop3_b32 v109, v112, v113, s98 bitop3:0xa8
v_pk_sub_u16 v108, s83, v108 clamp
v_cmp_ne_u32_e64 vcc, v108, v109
v_perm_b32 v112, s99, v115, v112
v_perm_b32 v113, s99, v115, v113
v_bitop3_b32 v112, v112, v104, s98 bitop3:0x8d
v_bitop3_b32 v113, v113, v105, s98 bitop3:0x8d
v_alignbit_b32 v113, v113, v113, 28
v_or_b32 v114, v112, v113
s_getpc_b64 s[92:93]
s_cbranch_vccnz Lexc
v_accvgpr_write_b32 a125, v114
v_accvgpr_write_b32 a126, v106
v_accvgpr_write_b32 a127, v107
s_branch LP45_%=b
LP45_%=:
s_sleep 1
LP45_%=b:
s_memrealtime vcc
s_waitcnt lgkmcnt(0)
s_sub_u32 %[so], vcc_lo, %[t0]
s_cmp_lt_u32 %[so], 2914
s_cbranch_scc1 LP45_%=
s_cmp_lt_u32 %[so], 2994
s_cbranch_scc0 LP45_%=f
s_waitcnt vmcnt(8)
s_cmp_lt_u32 %[so], 2944
s_cbranch_scc0 LP45_%=f
s_waitcnt vmcnt(4)
LP45_%=f:
s_mov_b32 %[so], 57344
buffer_load_dwordx4 v[132:135], %[vo], %[rv], %[so] offen offset:0 nt
v_and_b32_dpp v52, %[p2], %[hm] row_newbcast:9 row_mask:0xf bank_mask:0x1
v_and_b32_dpp v53, %[p2], %[hm] row_newbcast:9 row_mask:0xf bank_mask:0x2
v_and_b32_dpp v54, %[p2], %[hm] row_newbcast:9 row_mask:0xf bank_mask:0x4
v_and_b32_dpp v55, %[p2], %[hm] row_newbcast:9 row_mask:0xf bank_mask:0x8
s_waitcnt vmcnt(15)
s_nop 0
v_mfma_f32_16x16x32_bf16 %[acc], v[188:191], v[52:55], %[acc]
s_movk_i32 s94, 10816
s_nop 0
v_perm_b32 v104, v189, v188, s96
v_perm_b32 v105, v191, v190, s96
v_perm_b32 v106, v189, v188, s97
v_perm_b32 v107, v191, v190, s97
v_pk_mad_u16 v108, v188, 2, -1 op_sel_hi:[1,0,0]
v_pk_mad_u16 v109, v189, 2, -1 op_sel_hi:[1,0,0]
v_pk_mad_u16 v110, v190, 2, -1 op_sel_hi:[1,0,0]
v_pk_mad_u16 v111, v191, 2, -1 op_sel_hi:[1,0,0]
v_and_b32 v112, 0x7f7f7f7f, v104
v_and_b32 v113, 0x7f7f7f7f, v105
v_sub_u32 v112, s82, v112
v_sub_u32 v113, s82, v113
v_pk_min_u16 v108, v108, v109
v_pk_min_u16 v110, v110, v111
v_pk_min_u16 v108, v108, v110
v_bitop3_b32 v109, v112, v113, s98 bitop3:0xa8
v_pk_sub_u16 v108, s83, v108 clamp
v_cmp_ne_u32_e64 vcc, v108, v109
v_perm_b32 v112, s99, v115, v112
v_perm_b32 v113, s99, v115, v113
v_bitop3_b32 v112, v112, v104, s98 bitop3:0x8d
v_bitop3_b32 v113, v113, v105, s98 bitop3:0x8d
v_alignbit_b32 v113, v113, v113, 28
v_or_b32 v114, v112, v113
s_getpc_b64 s[92:93]
s_cbranch_vccnz Lexc
s_nop 0
v_accvgpr_write_b32 a144, v114
v_accvgpr_write_b32 a145, v106
v_accvgpr_write_b32 a146, v107
buffer_load_dwordx4 v[136:139], %[vo], %[rv], %[so] offen offset:1024 nt
v_and_b32_dpp v52, %[p2], %[hm] row_newbcast:10 row_mask:0xf bank_mask:0x1
v_and_b32_dpp v53, %[p2], %[hm] row_newbcast:10 row_mask:0xf bank_mask:0x2
v_and_b32_dpp v54, %[p2], %[hm] row_newbcast:10 row_mask:0xf bank_mask:0x4
v_and_b32_dpp v55, %[p2], %[hm] row_newbcast:10 row_mask:0xf bank_mask:0x8
s_waitcnt vmcnt(15)
s_nop 0
v_mfma_f32_16x16x32_bf16 %[acc], v[192:195], v[52:55], %[acc]
s_movk_i32 s94, 10880
s_nop 0
v_perm_b32 v104, v193, v192, s96
v_perm_b32 v105, v195, v194, s96
v_perm_b32 v106, v193, v192, s97
v_perm_b32 v107, v195, v194, s97
v_pk_mad_u16 v108, v192, 2, -1 op_sel_hi:[1,0,0]
v_pk_mad_u16 v109, v193, 2, -1 op_sel_hi:[1,0,0]
v_pk_mad_u16 v110, v194, 2, -1 op_sel_hi:[1,0,0]
v_pk_mad_u16 v111, v195, 2, -1 op_sel_hi:[1,0,0]
v_and_b32 v112, 0x7f7f7f7f, v104
v_and_b32 v113, 0x7f7f7f7f, v105
v_sub_u32 v112, s82, v112
v_sub_u32 v113, s82, v113
v_pk_min_u16 v108, v108, v109
v_pk_min_u16 v110, v110, v111
v_pk_min_u16 v108, v108, v110
v_bitop3_b32 v109, v112, v113, s98 bitop3:0xa8
v_pk_sub_u16 v108, s83, v108 clamp
v_cmp_ne_u32_e64 vcc, v108, v109
v_perm_b32 v112, s99, v115, v112
v_perm_b32 v113, s99, v115, v113
v_bitop3_b32 v112, v112, v104, s98 bitop3:0x8d
v_bitop3_b32 v113, v113, v105, s98 bitop3:0x8d
v_alignbit_b32 v113, v113, v113, 28
v_or_b32 v114, v112, v113
s_getpc_b64 s[92:93]
s_cbranch_vccnz Lexc
s_nop 0
v_accvgpr_write_b32 a147, v114
v_accvgpr_write_b32 a160, v106
v_accvgpr_write_b32 a161, v107
buffer_load_dwordx4 v[140:143], %[vo], %[rv], %[so] offen offset:2048 nt
v_and_b32_dpp v52, %[p2], %[hm] row_newbcast:11 row_mask:0xf bank_mask:0x1
v_and_b32_dpp v53, %[p2], %[hm] row_newbcast:11 row_mask:0xf bank_mask:0x2
v_and_b32_dpp v54, %[p2], %[hm] row_newbcast:11 row_mask:0xf bank_mask:0x4
v_and_b32_dpp v55, %[p2], %[hm] row_newbcast:11 row_mask:0xf bank_mask:0x8
s_waitcnt vmcnt(15)
s_nop 0
v_mfma_f32_16x16x32_bf16 %[acc], v[196:199], v[52:55], %[acc]
s_movk_i32 s94, 10944
s_nop 0
v_perm_b32 v104, v197, v196, s96
v_perm_b32 v105, v199, v198, s96
v_perm_b32 v106, v197, v196, s97
v_perm_b32 v107, v199, v198, s97
v_pk_mad_u16 v108, v196, 2, -1 op_sel_hi:[1,0,0]
v_pk_mad_u16 v109, v197, 2, -1 op_sel_hi:[1,0,0]
v_pk_mad_u16 v110, v198, 2, -1 op_sel_hi:[1,0,0]
v_pk_mad_u16 v111, v199, 2, -1 op_sel_hi:[1,0,0]
v_and_b32 v112, 0x7f7f7f7f, v104
v_and_b32 v113, 0x7f7f7f7f, v105
v_sub_u32 v112, s82, v112
v_sub_u32 v113, s82, v113
v_pk_min_u16 v108, v108, v109
v_pk_min_u16 v110, v110, v111
v_pk_min_u16 v108, v108, v110
v_bitop3_b32 v109, v112, v113, s98 bitop3:0xa8
v_pk_sub_u16 v108, s83, v108 clamp
v_cmp_ne_u32_e64 vcc, v108, v109
v_perm_b32 v112, s99, v115, v112
v_perm_b32 v113, s99, v115, v113
v_bitop3_b32 v112, v112, v104, s98 bitop3:0x8d
v_bitop3_b32 v113, v113, v105, s98 bitop3:0x8d
v_alignbit_b32 v113, v113, v113, 28
v_or_b32 v114, v112, v113
s_getpc_b64 s[92:93]
s_cbranch_vccnz Lexc
v_accvgpr_write_b32 a162, v114
v_accvgpr_write_b32 a163, v106
v_mov_b32 v145, v107
buffer_load_dwordx4 v[148:151], %[vo], %[rv], %[so] offen offset:3072 nt
v_and_b32_dpp v52, %[p2], %[hm] row_newbcast:12 row_mask:0xf bank_mask:0x1
v_and_b32_dpp v53, %[p2], %[hm] row_newbcast:12 row_mask:0xf bank_mask:0x2
v_and_b32_dpp v54, %[p2], %[hm] row_newbcast:12 row_mask:0xf bank_mask:0x4
v_and_b32_dpp v55, %[p2], %[hm] row_newbcast:12 row_mask:0xf bank_mask:0x8
s_waitcnt vmcnt(15)
s_nop 0
v_mfma_f32_16x16x32_bf16 %[acc], v[200:203], v[52:55], %[acc]
s_movk_i32 s94, 11008
s_nop 0
v_perm_b32 v104, v201, v200, s96
v_perm_b32 v105, v203, v202, s96
v_perm_b32 v106, v201, v200, s97
v_perm_b32 v107, v203, v202, s97
v_pk_mad_u16 v108, v200, 2, -1 op_sel_hi:[1,0,0]
v_pk_mad_u16 v109, v201, 2, -1 op_sel_hi:[1,0,0]
v_pk_mad_u16 v110, v202, 2, -1 op_sel_hi:[1,0,0]
v_pk_mad_u16 v111, v203, 2, -1 op_sel_hi:[1,0,0]
v_and_b32 v112, 0x7f7f7f7f, v104
v_and_b32 v113, 0x7f7f7f7f, v105
v_sub_u32 v112, s82, v112
v_sub_u32 v113, s82, v113
)" R"(
v_pk_min_u16 v108, v108, v109
v_pk_min_u16 v110, v110, v111
v_pk_min_u16 v108, v108, v110
v_bitop3_b32 v109, v112, v113, s98 bitop3:0xa8
v_pk_sub_u16 v108, s83, v108 clamp
v_cmp_ne_u32_e64 vcc, v108, v109
v_perm_b32 v112, s99, v115, v112
v_perm_b32 v113, s99, v115, v113
v_bitop3_b32 v112, v112, v104, s98 bitop3:0x8d
v_bitop3_b32 v113, v113, v105, s98 bitop3:0x8d
v_alignbit_b32 v113, v113, v113, 28
v_or_b32 v114, v112, v113
s_getpc_b64 s[92:93]
s_cbranch_vccnz Lexc
v_mov_b32 v146, v114
v_mov_b32 v147, v106
v_mov_b32 v152, v107
s_branch LP46_%=b
LP46_%=:
s_sleep 1
LP46_%=b:
s_memrealtime vcc
s_waitcnt lgkmcnt(0)
s_sub_u32 %[so], vcc_lo, %[t0]
s_cmp_lt_u32 %[so], 2976
s_cbranch_scc1 LP46_%=
s_cmp_lt_u32 %[so], 3056
s_cbranch_scc0 LP46_%=f
s_waitcnt vmcnt(8)
s_cmp_lt_u32 %[so], 3006
s_cbranch_scc0 LP46_%=f
s_waitcnt vmcnt(4)
s_nop 0
LP46_%=f:
s_mov_b32 %[so], 61440
buffer_load_dwordx4 v[156:159], %[vo], %[rv], %[so] offen offset:0 nt
v_and_b32_dpp v52, %[p2], %[hm] row_newbcast:13 row_mask:0xf bank_mask:0x1
v_and_b32_dpp v53, %[p2], %[hm] row_newbcast:13 row_mask:0xf bank_mask:0x2
v_and_b32_dpp v54, %[p2], %[hm] row_newbcast:13 row_mask:0xf bank_mask:0x4
v_and_b32_dpp v55, %[p2], %[hm] row_newbcast:13 row_mask:0xf bank_mask:0x8
s_waitcnt vmcnt(15)
s_nop 0
v_mfma_f32_16x16x32_bf16 %[acc], v[204:207], v[52:55], %[acc]
s_movk_i32 s94, 11072
s_nop 0
v_perm_b32 v104, v205, v204, s96
v_perm_b32 v105, v207, v206, s96
v_perm_b32 v106, v205, v204, s97
v_perm_b32 v107, v207, v206, s97
v_pk_mad_u16 v108, v204, 2, -1 op_sel_hi:[1,0,0]
v_pk_mad_u16 v109, v205, 2, -1 op_sel_hi:[1,0,0]
v_pk_mad_u16 v110, v206, 2, -1 op_sel_hi:[1,0,0]
v_pk_mad_u16 v111, v207, 2, -1 op_sel_hi:[1,0,0]
v_and_b32 v112, 0x7f7f7f7f, v104
v_and_b32 v113, 0x7f7f7f7f, v105
v_sub_u32 v112, s82, v112
v_sub_u32 v113, s82, v113
v_pk_min_u16 v108, v108, v109
v_pk_min_u16 v110, v110, v111
v_pk_min_u16 v108, v108, v110
v_bitop3_b32 v109, v112, v113, s98 bitop3:0xa8
v_pk_sub_u16 v108, s83, v108 clamp
v_cmp_ne_u32_e64 vcc, v108, v109
v_perm_b32 v112, s99, v115, v112
v_perm_b32 v113, s99, v115, v113
v_bitop3_b32 v112, v112, v104, s98 bitop3:0x8d
v_bitop3_b32 v113, v113, v105, s98 bitop3:0x8d
v_alignbit_b32 v113, v113, v113, 28
v_or_b32 v114, v112, v113
s_getpc_b64 s[92:93]
s_cbranch_vccnz Lexc
v_mov_b32 v153, v114
v_mov_b32 v154, v106
v_mov_b32 v155, v107
buffer_load_dwordx4 v[160:163], %[vo], %[rv], %[so] offen offset:1024 nt
v_and_b32_dpp v52, %[p2], %[hm] row_newbcast:14 row_mask:0xf bank_mask:0x1
v_and_b32_dpp v53, %[p2], %[hm] row_newbcast:14 row_mask:0xf bank_mask:0x2
v_and_b32_dpp v54, %[p2], %[hm] row_newbcast:14 row_mask:0xf bank_mask:0x4
v_and_b32_dpp v55, %[p2], %[hm] row_newbcast:14 row_mask:0xf bank_mask:0x8
s_waitcnt vmcnt(15)
s_nop 0
v_mfma_f32_16x16x32_bf16 %[acc], v[208:211], v[52:55], %[acc]
s_movk_i32 s94, 11136
s_nop 0
v_perm_b32 v104, v209, v208, s96
v_perm_b32 v105, v211, v210, s96
v_perm_b32 v106, v209, v208, s97
v_perm_b32 v107, v211, v210, s97
v_pk_mad_u16 v108, v208, 2, -1 op_sel_hi:[1,0,0]
v_pk_mad_u16 v109, v209, 2, -1 op_sel_hi:[1,0,0]
v_pk_mad_u16 v110, v210, 2, -1 op_sel_hi:[1,0,0]
v_pk_mad_u16 v111, v211, 2, -1 op_sel_hi:[1,0,0]
v_and_b32 v112, 0x7f7f7f7f, v104
v_and_b32 v113, 0x7f7f7f7f, v105
v_sub_u32 v112, s82, v112
v_sub_u32 v113, s82, v113
v_pk_min_u16 v108, v108, v109
v_pk_min_u16 v110, v110, v111
v_pk_min_u16 v108, v108, v110
v_bitop3_b32 v109, v112, v113, s98 bitop3:0xa8
v_pk_sub_u16 v108, s83, v108 clamp
v_cmp_ne_u32_e64 vcc, v108, v109
v_perm_b32 v112, s99, v115, v112
v_perm_b32 v113, s99, v115, v113
v_bitop3_b32 v112, v112, v104, s98 bitop3:0x8d
v_bitop3_b32 v113, v113, v105, s98 bitop3:0x8d
v_alignbit_b32 v113, v113, v113, 28
v_or_b32 v114, v112, v113
s_getpc_b64 s[92:93]
s_cbranch_vccnz Lexc
v_mov_b32 v164, v114
v_mov_b32 v165, v106
v_mov_b32 v166, v107
buffer_load_dwordx4 v[168:171], %[vo], %[rv], %[so] offen offset:2048 nt
v_and_b32_dpp v52, %[p2], %[hm] row_newbcast:15 row_mask:0xf bank_mask:0x1
v_and_b32_dpp v53, %[p2], %[hm] row_newbcast:15 row_mask:0xf bank_mask:0x2
v_and_b32_dpp v54, %[p2], %[hm] row_newbcast:15 row_mask:0xf bank_mask:0x4
v_and_b32_dpp v55, %[p2], %[hm] row_newbcast:15 row_mask:0xf bank_mask:0x8
s_waitcnt vmcnt(15)
s_nop 0
v_mfma_f32_16x16x32_bf16 %[acc], v[212:215], v[52:55], %[acc]
s_movk_i32 s94, 11200
s_nop 0
v_perm_b32 v104, v213, v212, s96
v_perm_b32 v105, v215, v214, s96
v_perm_b32 v106, v213, v212, s97
v_perm_b32 v107, v215, v214, s97
v_pk_mad_u16 v108, v212, 2, -1 op_sel_hi:[1,0,0]
v_pk_mad_u16 v109, v213, 2, -1 op_sel_hi:[1,0,0]
v_pk_mad_u16 v110, v214, 2, -1 op_sel_hi:[1,0,0]
v_pk_mad_u16 v111, v215, 2, -1 op_sel_hi:[1,0,0]
v_and_b32 v112, 0x7f7f7f7f, v104
v_and_b32 v113, 0x7f7f7f7f, v105
v_sub_u32 v112, s82, v112
v_sub_u32 v113, s82, v113
v_pk_min_u16 v108, v108, v109
v_pk_min_u16 v110, v110, v111
v_pk_min_u16 v108, v108, v110
v_bitop3_b32 v109, v112, v113, s98 bitop3:0xa8
v_pk_sub_u16 v108, s83, v108 clamp
v_cmp_ne_u32_e64 vcc, v108, v109
v_perm_b32 v112, s99, v115, v112
v_perm_b32 v113, s99, v115, v113
v_bitop3_b32 v112, v112, v104, s98 bitop3:0x8d
v_bitop3_b32 v113, v113, v105, s98 bitop3:0x8d
v_alignbit_b32 v113, v113, v113, 28
v_or_b32 v114, v112, v113
s_getpc_b64 s[92:93]
s_cbranch_vccnz Lexc
v_mov_b32 v167, v114
v_mov_b32 v172, v106
v_mov_b32 v173, v107
buffer_load_dwordx4 v[176:179], %[vo], %[rv], %[so] offen offset:3072 nt
v_and_b32_dpp v52, %[p3], %[hm] row_newbcast:0 row_mask:0xf bank_mask:0x1
v_and_b32_dpp v53, %[p3], %[hm] row_newbcast:0 row_mask:0xf bank_mask:0x2
v_and_b32_dpp v54, %[p3], %[hm] row_newbcast:0 row_mask:0xf bank_mask:0x4
v_and_b32_dpp v55, %[p3], %[hm] row_newbcast:0 row_mask:0xf bank_mask:0x8
s_waitcnt vmcnt(15)
s_nop 0
v_mfma_f32_16x16x32_bf16 %[acc], v[216:219], v[52:55], %[acc]
s_movk_i32 s94, 11264
s_nop 0
v_perm_b32 v104, v217, v216, s96
v_perm_b32 v105, v219, v218, s96
v_perm_b32 v106, v217, v216, s97
v_perm_b32 v107, v219, v218, s97
v_pk_mad_u16 v108, v216, 2, -1 op_sel_hi:[1,0,0]
v_pk_mad_u16 v109, v217, 2, -1 op_sel_hi:[1,0,0]
v_pk_mad_u16 v110, v218, 2, -1 op_sel_hi:[1,0,0]
v_pk_mad_u16 v111, v219, 2, -1 op_sel_hi:[1,0,0]
v_and_b32 v112, 0x7f7f7f7f, v104
v_and_b32 v113, 0x7f7f7f7f, v105
v_sub_u32 v112, s82, v112
v_sub_u32 v113, s82, v113
v_pk_min_u16 v108, v108, v109
v_pk_min_u16 v110, v110, v111
v_pk_min_u16 v108, v108, v110
v_bitop3_b32 v109, v112, v113, s98 bitop3:0xa8
v_pk_sub_u16 v108, s83, v108 clamp
v_cmp_ne_u32_e64 vcc, v108, v109
v_perm_b32 v112, s99, v115, v112
v_perm_b32 v113, s99, v115, v113
v_bitop3_b32 v112, v112, v104, s98 bitop3:0x8d
v_bitop3_b32 v113, v113, v105, s98 bitop3:0x8d
v_alignbit_b32 v113, v113, v113, 28
v_or_b32 v114, v112, v113
s_getpc_b64 s[92:93]
s_cbranch_vccnz Lexc
v_mov_b32 v174, v114
v_mov_b32 v175, v106
v_mov_b32 v180, v107
s_branch LP47_%=b
LP47_%=:
s_sleep 1
LP47_%=b:
s_memrealtime vcc
s_waitcnt lgkmcnt(0)
s_sub_u32 %[so], vcc_lo, %[t0]
s_cmp_lt_u32 %[so], 3039
s_cbranch_scc1 LP47_%=
s_cmp_lt_u32 %[so], 3119
s_cbranch_scc0 LP47_%=f
s_waitcnt vmcnt(8)
s_cmp_lt_u32 %[so], 3069
s_cbranch_scc0 LP47_%=f
s_waitcnt vmcnt(4)
s_nop 0
LP47_%=f:
s_mov_b32 %[so], 65536
buffer_load_dwordx4 v[184:187], %[vo], %[rv], %[so] offen offset:0 nt
v_and_b32_dpp v52, %[p3], %[hm] row_newbcast:1 row_mask:0xf bank_mask:0x1
v_and_b32_dpp v53, %[p3], %[hm] row_newbcast:1 row_mask:0xf bank_mask:0x2
v_and_b32_dpp v54, %[p3], %[hm] row_newbcast:1 row_mask:0xf bank_mask:0x4
v_and_b32_dpp v55, %[p3], %[hm] row_newbcast:1 row_mask:0xf bank_mask:0x8
s_waitcnt vmcnt(15)
s_nop 0
v_mfma_f32_16x16x32_bf16 %[acc], v[220:223], v[52:55], %[acc]
s_movk_i32 s94, 11328
s_nop 0
v_perm_b32 v104, v221, v220, s96
v_perm_b32 v105, v223, v222, s96
v_perm_b32 v106, v221, v220, s97
v_perm_b32 v107, v223, v222, s97
v_pk_mad_u16 v108, v220, 2, -1 op_sel_hi:[1,0,0]
v_pk_mad_u16 v109, v221, 2, -1 op_sel_hi:[1,0,0]
v_pk_mad_u16 v110, v222, 2, -1 op_sel_hi:[1,0,0]
v_pk_mad_u16 v111, v223, 2, -1 op_sel_hi:[1,0,0]
v_and_b32 v112, 0x7f7f7f7f, v104
v_and_b32 v113, 0x7f7f7f7f, v105
v_sub_u32 v112, s82, v112
v_sub_u32 v113, s82, v113
v_pk_min_u16 v108, v108, v109
v_pk_min_u16 v110, v110, v111
v_pk_min_u16 v108, v108, v110
v_bitop3_b32 v109, v112, v113, s98 bitop3:0xa8
v_pk_sub_u16 v108, s83, v108 clamp
v_cmp_ne_u32_e64 vcc, v108, v109
v_perm_b32 v112, s99, v115, v112
v_perm_b32 v113, s99, v115, v113
v_bitop3_b32 v112, v112, v104, s98 bitop3:0x8d
v_bitop3_b32 v113, v113, v105, s98 bitop3:0x8d
v_alignbit_b32 v113, v113, v113, 28
v_or_b32 v114, v112, v113
s_getpc_b64 s[92:93]
s_cbranch_vccnz Lexc
v_mov_b32 v181, v114
v_mov_b32 v182, v106
v_mov_b32 v183, v107
buffer_load_dwordx4 v[188:191], %[vo], %[rv], %[so] offen offset:1024 nt
v_and_b32_dpp v52, %[p3], %[hm] row_newbcast:2 row_mask:0xf bank_mask:0x1
v_and_b32_dpp v53, %[p3], %[hm] row_newbcast:2 row_mask:0xf bank_mask:0x2
v_and_b32_dpp v54, %[p3], %[hm] row_newbcast:2 row_mask:0xf bank_mask:0x4
v_and_b32_dpp v55, %[p3], %[hm] row_newbcast:2 row_mask:0xf bank_mask:0x8
s_waitcnt vmcnt(15)
s_nop 0
v_mfma_f32_16x16x32_bf16 %[acc], v[224:227], v[52:55], %[acc]
s_movk_i32 s94, 11392
s_nop 0
v_perm_b32 v104, v225, v224, s96
v_perm_b32 v105, v227, v226, s96
v_perm_b32 v106, v225, v224, s97
v_perm_b32 v107, v227, v226, s97
v_pk_mad_u16 v108, v224, 2, -1 op_sel_hi:[1,0,0]
v_pk_mad_u16 v109, v225, 2, -1 op_sel_hi:[1,0,0]
v_pk_mad_u16 v110, v226, 2, -1 op_sel_hi:[1,0,0]
v_pk_mad_u16 v111, v227, 2, -1 op_sel_hi:[1,0,0]
v_and_b32 v112, 0x7f7f7f7f, v104
v_and_b32 v113, 0x7f7f7f7f, v105
v_sub_u32 v112, s82, v112
v_sub_u32 v113, s82, v113
v_pk_min_u16 v108, v108, v109
v_pk_min_u16 v110, v110, v111
v_pk_min_u16 v108, v108, v110
v_bitop3_b32 v109, v112, v113, s98 bitop3:0xa8
v_pk_sub_u16 v108, s83, v108 clamp
v_cmp_ne_u32_e64 vcc, v108, v109
v_perm_b32 v112, s99, v115, v112
v_perm_b32 v113, s99, v115, v113
v_bitop3_b32 v112, v112, v104, s98 bitop3:0x8d
v_bitop3_b32 v113, v113, v105, s98 bitop3:0x8d
v_alignbit_b32 v113, v113, v113, 28
v_or_b32 v114, v112, v113
s_getpc_b64 s[92:93]
s_cbranch_vccnz Lexc
v_mov_b32 v192, v114
v_mov_b32 v193, v106
v_mov_b32 v194, v107
buffer_load_dwordx4 v[196:199], %[vo], %[rv], %[so] offen offset:2048 nt
v_and_b32_dpp v52, %[p3], %[hm] row_newbcast:3 row_mask:0xf bank_mask:0x1
v_and_b32_dpp v53, %[p3], %[hm] row_newbcast:3 row_mask:0xf bank_mask:0x2
v_and_b32_dpp v54, %[p3], %[hm] row_newbcast:3 row_mask:0xf bank_mask:0x4
v_and_b32_dpp v55, %[p3], %[hm] row_newbcast:3 row_mask:0xf bank_mask:0x8
s_waitcnt vmcnt(15)
s_nop 0
v_mfma_f32_16x16x32_bf16 %[acc], v[228:231], v[52:55], %[acc]
s_movk_i32 s94, 11456
s_nop 0
v_perm_b32 v104, v229, v228, s96
v_perm_b32 v105, v231, v230, s96
v_perm_b32 v106, v229, v228, s97
v_perm_b32 v107, v231, v230, s97
v_pk_mad_u16 v108, v228, 2, -1 op_sel_hi:[1,0,0]
v_pk_mad_u16 v109, v229, 2, -1 op_sel_hi:[1,0,0]
v_pk_mad_u16 v110, v230, 2, -1 op_sel_hi:[1,0,0]
v_pk_mad_u16 v111, v231, 2, -1 op_sel_hi:[1,0,0]
v_and_b32 v112, 0x7f7f7f7f, v104
v_and_b32 v113, 0x7f7f7f7f, v105
v_sub_u32 v112, s82, v112
v_sub_u32 v113, s82, v113
v_pk_min_u16 v108, v108, v109
v_pk_min_u16 v110, v110, v111
v_pk_min_u16 v108, v108, v110
v_bitop3_b32 v109, v112, v113, s98 bitop3:0xa8
v_pk_sub_u16 v108, s83, v108 clamp
v_cmp_ne_u32_e64 vcc, v108, v109
v_perm_b32 v112, s99, v115, v112
v_perm_b32 v113, s99, v115, v113
v_bitop3_b32 v112, v112, v104, s98 bitop3:0x8d
v_bitop3_b32 v113, v113, v105, s98 bitop3:0x8d
v_alignbit_b32 v113, v113, v113, 28
v_or_b32 v114, v112, v113
s_getpc_b64 s[92:93]
s_cbranch_vccnz Lexc
v_mov_b32 v195, v114
v_mov_b32 v200, v106
v_mov_b32 v201, v107
buffer_load_dwordx4 v[204:207], %[vo], %[rv], %[so] offen offset:3072 nt
v_and_b32_dpp v52, %[p3], %[hm] row_newbcast:4 row_mask:0xf bank_mask:0x1
v_and_b32_dpp v53, %[p3], %[hm] row_newbcast:4 row_mask:0xf bank_mask:0x2
v_and_b32_dpp v54, %[p3], %[hm] row_newbcast:4 row_mask:0xf bank_mask:0x4
v_and_b32_dpp v55, %[p3], %[hm] row_newbcast:4 row_mask:0xf bank_mask:0x8
s_waitcnt vmcnt(15)
s_nop 0
v_mfma_f32_16x16x32_bf16 %[acc], v[232:235], v[52:55], %[acc]
s_movk_i32 s94, 11520
s_nop 0
v_perm_b32 v104, v233, v232, s96
)" R"(
v_perm_b32 v105, v235, v234, s96
v_perm_b32 v106, v233, v232, s97
v_perm_b32 v107, v235, v234, s97
v_pk_mad_u16 v108, v232, 2, -1 op_sel_hi:[1,0,0]
v_pk_mad_u16 v109, v233, 2, -1 op_sel_hi:[1,0,0]
v_pk_mad_u16 v110, v234, 2, -1 op_sel_hi:[1,0,0]
v_pk_mad_u16 v111, v235, 2, -1 op_sel_hi:[1,0,0]
v_and_b32 v112, 0x7f7f7f7f, v104
v_and_b32 v113, 0x7f7f7f7f, v105
v_sub_u32 v112, s82, v112
v_sub_u32 v113, s82, v113
v_pk_min_u16 v108, v108, v109
v_pk_min_u16 v110, v110, v111
v_pk_min_u16 v108, v108, v110
v_bitop3_b32 v109, v112, v113, s98 bitop3:0xa8
v_pk_sub_u16 v108, s83, v108 clamp
v_cmp_ne_u32_e64 vcc, v108, v109
v_perm_b32 v112, s99, v115, v112
v_perm_b32 v113, s99, v115, v113
v_bitop3_b32 v112, v112, v104, s98 bitop3:0x8d
v_bitop3_b32 v113, v113, v105, s98 bitop3:0x8d
v_alignbit_b32 v113, v113, v113, 28
v_or_b32 v114, v112, v113
s_getpc_b64 s[92:93]
s_cbranch_vccnz Lexc
v_mov_b32 v202, v114
v_mov_b32 v203, v106
v_mov_b32 v208, v107
s_branch LP48_%=b
LP48_%=:
s_sleep 1
LP48_%=b:
s_memrealtime vcc
s_waitcnt lgkmcnt(0)
s_sub_u32 %[so], vcc_lo, %[t0]
s_cmp_lt_u32 %[so], 3101
s_cbranch_scc1 LP48_%=
s_cmp_lt_u32 %[so], 3181
s_cbranch_scc0 LP48_%=f
s_waitcnt vmcnt(8)
s_cmp_lt_u32 %[so], 3131
s_cbranch_scc0 LP48_%=f
s_waitcnt vmcnt(4)
s_nop 0
LP48_%=f:
s_mov_b32 %[so], 69632
buffer_load_dwordx4 v[36:39], %[vo], %[rv], %[so] offen offset:0
v_and_b32_dpp v52, %[p3], %[hm] row_newbcast:5 row_mask:0xf bank_mask:0x1
v_and_b32_dpp v53, %[p3], %[hm] row_newbcast:5 row_mask:0xf bank_mask:0x2
v_and_b32_dpp v54, %[p3], %[hm] row_newbcast:5 row_mask:0xf bank_mask:0x4
v_and_b32_dpp v55, %[p3], %[hm] row_newbcast:5 row_mask:0xf bank_mask:0x8
s_waitcnt vmcnt(15)
s_nop 0
v_mfma_f32_16x16x32_bf16 %[acc], v[236:239], v[52:55], %[acc]
s_movk_i32 s94, 11584
s_nop 0
v_perm_b32 v104, v237, v236, s96
v_perm_b32 v105, v239, v238, s96
v_perm_b32 v106, v237, v236, s97
v_perm_b32 v107, v239, v238, s97
v_pk_mad_u16 v108, v236, 2, -1 op_sel_hi:[1,0,0]
v_pk_mad_u16 v109, v237, 2, -1 op_sel_hi:[1,0,0]
v_pk_mad_u16 v110, v238, 2, -1 op_sel_hi:[1,0,0]
v_pk_mad_u16 v111, v239, 2, -1 op_sel_hi:[1,0,0]
v_and_b32 v112, 0x7f7f7f7f, v104
v_and_b32 v113, 0x7f7f7f7f, v105
v_sub_u32 v112, s82, v112
v_sub_u32 v113, s82, v113
v_pk_min_u16 v108, v108, v109
v_pk_min_u16 v110, v110, v111
v_pk_min_u16 v108, v108, v110
v_bitop3_b32 v109, v112, v113, s98 bitop3:0xa8
v_pk_sub_u16 v108, s83, v108 clamp
v_cmp_ne_u32_e64 vcc, v108, v109
v_perm_b32 v112, s99, v115, v112
v_perm_b32 v113, s99, v115, v113
v_bitop3_b32 v112, v112, v104, s98 bitop3:0x8d
v_bitop3_b32 v113, v113, v105, s98 bitop3:0x8d
v_alignbit_b32 v113, v113, v113, 28
v_or_b32 v114, v112, v113
s_getpc_b64 s[92:93]
s_cbranch_vccnz Lexc
v_mov_b32 v209, v114
v_mov_b32 v210, v106
v_mov_b32 v211, v107
buffer_load_dwordx4 v[40:43], %[vo], %[rv], %[so] offen offset:1024
v_and_b32_dpp v52, %[p3], %[hm] row_newbcast:6 row_mask:0xf bank_mask:0x1
v_and_b32_dpp v53, %[p3], %[hm] row_newbcast:6 row_mask:0xf bank_mask:0x2
v_and_b32_dpp v54, %[p3], %[hm] row_newbcast:6 row_mask:0xf bank_mask:0x4
v_and_b32_dpp v55, %[p3], %[hm] row_newbcast:6 row_mask:0xf bank_mask:0x8
s_waitcnt vmcnt(15)
s_nop 0
v_mfma_f32_16x16x32_bf16 %[acc], v[240:243], v[52:55], %[acc]
s_movk_i32 s94, 11648
s_nop 0
v_perm_b32 v104, v241, v240, s96
v_perm_b32 v105, v243, v242, s96
v_perm_b32 v106, v241, v240, s97
v_perm_b32 v107, v243, v242, s97
v_pk_mad_u16 v108, v240, 2, -1 op_sel_hi:[1,0,0]
v_pk_mad_u16 v109, v241, 2, -1 op_sel_hi:[1,0,0]
v_pk_mad_u16 v110, v242, 2, -1 op_sel_hi:[1,0,0]
v_pk_mad_u16 v111, v243, 2, -1 op_sel_hi:[1,0,0]
v_and_b32 v112, 0x7f7f7f7f, v104
v_and_b32 v113, 0x7f7f7f7f, v105
v_sub_u32 v112, s82, v112
v_sub_u32 v113, s82, v113
v_pk_min_u16 v108, v108, v109
v_pk_min_u16 v110, v110, v111
v_pk_min_u16 v108, v108, v110
v_bitop3_b32 v109, v112, v113, s98 bitop3:0xa8
v_pk_sub_u16 v108, s83, v108 clamp
v_cmp_ne_u32_e64 vcc, v108, v109
v_perm_b32 v112, s99, v115, v112
v_perm_b32 v113, s99, v115, v113
v_bitop3_b32 v112, v112, v104, s98 bitop3:0x8d
v_bitop3_b32 v113, v113, v105, s98 bitop3:0x8d
v_alignbit_b32 v113, v113, v113, 28
v_or_b32 v114, v112, v113
s_getpc_b64 s[92:93]
s_cbranch_vccnz Lexc
v_mov_b32 v212, v114
v_mov_b32 v213, v106
v_mov_b32 v214, v107
buffer_load_dwordx4 v[44:47], %[vo], %[rv], %[so] offen offset:2048
v_and_b32_dpp v52, %[p3], %[hm] row_newbcast:7 row_mask:0xf bank_mask:0x1
v_and_b32_dpp v53, %[p3], %[hm] row_newbcast:7 row_mask:0xf bank_mask:0x2
v_and_b32_dpp v54, %[p3], %[hm] row_newbcast:7 row_mask:0xf bank_mask:0x4
v_and_b32_dpp v55, %[p3], %[hm] row_newbcast:7 row_mask:0xf bank_mask:0x8
s_waitcnt vmcnt(15)
s_nop 0
v_mfma_f32_16x16x32_bf16 %[acc], v[244:247], v[52:55], %[acc]
s_movk_i32 s94, 11712
s_nop 0
v_perm_b32 v104, v245, v244, s96
v_perm_b32 v105, v247, v246, s96
v_perm_b32 v106, v245, v244, s97
v_perm_b32 v107, v247, v246, s97
v_pk_mad_u16 v108, v244, 2, -1 op_sel_hi:[1,0,0]
v_pk_mad_u16 v109, v245, 2, -1 op_sel_hi:[1,0,0]
v_pk_mad_u16 v110, v246, 2, -1 op_sel_hi:[1,0,0]
v_pk_mad_u16 v111, v247, 2, -1 op_sel_hi:[1,0,0]
v_and_b32 v112, 0x7f7f7f7f, v104
v_and_b32 v113, 0x7f7f7f7f, v105
v_sub_u32 v112, s82, v112
v_sub_u32 v113, s82, v113
v_pk_min_u16 v108, v108, v109
v_pk_min_u16 v110, v110, v111
v_pk_min_u16 v108, v108, v110
v_bitop3_b32 v109, v112, v113, s98 bitop3:0xa8
v_pk_sub_u16 v108, s83, v108 clamp
v_cmp_ne_u32_e64 vcc, v108, v109
v_perm_b32 v112, s99, v115, v112
v_perm_b32 v113, s99, v115, v113
v_bitop3_b32 v112, v112, v104, s98 bitop3:0x8d
v_bitop3_b32 v113, v113, v105, s98 bitop3:0x8d
v_alignbit_b32 v113, v113, v113, 28
v_or_b32 v114, v112, v113
s_getpc_b64 s[92:93]
s_cbranch_vccnz Lexc
v_mov_b32 v215, v114
v_mov_b32 v216, v106
v_mov_b32 v217, v107
buffer_load_dwordx4 v[48:51], %[vo], %[rv], %[so] offen offset:3072
v_and_b32_dpp v52, %[p3], %[hm] row_newbcast:8 row_mask:0xf bank_mask:0x1
v_and_b32_dpp v53, %[p3], %[hm] row_newbcast:8 row_mask:0xf bank_mask:0x2
v_and_b32_dpp v54, %[p3], %[hm] row_newbcast:8 row_mask:0xf bank_mask:0x4
v_and_b32_dpp v55, %[p3], %[hm] row_newbcast:8 row_mask:0xf bank_mask:0x8
s_waitcnt vmcnt(15)
s_nop 0
v_mfma_f32_16x16x32_bf16 %[acc], v[132:135], v[52:55], %[acc]
s_movk_i32 s94, 11776
s_nop 0
v_perm_b32 v104, v133, v132, s96
v_perm_b32 v105, v135, v134, s96
v_perm_b32 v106, v133, v132, s97
v_perm_b32 v107, v135, v134, s97
v_pk_mad_u16 v108, v132, 2, -1 op_sel_hi:[1,0,0]
v_pk_mad_u16 v109, v133, 2, -1 op_sel_hi:[1,0,0]
v_pk_mad_u16 v110, v134, 2, -1 op_sel_hi:[1,0,0]
v_pk_mad_u16 v111, v135, 2, -1 op_sel_hi:[1,0,0]
v_and_b32 v112, 0x7f7f7f7f, v104
v_and_b32 v113, 0x7f7f7f7f, v105
v_sub_u32 v112, s82, v112
v_sub_u32 v113, s82, v113
v_pk_min_u16 v108, v108, v109
v_pk_min_u16 v110, v110, v111
v_pk_min_u16 v108, v108, v110
v_bitop3_b32 v109, v112, v113, s98 bitop3:0xa8
v_pk_sub_u16 v108, s83, v108 clamp
v_cmp_ne_u32_e64 vcc, v108, v109
v_perm_b32 v112, s99, v115, v112
v_perm_b32 v113, s99, v115, v113
v_bitop3_b32 v112, v112, v104, s98 bitop3:0x8d
v_bitop3_b32 v113, v113, v105, s98 bitop3:0x8d
v_alignbit_b32 v113, v113, v113, 28
v_or_b32 v114, v112, v113
s_getpc_b64 s[92:93]
s_cbranch_vccnz Lexc
v_mov_b32 v218, v114
v_mov_b32 v219, v106
v_mov_b32 v220, v107
s_branch LP49_%=b
LP49_%=:
s_sleep 1
LP49_%=b:
s_memrealtime vcc
s_waitcnt lgkmcnt(0)
s_sub_u32 %[so], vcc_lo, %[t0]
s_cmp_lt_u32 %[so], 3168
s_cbranch_scc1 LP49_%=
s_cmp_lt_u32 %[so], 3248
s_cbranch_scc0 LP49_%=f
s_waitcnt vmcnt(8)
s_cmp_lt_u32 %[so], 3198
s_cbranch_scc0 LP49_%=f
s_waitcnt vmcnt(4)
s_nop 0
LP49_%=f:
s_mov_b32 %[so], 73728
buffer_load_dwordx4 v[224:227], %[vo], %[rv], %[so] offen offset:0
v_and_b32_dpp v52, %[p3], %[hm] row_newbcast:9 row_mask:0xf bank_mask:0x1
v_and_b32_dpp v53, %[p3], %[hm] row_newbcast:9 row_mask:0xf bank_mask:0x2
v_and_b32_dpp v54, %[p3], %[hm] row_newbcast:9 row_mask:0xf bank_mask:0x4
v_and_b32_dpp v55, %[p3], %[hm] row_newbcast:9 row_mask:0xf bank_mask:0x8
s_waitcnt vmcnt(15)
s_nop 0
v_mfma_f32_16x16x32_bf16 %[acc], v[136:139], v[52:55], %[acc]
s_movk_i32 s94, 11840
s_nop 0
v_perm_b32 v104, v137, v136, s96
v_perm_b32 v105, v139, v138, s96
v_perm_b32 v106, v137, v136, s97
v_perm_b32 v107, v139, v138, s97
v_pk_mad_u16 v108, v136, 2, -1 op_sel_hi:[1,0,0]
v_pk_mad_u16 v109, v137, 2, -1 op_sel_hi:[1,0,0]
v_pk_mad_u16 v110, v138, 2, -1 op_sel_hi:[1,0,0]
v_pk_mad_u16 v111, v139, 2, -1 op_sel_hi:[1,0,0]
v_and_b32 v112, 0x7f7f7f7f, v104
v_and_b32 v113, 0x7f7f7f7f, v105
v_sub_u32 v112, s82, v112
v_sub_u32 v113, s82, v113
v_pk_min_u16 v108, v108, v109
v_pk_min_u16 v110, v110, v111
v_pk_min_u16 v108, v108, v110
v_bitop3_b32 v109, v112, v113, s98 bitop3:0xa8
v_pk_sub_u16 v108, s83, v108 clamp
v_cmp_ne_u32_e64 vcc, v108, v109
v_perm_b32 v112, s99, v115, v112
v_perm_b32 v113, s99, v115, v113
v_bitop3_b32 v112, v112, v104, s98 bitop3:0x8d
v_bitop3_b32 v113, v113, v105, s98 bitop3:0x8d
v_alignbit_b32 v113, v113, v113, 28
v_or_b32 v114, v112, v113
s_getpc_b64 s[92:93]
s_cbranch_vccnz Lexc
v_mov_b32 v221, v114
v_mov_b32 v222, v106
v_mov_b32 v223, v107
buffer_load_dwordx4 v[228:231], %[vo], %[rv], %[so] offen offset:1024
v_and_b32_dpp v52, %[p3], %[hm] row_newbcast:10 row_mask:0xf bank_mask:0x1
v_and_b32_dpp v53, %[p3], %[hm] row_newbcast:10 row_mask:0xf bank_mask:0x2
v_and_b32_dpp v54, %[p3], %[hm] row_newbcast:10 row_mask:0xf bank_mask:0x4
v_and_b32_dpp v55, %[p3], %[hm] row_newbcast:10 row_mask:0xf bank_mask:0x8
s_waitcnt vmcnt(15)
s_nop 0
v_mfma_f32_16x16x32_bf16 %[acc], v[140:143], v[52:55], %[acc]
s_movk_i32 s94, 11904
s_nop 0
v_perm_b32 v104, v141, v140, s96
v_perm_b32 v105, v143, v142, s96
v_perm_b32 v106, v141, v140, s97
v_perm_b32 v107, v143, v142, s97
v_pk_mad_u16 v108, v140, 2, -1 op_sel_hi:[1,0,0]
v_pk_mad_u16 v109, v141, 2, -1 op_sel_hi:[1,0,0]
v_pk_mad_u16 v110, v142, 2, -1 op_sel_hi:[1,0,0]
v_pk_mad_u16 v111, v143, 2, -1 op_sel_hi:[1,0,0]
v_and_b32 v112, 0x7f7f7f7f, v104
v_and_b32 v113, 0x7f7f7f7f, v105
v_sub_u32 v112, s82, v112
v_sub_u32 v113, s82, v113
v_pk_min_u16 v108, v108, v109
v_pk_min_u16 v110, v110, v111
v_pk_min_u16 v108, v108, v110
v_bitop3_b32 v109, v112, v113, s98 bitop3:0xa8
v_pk_sub_u16 v108, s83, v108 clamp
v_cmp_ne_u32_e64 vcc, v108, v109
v_perm_b32 v112, s99, v115, v112
v_perm_b32 v113, s99, v115, v113
v_bitop3_b32 v112, v112, v104, s98 bitop3:0x8d
v_bitop3_b32 v113, v113, v105, s98 bitop3:0x8d
v_alignbit_b32 v113, v113, v113, 28
v_or_b32 v114, v112, v113
s_getpc_b64 s[92:93]
s_cbranch_vccnz Lexc
v_mov_b32 v232, v114
v_mov_b32 v233, v106
v_mov_b32 v234, v107
buffer_load_dwordx4 v[236:239], %[vo], %[rv], %[so] offen offset:2048
v_and_b32_dpp v52, %[p3], %[hm] row_newbcast:11 row_mask:0xf bank_mask:0x1
v_and_b32_dpp v53, %[p3], %[hm] row_newbcast:11 row_mask:0xf bank_mask:0x2
v_and_b32_dpp v54, %[p3], %[hm] row_newbcast:11 row_mask:0xf bank_mask:0x4
v_and_b32_dpp v55, %[p3], %[hm] row_newbcast:11 row_mask:0xf bank_mask:0x8
s_waitcnt vmcnt(15)
s_nop 0
v_mfma_f32_16x16x32_bf16 %[acc], v[148:151], v[52:55], %[acc]
s_movk_i32 s94, 11968
s_nop 0
v_perm_b32 v104, v149, v148, s96
v_perm_b32 v105, v151, v150, s96
v_perm_b32 v106, v149, v148, s97
v_perm_b32 v107, v151, v150, s97
v_pk_mad_u16 v108, v148, 2, -1 op_sel_hi:[1,0,0]
v_pk_mad_u16 v109, v149, 2, -1 op_sel_hi:[1,0,0]
v_pk_mad_u16 v110, v150, 2, -1 op_sel_hi:[1,0,0]
v_pk_mad_u16 v111, v151, 2, -1 op_sel_hi:[1,0,0]
v_and_b32 v112, 0x7f7f7f7f, v104
v_and_b32 v113, 0x7f7f7f7f, v105
v_sub_u32 v112, s82, v112
v_sub_u32 v113, s82, v113
v_pk_min_u16 v108, v108, v109
v_pk_min_u16 v110, v110, v111
v_pk_min_u16 v108, v108, v110
v_bitop3_b32 v109, v112, v113, s98 bitop3:0xa8
v_pk_sub_u16 v108, s83, v108 clamp
v_cmp_ne_u32_e64 vcc, v108, v109
v_perm_b32 v112, s99, v115, v112
v_perm_b32 v113, s99, v115, v113
v_bitop3_b32 v112, v112, v104, s98 bitop3:0x8d
v_bitop3_b32 v113, v113, v105, s98 bitop3:0x8d
v_alignbit_b32 v113, v113, v113, 28
v_or_b32 v114, v112, v113
s_getpc_b64 s[92:93]
s_cbranch_vccnz Lexc
v_mov_b32 v235, v114
v_mov_b32 v240, v106
v_mov_b32 v241, v107
buffer_load_dwordx4 v[244:247], %[vo], %[rv], %[so] offen offset:3072
s_nop 7
)"
      : [acc] "=&v"(acc), [so] "=&s"(so), "+{s78}"(rcn), "=&{s82}"(t4v), "=&{s83}"(thv)
      : [rv] "{s[60:63]}"(c.rsV), [t0] "s"(t0), [vo] "v"(c.lane * 16), [hm] "v"(half_mask(c)), [p0] "v"(a.pp[0]), [p1] "v"(a.pp[1]), [p2] "v"(a.pp[2]), [p3] "v"(a.pp[3]), "{s79}"(r_base(c)), "{s80}"(t4k), "{s81}"(thk)
      : "memory", "vcc", "scc", "s84", "s85", "s86", "s87", "s88", "s89", "s90", "s91", "s92", "s93", "s94", "s95", "s96", "s97", "s98", "s99");

    if (2 == 1) { t0 -= (tb & 1) * max((int)(t0 - tb) + (30) - (20), 0); asm volatile("" : "+s"(t0)); }
    derive(c);
    const int lb = WS_LAYER0 + O_ML + c.h * 512;
    asm volatile(
R"(
.p2align 3
s_nop 4
s_nop 0
s_mov_b32 s96, 0x07050301
s_mov_b32 s97, 0x06040200
s_mov_b32 s98, 0x80808080
s_mov_b32 s99, 0xfffefdfc
buffer_load_dwordx4 %[ml], %[mo], %[rw], %[lb] offen sc1
buffer_load_dword %[ak], %[ao], %[rw], 0 offen sc1
v_and_b32_dpp v52, %[p3], %[hm] row_newbcast:12 row_mask:0xf bank_mask:0x1
v_and_b32_dpp v53, %[p3], %[hm] row_newbcast:12 row_mask:0xf bank_mask:0x2
v_and_b32_dpp v54, %[p3], %[hm] row_newbcast:12 row_mask:0xf bank_mask:0x4
v_and_b32_dpp v55, %[p3], %[hm] row_newbcast:12 row_mask:0xf bank_mask:0x8
s_waitcnt vmcnt(17)
s_nop 0
v_mfma_f32_16x16x32_bf16 %[acc], v[156:159], v[52:55], %[acc]
s_movk_i32 s94, 12032
s_nop 0
v_perm_b32 v104, v157, v156, s96
v_perm_b32 v105, v159, v158, s96
v_perm_b32 v106, v157, v156, s97
v_perm_b32 v107, v159, v158, s97
v_pk_mad_u16 v108, v156, 2, -1 op_sel_hi:[1,0,0]
v_pk_mad_u16 v109, v157, 2, -1 op_sel_hi:[1,0,0]
v_pk_mad_u16 v110, v158, 2, -1 op_sel_hi:[1,0,0]
v_pk_mad_u16 v111, v159, 2, -1 op_sel_hi:[1,0,0]
v_and_b32 v112, 0x7f7f7f7f, v104
v_and_b32 v113, 0x7f7f7f7f, v105
v_sub_u32 v112, s82, v112
v_sub_u32 v113, s82, v113
v_pk_min_u16 v108, v108, v109
v_pk_min_u16 v110, v110, v111
v_pk_min_u16 v108, v108, v110
v_bitop3_b32 v109, v112, v113, s98 bitop3:0xa8
v_pk_sub_u16 v108, s83, v108 clamp
v_cmp_ne_u32_e64 vcc, v108, v109
v_perm_b32 v112, s99, v115, v112
v_perm_b32 v113, s99, v115, v113
v_bitop3_b32 v112, v112, v104, s98 bitop3:0x8d
v_bitop3_b32 v113, v113, v105, s98 bitop3:0x8d
v_alignbit_b32 v113, v113, v113, 28
v_or_b32 v114, v112, v113
s_getpc_b64 s[92:93]
s_cbranch_vccnz Lexc
v_mov_b32 v242, v114
v_mov_b32 v243, v106
v_mov_b32 v132, v107
s_branch LP50_%=b
LP50_%=:
s_sleep 1
LP50_%=b:
s_memrealtime vcc
s_waitcnt lgkmcnt(0)
s_sub_u32 %[so], vcc_lo, %[t0]
s_cmp_lt_u32 %[so], 3235
s_cbranch_scc1 LP50_%=
s_cmp_lt_u32 %[so], 3315
s_cbranch_scc0 LP50_%=f
s_waitcnt vmcnt(8)
s_cmp_lt_u32 %[so], 3265
s_cbranch_scc0 LP50_%=f
s_waitcnt vmcnt(4)
s_nop 0
LP50_%=f:
buffer_load_dword v136, %[vo], %[rc], 0 offen
s_mov_b32 %[so], 77824
buffer_load_dwordx4 v[136:139], %[vo], %[rv], %[so] offen offset:0
v_and_b32_dpp v52, %[p3], %[hm] row_newbcast:13 row_mask:0xf bank_mask:0x1
v_and_b32_dpp v53, %[p3], %[hm] row_newbcast:13 row_mask:0xf bank_mask:0x2
v_and_b32_dpp v54, %[p3], %[hm] row_newbcast:13 row_mask:0xf bank_mask:0x4
v_and_b32_dpp v55, %[p3], %[hm] row_newbcast:13 row_mask:0xf bank_mask:0x8
s_waitcnt vmcnt(18)
s_nop 0
v_mfma_f32_16x16x32_bf16 %[acc], v[160:163], v[52:55], %[acc]
s_movk_i32 s94, 12096
s_nop 0
v_perm_b32 v104, v161, v160, s96
v_perm_b32 v105, v163, v162, s96
v_perm_b32 v106, v161, v160, s97
v_perm_b32 v107, v163, v162, s97
v_pk_mad_u16 v108, v160, 2, -1 op_sel_hi:[1,0,0]
v_pk_mad_u16 v109, v161, 2, -1 op_sel_hi:[1,0,0]
v_pk_mad_u16 v110, v162, 2, -1 op_sel_hi:[1,0,0]
v_pk_mad_u16 v111, v163, 2, -1 op_sel_hi:[1,0,0]
v_and_b32 v112, 0x7f7f7f7f, v104
v_and_b32 v113, 0x7f7f7f7f, v105
v_sub_u32 v112, s82, v112
v_sub_u32 v113, s82, v113
v_pk_min_u16 v108, v108, v109
v_pk_min_u16 v110, v110, v111
v_pk_min_u16 v108, v108, v110
v_bitop3_b32 v109, v112, v113, s98 bitop3:0xa8
v_pk_sub_u16 v108, s83, v108 clamp
v_cmp_ne_u32_e64 vcc, v108, v109
v_perm_b32 v112, s99, v115, v112
v_perm_b32 v113, s99, v115, v113
v_bitop3_b32 v112, v112, v104, s98 bitop3:0x8d
v_bitop3_b32 v113, v113, v105, s98 bitop3:0x8d
v_alignbit_b32 v113, v113, v113, 28
v_or_b32 v114, v112, v113
s_getpc_b64 s[92:93]
s_cbranch_vccnz Lexc
v_mov_b32 v133, v114
v_mov_b32 v134, v106
v_mov_b32 v135, v107
buffer_load_dwordx4 v[140:143], %[vo], %[rv], %[so] offen offset:1024
buffer_load_dword v148, %[td], %[rd], 0 offen
v_and_b32_dpp v52, %[p3], %[hm] row_newbcast:14 row_mask:0xf bank_mask:0x1
v_and_b32_dpp v53, %[p3], %[hm] row_newbcast:14 row_mask:0xf bank_mask:0x2
v_and_b32_dpp v54, %[p3], %[hm] row_newbcast:14 row_mask:0xf bank_mask:0x4
v_and_b32_dpp v55, %[p3], %[hm] row_newbcast:14 row_mask:0xf bank_mask:0x8
s_waitcnt vmcnt(19)
s_nop 0
v_mfma_f32_16x16x32_bf16 %[acc], v[168:171], v[52:55], %[acc]
s_movk_i32 s94, 12160
s_nop 0
v_perm_b32 v104, v169, v168, s96
v_perm_b32 v105, v171, v170, s96
v_perm_b32 v106, v169, v168, s97
v_perm_b32 v107, v171, v170, s97
v_pk_mad_u16 v108, v168, 2, -1 op_sel_hi:[1,0,0]
v_pk_mad_u16 v109, v169, 2, -1 op_sel_hi:[1,0,0]
v_pk_mad_u16 v110, v170, 2, -1 op_sel_hi:[1,0,0]
v_pk_mad_u16 v111, v171, 2, -1 op_sel_hi:[1,0,0]
v_and_b32 v112, 0x7f7f7f7f, v104
v_and_b32 v113, 0x7f7f7f7f, v105
v_sub_u32 v112, s82, v112
v_sub_u32 v113, s82, v113
v_pk_min_u16 v108, v108, v109
v_pk_min_u16 v110, v110, v111
v_pk_min_u16 v108, v108, v110
v_bitop3_b32 v109, v112, v113, s98 bitop3:0xa8
v_pk_sub_u16 v108, s83, v108 clamp
v_cmp_ne_u32_e64 vcc, v108, v109
v_perm_b32 v112, s99, v115, v112
v_perm_b32 v113, s99, v115, v113
v_bitop3_b32 v112, v112, v104, s98 bitop3:0x8d
v_bitop3_b32 v113, v113, v105, s98 bitop3:0x8d
v_alignbit_b32 v113, v113, v113, 28
v_or_b32 v114, v112, v113
s_getpc_b64 s[92:93]
s_cbranch_vccnz Lexc
v_mov_b32 v156, v114
v_mov_b32 v157, v106
v_mov_b32 v158, v107
buffer_load_dwordx4 v[148:151], %[vo], %[rv], %[so] offen offset:2048
v_and_b32_dpp v52, %[p3], %[hm] row_newbcast:15 row_mask:0xf bank_mask:0x1
v_and_b32_dpp v53, %[p3], %[hm] row_newbcast:15 row_mask:0xf bank_mask:0x2
v_and_b32_dpp v54, %[p3], %[hm] row_newbcast:15 row_mask:0xf bank_mask:0x4
v_and_b32_dpp v55, %[p3], %[hm] row_newbcast:15 row_mask:0xf bank_mask:0x8
s_waitcnt vmcnt(19)
s_nop 0
v_mfma_f32_16x16x32_bf16 %[acc], v[176:179], v[52:55], %[acc]
s_movk_i32 s94, 12224
s_nop 0
v_perm_b32 v104, v177, v176, s96
v_perm_b32 v105, v179, v178, s96
v_perm_b32 v106, v177, v176, s97
v_perm_b32 v107, v179, v178, s97
v_pk_mad_u16 v108, v176, 2, -1 op_sel_hi:[1,0,0]
v_pk_mad_u16 v109, v177, 2, -1 op_sel_hi:[1,0,0]
v_pk_mad_u16 v110, v178, 2, -1 op_sel_hi:[1,0,0]
v_pk_mad_u16 v111, v179, 2, -1 op_sel_hi:[1,0,0]
v_and_b32 v112, 0x7f7f7f7f, v104
v_and_b32 v113, 0x7f7f7f7f, v105
v_sub_u32 v112, s82, v112
v_sub_u32 v113, s82, v113
v_pk_min_u16 v108, v108, v109
v_pk_min_u16 v110, v110, v111
v_pk_min_u16 v108, v108, v110
v_bitop3_b32 v109, v112, v113, s98 bitop3:0xa8
v_pk_sub_u16 v108, s83, v108 clamp
v_cmp_ne_u32_e64 vcc, v108, v109
v_perm_b32 v112, s99, v115, v112
v_perm_b32 v113, s99, v115, v113
v_bitop3_b32 v112, v112, v104, s98 bitop3:0x8d
v_bitop3_b32 v113, v113, v105, s98 bitop3:0x8d
v_alignbit_b32 v113, v113, v113, 28
v_or_b32 v114, v112, v113
s_getpc_b64 s[92:93]
s_cbranch_vccnz Lexc
v_mov_b32 v159, v114
v_mov_b32 v160, v106
v_mov_b32 v161, v107
buffer_load_dwordx4 v[168:171], %[vo], %[rv], %[so] offen offset:3072
v_and_b32_dpp v52, %[p4], %[hm] row_newbcast:0 row_mask:0xf bank_mask:0x1
v_and_b32_dpp v53, %[p4], %[hm] row_newbcast:0 row_mask:0xf bank_mask:0x2
v_and_b32_dpp v54, %[p4], %[hm] row_newbcast:0 row_mask:0xf bank_mask:0x4
v_and_b32_dpp v55, %[p4], %[hm] row_newbcast:0 row_mask:0xf bank_mask:0x8
s_waitcnt vmcnt(19)
s_nop 0
v_mfma_f32_16x16x32_bf16 %[acc], v[184:187], v[52:55], %[acc]
s_movk_i32 s94, 12288
s_nop 0
v_perm_b32 v104, v185, v184, s96
v_perm_b32 v105, v187, v186, s96
v_perm_b32 v106, v185, v184, s97
v_perm_b32 v107, v187, v186, s97
v_pk_mad_u16 v108, v184, 2, -1 op_sel_hi:[1,0,0]
v_pk_mad_u16 v109, v185, 2, -1 op_sel_hi:[1,0,0]
v_pk_mad_u16 v110, v186, 2, -1 op_sel_hi:[1,0,0]
v_pk_mad_u16 v111, v187, 2, -1 op_sel_hi:[1,0,0]
v_and_b32 v112, 0x7f7f7f7f, v104
v_and_b32 v113, 0x7f7f7f7f, v105
v_sub_u32 v112, s82, v112
v_sub_u32 v113, s82, v113
v_pk_min_u16 v108, v108, v109
v_pk_min_u16 v110, v110, v111
v_pk_min_u16 v108, v108, v110
v_bitop3_b32 v109, v112, v113, s98 bitop3:0xa8
v_pk_sub_u16 v108, s83, v108 clamp
v_cmp_ne_u32_e64 vcc, v108, v109
v_perm_b32 v112, s99, v115, v112
v_perm_b32 v113, s99, v115, v113
v_bitop3_b32 v112, v112, v104, s98 bitop3:0x8d
v_bitop3_b32 v113, v113, v105, s98 bitop3:0x8d
v_alignbit_b32 v113, v113, v113, 28
v_or_b32 v114, v112, v113
s_getpc_b64 s[92:93]
s_cbranch_vccnz Lexc
v_mov_b32 v162, v114
v_mov_b32 v163, v106
v_mov_b32 v176, v107
s_branch LP51_%=b
LP51_%=:
s_sleep 1
LP51_%=b:
s_memrealtime vcc
s_waitcnt lgkmcnt(0)
s_sub_u32 %[so], vcc_lo, %[t0]
s_cmp_lt_u32 %[so], 3323
s_cbranch_scc1 LP51_%=
s_cmp_lt_u32 %[so], 3403
s_cbranch_scc0 LP51_%=f
s_waitcnt vmcnt(8)
s_cmp_lt_u32 %[so], 3353
s_cbranch_scc0 LP51_%=f
s_waitcnt vmcnt(4)
s_nop 0
LP51_%=f:
s_mov_b32 %[so], 81920
buffer_load_dwordx4 v[184:187], %[vo], %[rv], %[so] offen offset:0
v_and_b32_dpp v52, %[p4], %[hm] row_newbcast:1 row_mask:0xf bank_mask:0x1
v_and_b32_dpp v53, %[p4], %[hm] row_newbcast:1 row_mask:0xf bank_mask:0x2
v_and_b32_dpp v54, %[p4], %[hm] row_newbcast:1 row_mask:0xf bank_mask:0x4
v_and_b32_dpp v55, %[p4], %[hm] row_newbcast:1 row_mask:0xf bank_mask:0x8
s_waitcnt vmcnt(19)
s_nop 0
v_mfma_f32_16x16x32_bf16 %[acc], v[188:191], v[52:55], %[acc]
s_movk_i32 s94, 12352
s_nop 0
v_perm_b32 v104, v189, v188, s96
v_perm_b32 v105, v191, v190, s96
v_perm_b32 v106, v189, v188, s97
v_perm_b32 v107, v191, v190, s97
v_pk_mad_u16 v108, v188, 2, -1 op_sel_hi:[1,0,0]
v_pk_mad_u16 v109, v189, 2, -1 op_sel_hi:[1,0,0]
v_pk_mad_u16 v110, v190, 2, -1 op_sel_hi:[1,0,0]
v_pk_mad_u16 v111, v191, 2, -1 op_sel_hi:[1,0,0]
v_and_b32 v112, 0x7f7f7f7f, v104
v_and_b32 v113, 0x7f7f7f7f, v105
v_sub_u32 v112, s82, v112
v_sub_u32 v113, s82, v113
v_pk_min_u16 v108, v108, v109
v_pk_min_u16 v110, v110, v111
v_pk_min_u16 v108, v108, v110
v_bitop3_b32 v109, v112, v113, s98 bitop3:0xa8
v_pk_sub_u16 v108, s83, v108 clamp
v_cmp_ne_u32_e64 vcc, v108, v109
v_perm_b32 v112, s99, v115, v112
v_perm_b32 v113, s99, v115, v113
v_bitop3_b32 v112, v112, v104, s98 bitop3:0x8d
v_bitop3_b32 v113, v113, v105, s98 bitop3:0x8d
v_alignbit_b32 v113, v113, v113, 28
v_or_b32 v114, v112, v113
s_getpc_b64 s[92:93]
s_cbranch_vccnz Lexc
v_mov_b32 v177, v114
v_mov_b32 v178, v106
v_mov_b32 v179, v107
buffer_load_dwordx4 v[188:191], %[vo], %[rv], %[so] offen offset:1024
v_and_b32_dpp v52, %[p4], %[hm] row_newbcast:2 row_mask:0xf bank_mask:0x1
v_and_b32_dpp v53, %[p4], %[hm] row_newbcast:2 row_mask:0xf bank_mask:0x2
v_and_b32_dpp v54, %[p4], %[hm] row_newbcast:2 row_mask:0xf bank_mask:0x4
v_and_b32_dpp v55, %[p4], %[hm] row_newbcast:2 row_mask:0xf bank_mask:0x8
s_waitcnt vmcnt(19)
s_nop 0
v_mfma_f32_16x16x32_bf16 %[acc], v[196:199], v[52:55], %[acc]
s_movk_i32 s94, 12416
s_nop 0
v_perm_b32 v104, v197, v196, s96
v_perm_b32 v105, v199, v198, s96
v_perm_b32 v106, v197, v196, s97
v_perm_b32 v107, v199, v198, s97
v_pk_mad_u16 v108, v196, 2, -1 op_sel_hi:[1,0,0]
v_pk_mad_u16 v109, v197, 2, -1 op_sel_hi:[1,0,0]
v_pk_mad_u16 v110, v198, 2, -1 op_sel_hi:[1,0,0]
v_pk_mad_u16 v111, v199, 2, -1 op_sel_hi:[1,0,0]
v_and_b32 v112, 0x7f7f7f7f, v104
v_and_b32 v113, 0x7f7f7f7f, v105
v_sub_u32 v112, s82, v112
v_sub_u32 v113, s82, v113
v_pk_min_u16 v108, v108, v109
v_pk_min_u16 v110, v110, v111
v_pk_min_u16 v108, v108, v110
v_bitop3_b32 v109, v112, v113, s98 bitop3:0xa8
v_pk_sub_u16 v108, s83, v108 clamp
v_cmp_ne_u32_e64 vcc, v108, v109
v_perm_b32 v112, s99, v115, v112
v_perm_b32 v113, s99, v115, v113
v_bitop3_b32 v112, v112, v104, s98 bitop3:0x8d
v_bitop3_b32 v113, v113, v105, s98 bitop3:0x8d
v_alignbit_b32 v113, v113, v113, 28
v_or_b32 v114, v112, v113
s_getpc_b64 s[92:93]
s_cbranch_vccnz Lexc
v_mov_b32 v196, v114
v_mov_b32 v197, v106
v_mov_b32 v198, v107
v_and_b32_dpp v52, %[p4], %[hm] row_newbcast:3 row_mask:0xf bank_mask:0x1
v_and_b32_dpp v53, %[p4], %[hm] row_newbcast:3 row_mask:0xf bank_mask:0x2
v_and_b32_dpp v54, %[p4], %[hm] row_newbcast:3 row_mask:0xf bank_mask:0x4
v_and_b32_dpp v55, %[p4], %[hm] row_newbcast:3 row_mask:0xf bank_mask:0x8
s_waitcnt vmcnt(18)
s_nop 0
v_mfma_f32_16x16x32_bf16 %[acc], v[204:207], v[52:55], %[acc]
s_movk_i32 s94, 12480
s_nop 0
v_perm_b32 v104, v205, v204, s96
v_perm_b32 v105, v207, v206, s96
v_perm_b32 v106, v205, v204, s97
v_perm_b32 v107, v207, v206, s97
v_pk_mad_u16 v108, v204, 2, -1 op_sel_hi:[1,0,0]
v_pk_mad_u16 v109, v205, 2, -1 op_sel_hi:[1,0,0]
v_pk_mad_u16 v110, v206, 2, -1 op_sel_hi:[1,0,0]
v_pk_mad_u16 v111, v207, 2, -1 op_sel_hi:[1,0,0]
v_and_b32 v112, 0x7f7f7f7f, v104
v_and_b32 v113, 0x7f7f7f7f, v105
)" R"(
v_sub_u32 v112, s82, v112
v_sub_u32 v113, s82, v113
v_pk_min_u16 v108, v108, v109
v_pk_min_u16 v110, v110, v111
v_pk_min_u16 v108, v108, v110
v_bitop3_b32 v109, v112, v113, s98 bitop3:0xa8
v_pk_sub_u16 v108, s83, v108 clamp
v_cmp_ne_u32_e64 vcc, v108, v109
v_perm_b32 v112, s99, v115, v112
v_perm_b32 v113, s99, v115, v113
v_bitop3_b32 v112, v112, v104, s98 bitop3:0x8d
v_bitop3_b32 v113, v113, v105, s98 bitop3:0x8d
v_alignbit_b32 v113, v113, v113, 28
v_or_b32 v114, v112, v113
s_getpc_b64 s[92:93]
s_cbranch_vccnz Lexc
v_mov_b32 v199, v114
v_mov_b32 v204, v106
v_mov_b32 v205, v107
v_and_b32_dpp v52, %[p4], %[hm] row_newbcast:4 row_mask:0xf bank_mask:0x1
v_and_b32_dpp v53, %[p4], %[hm] row_newbcast:4 row_mask:0xf bank_mask:0x2
v_and_b32_dpp v54, %[p4], %[hm] row_newbcast:4 row_mask:0xf bank_mask:0x4
v_and_b32_dpp v55, %[p4], %[hm] row_newbcast:4 row_mask:0xf bank_mask:0x8
s_waitcnt vmcnt(17)
s_nop 0
v_mfma_f32_16x16x32_bf16 %[acc], v[36:39], v[52:55], %[acc]
buffer_load_dwordx4 v[36:39], %[vo], %[rv], %[so] offen offset:2048
v_and_b32_dpp v52, %[p4], %[hm] row_newbcast:5 row_mask:0xf bank_mask:0x1
v_and_b32_dpp v53, %[p4], %[hm] row_newbcast:5 row_mask:0xf bank_mask:0x2
v_and_b32_dpp v54, %[p4], %[hm] row_newbcast:5 row_mask:0xf bank_mask:0x4
v_and_b32_dpp v55, %[p4], %[hm] row_newbcast:5 row_mask:0xf bank_mask:0x8
s_waitcnt vmcnt(17)
s_nop 0
v_mfma_f32_16x16x32_bf16 %[acc], v[40:43], v[52:55], %[acc]
buffer_load_dwordx4 v[40:43], %[vo], %[rv], %[so] offen offset:3072
v_and_b32_dpp v52, %[p4], %[hm] row_newbcast:6 row_mask:0xf bank_mask:0x1
v_and_b32_dpp v53, %[p4], %[hm] row_newbcast:6 row_mask:0xf bank_mask:0x2
v_and_b32_dpp v54, %[p4], %[hm] row_newbcast:6 row_mask:0xf bank_mask:0x4
v_and_b32_dpp v55, %[p4], %[hm] row_newbcast:6 row_mask:0xf bank_mask:0x8
s_waitcnt vmcnt(17)
s_nop 0
v_mfma_f32_16x16x32_bf16 %[acc], v[44:47], v[52:55], %[acc]
s_branch LP52_%=b
LP52_%=:
s_sleep 1
LP52_%=b:
s_memrealtime vcc
s_waitcnt lgkmcnt(0)
s_sub_u32 %[so], vcc_lo, %[t0]
s_cmp_lt_u32 %[so], 3390
s_cbranch_scc1 LP52_%=
s_cmp_lt_u32 %[so], 3470
s_cbranch_scc0 LP52_%=f
s_waitcnt vmcnt(8)
s_cmp_lt_u32 %[so], 3420
s_cbranch_scc0 LP52_%=f
s_waitcnt vmcnt(4)
s_nop 0
LP52_%=f:
s_mov_b32 %[so], 86016
buffer_load_dwordx4 v[44:47], %[vo], %[rv], %[so] offen offset:0
v_and_b32_dpp v52, %[p4], %[hm] row_newbcast:7 row_mask:0xf bank_mask:0x1
v_and_b32_dpp v53, %[p4], %[hm] row_newbcast:7 row_mask:0xf bank_mask:0x2
v_and_b32_dpp v54, %[p4], %[hm] row_newbcast:7 row_mask:0xf bank_mask:0x4
v_and_b32_dpp v55, %[p4], %[hm] row_newbcast:7 row_mask:0xf bank_mask:0x8
s_waitcnt vmcnt(17)
s_nop 0
v_mfma_f32_16x16x32_bf16 %[acc], v[48:51], v[52:55], %[acc]
buffer_load_dwordx4 v[48:51], %[vo], %[rv], %[so] offen offset:1024
v_and_b32_dpp v52, %[p4], %[hm] row_newbcast:8 row_mask:0xf bank_mask:0x1
v_and_b32_dpp v53, %[p4], %[hm] row_newbcast:8 row_mask:0xf bank_mask:0x2
v_and_b32_dpp v54, %[p4], %[hm] row_newbcast:8 row_mask:0xf bank_mask:0x4
v_and_b32_dpp v55, %[p4], %[hm] row_newbcast:8 row_mask:0xf bank_mask:0x8
s_waitcnt vmcnt(17)
s_nop 0
v_mfma_f32_16x16x32_bf16 %[acc], v[224:227], v[52:55], %[acc]
buffer_load_dwordx4 v[224:227], %[vo], %[rv], %[so] offen offset:2048
v_and_b32_dpp v52, %[p4], %[hm] row_newbcast:9 row_mask:0xf bank_mask:0x1
v_and_b32_dpp v53, %[p4], %[hm] row_newbcast:9 row_mask:0xf bank_mask:0x2
v_and_b32_dpp v54, %[p4], %[hm] row_newbcast:9 row_mask:0xf bank_mask:0x4
v_and_b32_dpp v55, %[p4], %[hm] row_newbcast:9 row_mask:0xf bank_mask:0x8
s_waitcnt vmcnt(17)
s_nop 0
v_mfma_f32_16x16x32_bf16 %[acc], v[228:231], v[52:55], %[acc]
buffer_load_dwordx4 v[228:231], %[vo], %[rv], %[so] offen offset:3072
v_and_b32_dpp v52, %[p4], %[hm] row_newbcast:10 row_mask:0xf bank_mask:0x1
v_and_b32_dpp v53, %[p4], %[hm] row_newbcast:10 row_mask:0xf bank_mask:0x2
v_and_b32_dpp v54, %[p4], %[hm] row_newbcast:10 row_mask:0xf bank_mask:0x4
v_and_b32_dpp v55, %[p4], %[hm] row_newbcast:10 row_mask:0xf bank_mask:0x8
s_waitcnt vmcnt(17)
s_nop 0
v_mfma_f32_16x16x32_bf16 %[acc], v[236:239], v[52:55], %[acc]
s_branch LP53_%=b
LP53_%=:
s_sleep 1
LP53_%=b:
s_memrealtime vcc
s_waitcnt lgkmcnt(0)
s_sub_u32 %[so], vcc_lo, %[t0]
s_cmp_lt_u32 %[so], 3457
s_cbranch_scc1 LP53_%=
s_cmp_lt_u32 %[so], 3537
s_cbranch_scc0 LP53_%=f
s_waitcnt vmcnt(8)
s_cmp_lt_u32 %[so], 3487
s_cbranch_scc0 LP53_%=f
s_waitcnt vmcnt(4)
s_nop 0
LP53_%=f:
s_mov_b32 %[so], 90112
buffer_load_dwordx4 v[236:239], %[vo], %[rv], %[so] offen offset:0
v_and_b32_dpp v52, %[p4], %[hm] row_newbcast:11 row_mask:0xf bank_mask:0x1
v_and_b32_dpp v53, %[p4], %[hm] row_newbcast:11 row_mask:0xf bank_mask:0x2
v_and_b32_dpp v54, %[p4], %[hm] row_newbcast:11 row_mask:0xf bank_mask:0x4
v_and_b32_dpp v55, %[p4], %[hm] row_newbcast:11 row_mask:0xf bank_mask:0x8
s_waitcnt vmcnt(17)
s_nop 0
v_mfma_f32_16x16x32_bf16 %[acc], v[244:247], v[52:55], %[acc]
buffer_load_dwordx4 v[244:247], %[vo], %[rv], %[so] offen offset:1024
v_and_b32_dpp v52, %[p4], %[hm] row_newbcast:12 row_mask:0xf bank_mask:0x1
v_and_b32_dpp v53, %[p4], %[hm] row_newbcast:12 row_mask:0xf bank_mask:0x2
v_and_b32_dpp v54, %[p4], %[hm] row_newbcast:12 row_mask:0xf bank_mask:0x4
v_and_b32_dpp v55, %[p4], %[hm] row_newbcast:12 row_mask:0xf bank_mask:0x8
s_waitcnt vmcnt(14)
s_nop 0
v_mfma_f32_16x16x32_bf16 %[acc], v[136:139], v[52:55], %[acc]
buffer_load_dwordx4 v[136:139], %[vo], %[rv], %[so] offen offset:2048
v_and_b32_dpp v52, %[p4], %[hm] row_newbcast:13 row_mask:0xf bank_mask:0x1
v_and_b32_dpp v53, %[p4], %[hm] row_newbcast:13 row_mask:0xf bank_mask:0x2
v_and_b32_dpp v54, %[p4], %[hm] row_newbcast:13 row_mask:0xf bank_mask:0x4
v_and_b32_dpp v55, %[p4], %[hm] row_newbcast:13 row_mask:0xf bank_mask:0x8
s_waitcnt vmcnt(14)
s_nop 0
v_mfma_f32_16x16x32_bf16 %[acc], v[140:143], v[52:55], %[acc]
buffer_load_dwordx4 v[140:143], %[vo], %[rv], %[so] offen offset:3072
v_and_b32_dpp v52, %[p4], %[hm] row_newbcast:14 row_mask:0xf bank_mask:0x1
v_and_b32_dpp v53, %[p4], %[hm] row_newbcast:14 row_mask:0xf bank_mask:0x2
v_and_b32_dpp v54, %[p4], %[hm] row_newbcast:14 row_mask:0xf bank_mask:0x4
v_and_b32_dpp v55, %[p4], %[hm] row_newbcast:14 row_mask:0xf bank_mask:0x8
s_waitcnt vmcnt(13)
s_nop 0
v_mfma_f32_16x16x32_bf16 %[acc], v[148:151], v[52:55], %[acc]
s_branch LP54_%=b
LP54_%=:
s_sleep 1
LP54_%=b:
s_memrealtime vcc
s_waitcnt lgkmcnt(0)
s_sub_u32 %[so], vcc_lo, %[t0]
s_cmp_lt_u32 %[so], 3524
s_cbranch_scc1 LP54_%=
s_cmp_lt_u32 %[so], 3604
s_cbranch_scc0 LP54_%=f
s_waitcnt vmcnt(8)
s_cmp_lt_u32 %[so], 3554
s_cbranch_scc0 LP54_%=f
s_waitcnt vmcnt(4)
s_nop 0
LP54_%=f:
s_mov_b32 %[so], 94208
buffer_load_dwordx4 v[148:151], %[vo], %[rv], %[so] offen offset:0
v_and_b32_dpp v52, %[p4], %[hm] row_newbcast:15 row_mask:0xf bank_mask:0x1
v_and_b32_dpp v53, %[p4], %[hm] row_newbcast:15 row_mask:0xf bank_mask:0x2
v_and_b32_dpp v54, %[p4], %[hm] row_newbcast:15 row_mask:0xf bank_mask:0x4
v_and_b32_dpp v55, %[p4], %[hm] row_newbcast:15 row_mask:0xf bank_mask:0x8
s_waitcnt vmcnt(13)
s_nop 0
v_mfma_f32_16x16x32_bf16 %[acc], v[168:171], v[52:55], %[acc]
buffer_load_dwordx4 v[168:171], %[vo], %[rv], %[so] offen offset:1024
s_nop 7
)"
      : [acc] "+v"(acc), [ml] "=&v"(ml), [ak] "=&v"(ak), [so] "=&s"(so), "+{s78}"(rcn)
      : [rv] "{s[60:63]}"(c.rsV), [rw] "{s[56:59]}"(c.rsW), [rc] "{s[52:55]}"(c.rsC), [t0] "s"(t0), [vo] "v"(c.lane * 16), [hm] "v"(half_mask(c)), [rd] "s"(mkrs(p.wd, FF * DM * 2)), [td] "v"(touch_d(c)), [mo] "v"((c.lane & 31) * 16), [ao] "v"(WS_ACK + (c.w * 64 + c.lane) * 4), [lb] "s"(lb), [p3] "v"(a.pp[3]), [p4] "v"(a.pp[4]), "{s79}"(r_base(c)), "{s82}"(t4v), "{s83}"(thv)
      : "memory", "vcc", "scc", "s84", "s85", "s86", "s87", "s88", "s89", "s90", "s91", "s92", "s93", "s94", "s95", "s96", "s97", "s98", "s99");

    derive(c);
    if (!wave_all(ml[1] == tag && ml[3] == tag && ak == tag)) {
#pragma clang loop unroll(disable)
      for (int spin = 0; spin < SPIN_CAP; ++spin) {
        asm volatile("" ::: "memory");
        ml = ld128<SC1>(c.rsW, (c.lane & 31) * 16, WS_LAYER0 + O_ML + c.h * 512);
        ak = ld32<SC1>(c.rsW, (c.w * 64 + c.lane) * 4, WS_ACK);
        if (ml[1] == tag && ml[3] == tag && ak == tag) break;
        __builtin_amdgcn_s_sleep(4);
      }
    }
    u32x4 st = {asu(head_scale(ml, a.m)), xres, c.stv, 0}, ad;
    u32x16 yv = {};
    chain_common(c, yv, ad);
    u32 tg = tb;
    const int a4 = c.lane >> 2, ch = 11 * (c.w & 1) + a4;
    const bool cv = a4 < 11 && ch < c.nb;
    const int chunk = (c.n & 3) + 4 * c.g;
    yv[0] = a.pp[5]; yv[13] = a.pp[6]; yv[14] = a.pp[7];
    yv[6] = cv && !(0 & 2) ? 8 * (c.fb0 + ch) * (DM * 2) + (c.cp & 31) * 32 + (c.w >> 1) * 16 + (c.n & 3) * 4 : OOB;
    yv[8] = cv ? ch * HSTR : OOB;
    yv[9] = c.lds + LDS_MISC + (c.w & 1) * 1024 + 768 + (8 * (c.w >> 1) + 2 * (c.n & 3)) * 16 + c.g * 4;
    yv[10] = c.lds + LDS_MISC + c.lane * 16;
    yv[11] = (chunk >> 1) * QSTR + (chunk & 1) * 16;
    asm volatile(
R"(
.p2align 3
s_nop 4
s_nop 0
s_mov_b32 s96, 0x07050301
s_mov_b32 s97, 0x06040200
s_mov_b32 s98, 0x80808080
s_mov_b32 s99, 0xfffefdfc
v_and_b32_dpp v52, v8, v9 row_newbcast:0 row_mask:0xf bank_mask:0x1
v_and_b32_dpp v53, v8, v9 row_newbcast:0 row_mask:0xf bank_mask:0x2
v_and_b32_dpp v54, v8, v9 row_newbcast:0 row_mask:0xf bank_mask:0x4
v_and_b32_dpp v55, v8, v9 row_newbcast:0 row_mask:0xf bank_mask:0x8
s_waitcnt vmcnt(13)
s_nop 0
v_mfma_f32_16x16x32_bf16 v[0:3], v[184:187], v[52:55], v[0:3]
s_mov_b32 s91, 94208
buffer_load_dwordx4 v[184:187], v20, %[rv], s91 offen offset:2048
buffer_load_dword v24, v14, s[48:51], 0 offen offset:0
v_and_b32_dpp v52, v8, v9 row_newbcast:1 row_mask:0xf bank_mask:0x1
v_and_b32_dpp v53, v8, v9 row_newbcast:1 row_mask:0xf bank_mask:0x2
v_and_b32_dpp v54, v8, v9 row_newbcast:1 row_mask:0xf bank_mask:0x4
v_and_b32_dpp v55, v8, v9 row_newbcast:1 row_mask:0xf bank_mask:0x8
s_waitcnt vmcnt(14)
s_nop 0
v_mfma_f32_16x16x32_bf16 v[0:3], v[188:191], v[52:55], v[0:3]
buffer_load_dwordx4 v[188:191], v20, %[rv], s91 offen offset:3072
v_and_b32_dpp v52, v8, v9 row_newbcast:2 row_mask:0xf bank_mask:0x1
v_and_b32_dpp v53, v8, v9 row_newbcast:2 row_mask:0xf bank_mask:0x2
v_and_b32_dpp v54, v8, v9 row_newbcast:2 row_mask:0xf bank_mask:0x4
v_and_b32_dpp v55, v8, v9 row_newbcast:2 row_mask:0xf bank_mask:0x8
s_waitcnt vmcnt(14)
s_nop 0
v_mfma_f32_16x16x32_bf16 v[0:3], v[36:39], v[52:55], v[0:3]
s_branch LP55_%=b
LP55_%=:
s_sleep 1
LP55_%=b:
s_memrealtime vcc
s_waitcnt lgkmcnt(0)
s_sub_u32 s91, vcc_lo, %[t0]
s_cmp_lt_u32 s91, 3590
s_cbranch_scc1 LP55_%=
s_cmp_lt_u32 s91, 3670
s_cbranch_scc0 LP55_%=f
s_waitcnt vmcnt(8)
s_cmp_lt_u32 s91, 3620
s_cbranch_scc0 LP55_%=f
s_waitcnt vmcnt(4)
s_nop 0
LP55_%=f:
buffer_load_dword v36, v20, %[rc], 0 offen
s_mov_b32 s91, 98304
buffer_load_dwordx4 v[36:39], v20, %[rv], s91 offen offset:0
buffer_load_dword v28, v14, s[48:51], 0 offen offset:1024
v_and_b32_dpp v52, v8, v9 row_newbcast:3 row_mask:0xf bank_mask:0x1
v_and_b32_dpp v53, v8, v9 row_newbcast:3 row_mask:0xf bank_mask:0x2
v_and_b32_dpp v54, v8, v9 row_newbcast:3 row_mask:0xf bank_mask:0x4
v_and_b32_dpp v55, v8, v9 row_newbcast:3 row_mask:0xf bank_mask:0x8
s_waitcnt vmcnt(16)
s_nop 0
v_mfma_f32_16x16x32_bf16 v[0:3], v[40:43], v[52:55], v[0:3]
buffer_load_dwordx4 v[40:43], v20, %[rv], s91 offen offset:1024
v_and_b32_dpp v52, v8, v9 row_newbcast:4 row_mask:0xf bank_mask:0x1
v_and_b32_dpp v53, v8, v9 row_newbcast:4 row_mask:0xf bank_mask:0x2
v_and_b32_dpp v54, v8, v9 row_newbcast:4 row_mask:0xf bank_mask:0x4
v_and_b32_dpp v55, v8, v9 row_newbcast:4 row_mask:0xf bank_mask:0x8
s_waitcnt vmcnt(16)
s_nop 0
v_mfma_f32_16x16x32_bf16 v[0:3], v[44:47], v[52:55], v[0:3]
buffer_load_dwordx4 v[44:47], v20, %[rv], s91 offen offset:2048
buffer_load_dword v25, v14, s[48:51], 0 offen offset:2048
v_and_b32_dpp v52, v8, v9 row_newbcast:5 row_mask:0xf bank_mask:0x1
v_and_b32_dpp v53, v8, v9 row_newbcast:5 row_mask:0xf bank_mask:0x2
v_and_b32_dpp v54, v8, v9 row_newbcast:5 row_mask:0xf bank_mask:0x4
v_and_b32_dpp v55, v8, v9 row_newbcast:5 row_mask:0xf bank_mask:0x8
s_waitcnt vmcnt(17)
s_nop 0
v_mfma_f32_16x16x32_bf16 v[0:3], v[48:51], v[52:55], v[0:3]
buffer_load_dwordx4 v[48:51], v20, %[rv], s91 offen offset:3072
v_and_b32_dpp v52, v8, v9 row_newbcast:6 row_mask:0xf bank_mask:0x1
v_and_b32_dpp v53, v8, v9 row_newbcast:6 row_mask:0xf bank_mask:0x2
v_and_b32_dpp v54, v8, v9 row_newbcast:6 row_mask:0xf bank_mask:0x4
v_and_b32_dpp v55, v8, v9 row_newbcast:6 row_mask:0xf bank_mask:0x8
s_waitcnt vmcnt(17)
s_nop 0
v_mfma_f32_16x16x32_bf16 v[0:3], v[224:227], v[52:55], v[0:3]
s_branch LP56_%=b
LP56_%=:
s_sleep 1
LP56_%=b:
s_memrealtime vcc
s_waitcnt lgkmcnt(0)
s_sub_u32 s91, vcc_lo, %[t0]
s_cmp_lt_u32 s91, 3657
s_cbranch_scc1 LP56_%=
s_cmp_lt_u32 s91, 3737
s_cbranch_scc0 LP56_%=f
s_waitcnt vmcnt(8)
s_cmp_lt_u32 s91, 3687
s_cbranch_scc0 LP56_%=f
s_waitcnt vmcnt(4)
s_nop 0
LP56_%=f:
s_mov_b32 s91, 102400
buffer_load_dwordx4 v[224:227], v20, %[rv], s91 offen offset:0
buffer_load_dword v29, v14, s[48:51], 0 offen offset:3072
v_and_b32_dpp v52, v8, v9 row_newbcast:7 row_mask:0xf bank_mask:0x1
v_and_b32_dpp v53, v8, v9 row_newbcast:7 row_mask:0xf bank_mask:0x2
v_and_b32_dpp v54, v8, v9 row_newbcast:7 row_mask:0xf bank_mask:0x4
v_and_b32_dpp v55, v8, v9 row_newbcast:7 row_mask:0xf bank_mask:0x8
s_waitcnt vmcnt(18)
s_nop 0
v_mfma_f32_16x16x32_bf16 v[0:3], v[228:231], v[52:55], v[0:3]
buffer_load_dwordx4 v[228:231], v20, %[rv], s91 offen offset:1024
v_and_b32_dpp v52, v8, v9 row_newbcast:8 row_mask:0xf bank_mask:0x1
v_and_b32_dpp v53, v8, v9 row_newbcast:8 row_mask:0xf bank_mask:0x2
v_and_b32_dpp v54, v8, v9 row_newbcast:8 row_mask:0xf bank_mask:0x4
v_and_b32_dpp v55, v8, v9 row_newbcast:8 row_mask:0xf bank_mask:0x8
s_waitcnt vmcnt(18)
s_nop 0
v_mfma_f32_16x16x32_bf16 v[0:3], v[236:239], v[52:55], v[0:3]
buffer_load_dwordx4 v[236:239], v20, %[rv], s91 offen offset:2048
s_mov_b32 s91, 4096
buffer_load_dword v26, v14, s[48:51], s91 offen offset:0
v_and_b32_dpp v52, v8, v9 row_newbcast:9 row_mask:0xf bank_mask:0x1
v_and_b32_dpp v53, v8, v9 row_newbcast:9 row_mask:0xf bank_mask:0x2
v_and_b32_dpp v54, v8, v9 row_newbcast:9 row_mask:0xf bank_mask:0x4
v_and_b32_dpp v55, v8, v9 row_newbcast:9 row_mask:0xf bank_mask:0x8
s_waitcnt vmcnt(19)
s_nop 0
v_mfma_f32_16x16x32_bf16 v[0:3], v[244:247], v[52:55], v[0:3]
s_mov_b32 s91, 102400
buffer_load_dwordx4 v[244:247], v20, %[rv], s91 offen offset:3072
v_and_b32_dpp v52, v8, v9 row_newbcast:10 row_mask:0xf bank_mask:0x1
v_and_b32_dpp v53, v8, v9 row_newbcast:10 row_mask:0xf bank_mask:0x2
v_and_b32_dpp v54, v8, v9 row_newbcast:10 row_mask:0xf bank_mask:0x4
v_and_b32_dpp v55, v8, v9 row_newbcast:10 row_mask:0xf bank_mask:0x8
s_waitcnt vmcnt(19)
s_nop 0
v_mfma_f32_16x16x32_bf16 v[0:3], v[136:139], v[52:55], v[0:3]
s_branch LP57_%=b
LP57_%=:
s_sleep 1
LP57_%=b:
s_memrealtime vcc
s_waitcnt lgkmcnt(0)
s_sub_u32 s91, vcc_lo, %[t0]
s_cmp_lt_u32 s91, 3724
s_cbranch_scc1 LP57_%=
s_cmp_lt_u32 s91, 3804
s_cbranch_scc0 LP57_%=f
s_waitcnt vmcnt(8)
s_cmp_lt_u32 s91, 3754
s_cbranch_scc0 LP57_%=f
s_waitcnt vmcnt(4)
s_nop 0
LP57_%=f:
s_mov_b32 s91, 106496
buffer_load_dwordx4 v[136:139], v20, %[rv], s91 offen offset:0
s_mov_b32 s91, 4096
buffer_load_dword v30, v14, s[48:51], s91 offen offset:1024
v_and_b32_dpp v52, v8, v9 row_newbcast:11 row_mask:0xf bank_mask:0x1
v_and_b32_dpp v53, v8, v9 row_newbcast:11 row_mask:0xf bank_mask:0x2
v_and_b32_dpp v54, v8, v9 row_newbcast:11 row_mask:0xf bank_mask:0x4
v_and_b32_dpp v55, v8, v9 row_newbcast:11 row_mask:0xf bank_mask:0x8
s_waitcnt vmcnt(20)
s_nop 0
v_mfma_f32_16x16x32_bf16 v[0:3], v[140:143], v[52:55], v[0:3]
s_mov_b32 s91, 106496
buffer_load_dwordx4 v[140:143], v20, %[rv], s91 offen offset:1024
v_and_b32_dpp v52, v8, v9 row_newbcast:12 row_mask:0xf bank_mask:0x1
v_and_b32_dpp v53, v8, v9 row_newbcast:12 row_mask:0xf bank_mask:0x2
v_and_b32_dpp v54, v8, v9 row_newbcast:12 row_mask:0xf bank_mask:0x4
v_and_b32_dpp v55, v8, v9 row_newbcast:12 row_mask:0xf bank_mask:0x8
s_waitcnt vmcnt(20)
s_nop 0
v_mfma_f32_16x16x32_bf16 v[0:3], v[148:151], v[52:55], v[0:3]
buffer_load_dwordx4 v[148:151], v20, %[rv], s91 offen offset:2048
s_mov_b32 s91, 4096
buffer_load_dword v27, v14, s[48:51], s91 offen offset:2048
v_and_b32_dpp v52, v8, v9 row_newbcast:13 row_mask:0xf bank_mask:0x1
v_and_b32_dpp v53, v8, v9 row_newbcast:13 row_mask:0xf bank_mask:0x2
v_and_b32_dpp v54, v8, v9 row_newbcast:13 row_mask:0xf bank_mask:0x4
v_and_b32_dpp v55, v8, v9 row_newbcast:13 row_mask:0xf bank_mask:0x8
s_waitcnt vmcnt(21)
s_nop 0
v_mfma_f32_16x16x32_bf16 v[0:3], v[168:171], v[52:55], v[0:3]
s_mov_b32 s91, 106496
buffer_load_dwordx4 v[168:171], v20, %[rv], s91 offen offset:3072
v_and_b32_dpp v52, v8, v9 row_newbcast:14 row_mask:0xf bank_mask:0x1
v_and_b32_dpp v53, v8, v9 row_newbcast:14 row_mask:0xf bank_mask:0x2
v_and_b32_dpp v54, v8, v9 row_newbcast:14 row_mask:0xf bank_mask:0x4
v_and_b32_dpp v55, v8, v9 row_newbcast:14 row_mask:0xf bank_mask:0x8
s_waitcnt vmcnt(21)
s_nop 0
v_mfma_f32_16x16x32_bf16 v[0:3], v[184:187], v[52:55], v[0:3]
s_branch LP58_%=b
LP58_%=:
s_sleep 1
LP58_%=b:
s_memrealtime vcc
s_waitcnt lgkmcnt(0)
s_sub_u32 s91, vcc_lo, %[t0]
s_cmp_lt_u32 s91, 3791
s_cbranch_scc1 LP58_%=
s_cmp_lt_u32 s91, 3871
s_cbranch_scc0 LP58_%=f
s_waitcnt vmcnt(8)
s_cmp_lt_u32 s91, 3821
s_cbranch_scc0 LP58_%=f
s_waitcnt vmcnt(4)
s_nop 0
LP58_%=f:
s_mov_b32 s91, 110592
buffer_load_dwordx4 v[184:187], v20, %[rv], s91 offen offset:0 nt
s_mov_b32 s91, 4096
buffer_load_dword v31, v14, s[48:51], s91 offen offset:3072
v_and_b32_dpp v52, v8, v9 row_newbcast:15 row_mask:0xf bank_mask:0x1
v_and_b32_dpp v53, v8, v9 row_newbcast:15 row_mask:0xf bank_mask:0x2
v_and_b32_dpp v54, v8, v9 row_newbcast:15 row_mask:0xf bank_mask:0x4
v_and_b32_dpp v55, v8, v9 row_newbcast:15 row_mask:0xf bank_mask:0x8
s_waitcnt vmcnt(21)
s_nop 0
v_mfma_f32_16x16x32_bf16 v[0:3], v[188:191], v[52:55], v[0:3]
s_mov_b32 s91, 110592
buffer_load_dwordx4 v[188:191], v20, %[rv], s91 offen offset:1024 nt
v_and_b32_dpp v52, v21, v9 row_newbcast:0 row_mask:0xf bank_mask:0x1
v_and_b32_dpp v53, v21, v9 row_newbcast:0 row_mask:0xf bank_mask:0x2
v_and_b32_dpp v54, v21, v9 row_newbcast:0 row_mask:0xf bank_mask:0x4
v_and_b32_dpp v55, v21, v9 row_newbcast:0 row_mask:0xf bank_mask:0x8
s_waitcnt vmcnt(20)
s_nop 0
v_mfma_f32_16x16x32_bf16 v[0:3], v[36:39], v[52:55], v[0:3]
v_and_b32_dpp v52, v21, v9 row_newbcast:1 row_mask:0xf bank_mask:0x1
v_and_b32_dpp v53, v21, v9 row_newbcast:1 row_mask:0xf bank_mask:0x2
v_and_b32_dpp v54, v21, v9 row_newbcast:1 row_mask:0xf bank_mask:0x4
v_and_b32_dpp v55, v21, v9 row_newbcast:1 row_mask:0xf bank_mask:0x8
s_waitcnt vmcnt(18)
s_nop 0
v_mfma_f32_16x16x32_bf16 v[0:3], v[40:43], v[52:55], v[0:3]
v_and_b32_dpp v52, v21, v9 row_newbcast:2 row_mask:0xf bank_mask:0x1
v_and_b32_dpp v53, v21, v9 row_newbcast:2 row_mask:0xf bank_mask:0x2
v_and_b32_dpp v54, v21, v9 row_newbcast:2 row_mask:0xf bank_mask:0x4
v_and_b32_dpp v55, v21, v9 row_newbcast:2 row_mask:0xf bank_mask:0x8
s_waitcnt vmcnt(17)
s_nop 0
v_mfma_f32_16x16x32_bf16 v[0:3], v[44:47], v[52:55], v[0:3]
v_and_b32_dpp v52, v21, v9 row_newbcast:3 row_mask:0xf bank_mask:0x1
v_and_b32_dpp v53, v21, v9 row_newbcast:3 row_mask:0xf bank_mask:0x2
v_and_b32_dpp v54, v21, v9 row_newbcast:3 row_mask:0xf bank_mask:0x4
v_and_b32_dpp v55, v21, v9 row_newbcast:3 row_mask:0xf bank_mask:0x8
s_waitcnt vmcnt(15)
s_nop 0
v_mfma_f32_16x16x32_bf16 v[0:3], v[48:51], v[52:55], v[0:3]
v_and_b32_dpp v52, v21, v9 row_newbcast:4 row_mask:0xf bank_mask:0x1
v_and_b32_dpp v53, v21, v9 row_newbcast:4 row_mask:0xf bank_mask:0x2
v_and_b32_dpp v54, v21, v9 row_newbcast:4 row_mask:0xf bank_mask:0x4
v_and_b32_dpp v55, v21, v9 row_newbcast:4 row_mask:0xf bank_mask:0x8
s_waitcnt vmcnt(14)
s_nop 0
v_mfma_f32_16x16x32_bf16 v[0:3], v[224:227], v[52:55], v[0:3]
buffer_load_dwordx4 v[224:227], v20, %[rv], s91 offen offset:2048 nt
v_and_b32_dpp v52, v21, v9 row_newbcast:5 row_mask:0xf bank_mask:0x1
v_and_b32_dpp v53, v21, v9 row_newbcast:5 row_mask:0xf bank_mask:0x2
v_and_b32_dpp v54, v21, v9 row_newbcast:5 row_mask:0xf bank_mask:0x4
v_and_b32_dpp v55, v21, v9 row_newbcast:5 row_mask:0xf bank_mask:0x8
s_waitcnt vmcnt(13)
s_nop 0
v_mfma_f32_16x16x32_bf16 v[0:3], v[228:231], v[52:55], v[0:3]
buffer_load_dwordx4 v[228:231], v20, %[rv], s91 offen offset:3072 nt
v_and_b32_dpp v52, v21, v9 row_newbcast:6 row_mask:0xf bank_mask:0x1
v_and_b32_dpp v53, v21, v9 row_newbcast:6 row_mask:0xf bank_mask:0x2
v_and_b32_dpp v54, v21, v9 row_newbcast:6 row_mask:0xf bank_mask:0x4
v_and_b32_dpp v55, v21, v9 row_newbcast:6 row_mask:0xf bank_mask:0x8
s_waitcnt vmcnt(13)
s_nop 0
v_mfma_f32_16x16x32_bf16 v[0:3], v[236:239], v[52:55], v[0:3]
s_branch LP59_%=b
LP59_%=:
s_sleep 1
LP59_%=b:
s_memrealtime vcc
s_waitcnt lgkmcnt(0)
s_sub_u32 s91, vcc_lo, %[t0]
s_cmp_lt_u32 s91, 3853
s_cbranch_scc1 LP59_%=
s_cmp_lt_u32 s91, 3933
s_cbranch_scc0 LP59_%=f
s_waitcnt vmcnt(8)
s_cmp_lt_u32 s91, 3883
s_cbranch_scc0 LP59_%=f
s_waitcnt vmcnt(4)
s_nop 0
)" R"(
LP59_%=f:
s_mov_b32 s91, 114688
buffer_load_dwordx4 v[236:239], v20, %[rv], s91 offen offset:0 nt
v_and_b32_dpp v52, v21, v9 row_newbcast:7 row_mask:0xf bank_mask:0x1
v_and_b32_dpp v53, v21, v9 row_newbcast:7 row_mask:0xf bank_mask:0x2
v_and_b32_dpp v54, v21, v9 row_newbcast:7 row_mask:0xf bank_mask:0x4
v_and_b32_dpp v55, v21, v9 row_newbcast:7 row_mask:0xf bank_mask:0x8
s_waitcnt vmcnt(12)
s_nop 0
v_mfma_f32_16x16x32_bf16 v[0:3], v[244:247], v[52:55], v[0:3]
buffer_load_dwordx4 v[244:247], v20, %[rv], s91 offen offset:1024 nt
v_and_b32_dpp v52, v21, v9 row_newbcast:8 row_mask:0xf bank_mask:0x1
v_and_b32_dpp v53, v21, v9 row_newbcast:8 row_mask:0xf bank_mask:0x2
v_and_b32_dpp v54, v21, v9 row_newbcast:8 row_mask:0xf bank_mask:0x4
v_and_b32_dpp v55, v21, v9 row_newbcast:8 row_mask:0xf bank_mask:0x8
s_waitcnt vmcnt(12)
s_nop 0
v_mfma_f32_16x16x32_bf16 v[0:3], v[136:139], v[52:55], v[0:3]
buffer_load_dwordx4 v[136:139], v20, %[rv], s91 offen offset:2048 nt
v_and_b32_dpp v52, v21, v9 row_newbcast:9 row_mask:0xf bank_mask:0x1
v_and_b32_dpp v53, v21, v9 row_newbcast:9 row_mask:0xf bank_mask:0x2
v_and_b32_dpp v54, v21, v9 row_newbcast:9 row_mask:0xf bank_mask:0x4
v_and_b32_dpp v55, v21, v9 row_newbcast:9 row_mask:0xf bank_mask:0x8
s_waitcnt vmcnt(11)
s_nop 0
v_mfma_f32_16x16x32_bf16 v[0:3], v[140:143], v[52:55], v[0:3]
buffer_load_dwordx4 v[140:143], v20, %[rv], s91 offen offset:3072 nt
v_and_b32_dpp v52, v21, v9 row_newbcast:10 row_mask:0xf bank_mask:0x1
v_and_b32_dpp v53, v21, v9 row_newbcast:10 row_mask:0xf bank_mask:0x2
v_and_b32_dpp v54, v21, v9 row_newbcast:10 row_mask:0xf bank_mask:0x4
v_and_b32_dpp v55, v21, v9 row_newbcast:10 row_mask:0xf bank_mask:0x8
s_waitcnt vmcnt(11)
s_nop 0
v_mfma_f32_16x16x32_bf16 v[0:3], v[148:151], v[52:55], v[0:3]
s_branch LP60_%=b
LP60_%=:
s_sleep 1
LP60_%=b:
s_memrealtime vcc
s_waitcnt lgkmcnt(0)
s_sub_u32 s91, vcc_lo, %[t0]
s_cmp_lt_u32 s91, 3916
s_cbranch_scc1 LP60_%=
s_cmp_lt_u32 s91, 3996
s_cbranch_scc0 LP60_%=f
s_waitcnt vmcnt(8)
s_cmp_lt_u32 s91, 3946
s_cbranch_scc0 LP60_%=f
s_waitcnt vmcnt(4)
s_nop 0
LP60_%=f:
buffer_load_dword v148, v20, %[rc], 0 offen
s_mov_b32 s91, 118784
buffer_load_dwordx4 v[148:151], v20, %[rv], s91 offen offset:0 nt
v_and_b32_dpp v52, v21, v9 row_newbcast:11 row_mask:0xf bank_mask:0x1
v_and_b32_dpp v53, v21, v9 row_newbcast:11 row_mask:0xf bank_mask:0x2
v_and_b32_dpp v54, v21, v9 row_newbcast:11 row_mask:0xf bank_mask:0x4
v_and_b32_dpp v55, v21, v9 row_newbcast:11 row_mask:0xf bank_mask:0x8
s_waitcnt vmcnt(11)
s_nop 0
v_mfma_f32_16x16x32_bf16 v[0:3], v[168:171], v[52:55], v[0:3]
buffer_load_dwordx4 v[168:171], v20, %[rv], s91 offen offset:1024 nt
buffer_load_dwordx4 v[36:39], v20, %[rv], s91 offen offset:2048
buffer_load_dwordx4 v[40:43], v20, %[rv], s91 offen offset:3072
v_and_b32_dpp v52, v21, v9 row_newbcast:12 row_mask:0xf bank_mask:0x1
v_and_b32_dpp v53, v21, v9 row_newbcast:12 row_mask:0xf bank_mask:0x2
v_and_b32_dpp v54, v21, v9 row_newbcast:12 row_mask:0xf bank_mask:0x4
v_and_b32_dpp v55, v21, v9 row_newbcast:12 row_mask:0xf bank_mask:0x8
s_waitcnt vmcnt(13)
s_nop 0
v_mfma_f32_16x16x32_bf16 v[0:3], v[184:187], v[52:55], v[0:3]
s_movk_i32 s94, 15104
s_nop 0
v_perm_b32 v104, v185, v184, s96
v_perm_b32 v105, v187, v186, s96
v_perm_b32 v106, v185, v184, s97
v_perm_b32 v107, v187, v186, s97
v_pk_mad_u16 v108, v184, 2, -1 op_sel_hi:[1,0,0]
v_pk_mad_u16 v109, v185, 2, -1 op_sel_hi:[1,0,0]
v_pk_mad_u16 v110, v186, 2, -1 op_sel_hi:[1,0,0]
v_pk_mad_u16 v111, v187, 2, -1 op_sel_hi:[1,0,0]
v_and_b32 v112, 0x7f7f7f7f, v104
v_and_b32 v113, 0x7f7f7f7f, v105
v_sub_u32 v112, s82, v112
v_sub_u32 v113, s82, v113
v_pk_min_u16 v108, v108, v109
v_pk_min_u16 v110, v110, v111
v_pk_min_u16 v108, v108, v110
v_bitop3_b32 v109, v112, v113, s98 bitop3:0xa8
v_pk_sub_u16 v108, s83, v108 clamp
v_cmp_ne_u32_e64 vcc, v108, v109
v_perm_b32 v112, s99, v115, v112
v_perm_b32 v113, s99, v115, v113
v_bitop3_b32 v112, v112, v104, s98 bitop3:0x8d
v_bitop3_b32 v113, v113, v105, s98 bitop3:0x8d
v_alignbit_b32 v113, v113, v113, 28
v_or_b32 v114, v112, v113
s_getpc_b64 s[92:93]
s_cbranch_vccnz Lexc
v_mov_b32 v206, v114
v_mov_b32 v207, v106
v_mov_b32 v184, v107
v_and_b32_dpp v52, v21, v9 row_newbcast:13 row_mask:0xf bank_mask:0x1
v_and_b32_dpp v53, v21, v9 row_newbcast:13 row_mask:0xf bank_mask:0x2
v_and_b32_dpp v54, v21, v9 row_newbcast:13 row_mask:0xf bank_mask:0x4
v_and_b32_dpp v55, v21, v9 row_newbcast:13 row_mask:0xf bank_mask:0x8
s_waitcnt vmcnt(11)
s_nop 0
v_mfma_f32_16x16x32_bf16 v[0:3], v[188:191], v[52:55], v[0:3]
s_movk_i32 s94, 15168
s_nop 0
v_perm_b32 v104, v189, v188, s96
v_perm_b32 v105, v191, v190, s96
v_perm_b32 v106, v189, v188, s97
v_perm_b32 v107, v191, v190, s97
v_pk_mad_u16 v108, v188, 2, -1 op_sel_hi:[1,0,0]
v_pk_mad_u16 v109, v189, 2, -1 op_sel_hi:[1,0,0]
v_pk_mad_u16 v110, v190, 2, -1 op_sel_hi:[1,0,0]
v_pk_mad_u16 v111, v191, 2, -1 op_sel_hi:[1,0,0]
v_and_b32 v112, 0x7f7f7f7f, v104
v_and_b32 v113, 0x7f7f7f7f, v105
v_sub_u32 v112, s82, v112
v_sub_u32 v113, s82, v113
v_pk_min_u16 v108, v108, v109
v_pk_min_u16 v110, v110, v111
v_pk_min_u16 v108, v108, v110
v_bitop3_b32 v109, v112, v113, s98 bitop3:0xa8
v_pk_sub_u16 v108, s83, v108 clamp
v_cmp_ne_u32_e64 vcc, v108, v109
v_perm_b32 v112, s99, v115, v112
v_perm_b32 v113, s99, v115, v113
v_bitop3_b32 v112, v112, v104, s98 bitop3:0x8d
v_bitop3_b32 v113, v113, v105, s98 bitop3:0x8d
v_alignbit_b32 v113, v113, v113, 28
v_or_b32 v114, v112, v113
s_getpc_b64 s[92:93]
s_cbranch_vccnz Lexc
v_mov_b32 v185, v114
v_mov_b32 v186, v106
v_mov_b32 v187, v107
s_branch LP61_%=b
LP61_%=:
s_sleep 1
LP61_%=b:
s_memrealtime vcc
s_waitcnt lgkmcnt(0)
s_sub_u32 s91, vcc_lo, %[t0]
s_cmp_lt_u32 s91, 3980
s_cbranch_scc1 LP61_%=
s_cmp_lt_u32 s91, 4060
s_cbranch_scc0 LP61_%=f
s_waitcnt vmcnt(8)
s_cmp_lt_u32 s91, 4010
s_cbranch_scc0 LP61_%=f
s_waitcnt vmcnt(4)
s_nop 0
LP61_%=f:
s_mov_b32 s91, 122880
buffer_load_dwordx4 v[188:191], v20, %[rv], s91 offen offset:0 nt
v_and_b32_dpp v52, v21, v9 row_newbcast:14 row_mask:0xf bank_mask:0x1
v_and_b32_dpp v53, v21, v9 row_newbcast:14 row_mask:0xf bank_mask:0x2
v_and_b32_dpp v54, v21, v9 row_newbcast:14 row_mask:0xf bank_mask:0x4
v_and_b32_dpp v55, v21, v9 row_newbcast:14 row_mask:0xf bank_mask:0x8
s_waitcnt vmcnt(11)
s_nop 0
v_mfma_f32_16x16x32_bf16 v[0:3], v[224:227], v[52:55], v[0:3]
s_movk_i32 s94, 15232
s_nop 0
v_perm_b32 v104, v225, v224, s96
v_perm_b32 v105, v227, v226, s96
v_perm_b32 v106, v225, v224, s97
v_perm_b32 v107, v227, v226, s97
v_pk_mad_u16 v108, v224, 2, -1 op_sel_hi:[1,0,0]
v_pk_mad_u16 v109, v225, 2, -1 op_sel_hi:[1,0,0]
v_pk_mad_u16 v110, v226, 2, -1 op_sel_hi:[1,0,0]
v_pk_mad_u16 v111, v227, 2, -1 op_sel_hi:[1,0,0]
v_and_b32 v112, 0x7f7f7f7f, v104
v_and_b32 v113, 0x7f7f7f7f, v105
v_sub_u32 v112, s82, v112
v_sub_u32 v113, s82, v113
v_pk_min_u16 v108, v108, v109
v_pk_min_u16 v110, v110, v111
v_pk_min_u16 v108, v108, v110
v_bitop3_b32 v109, v112, v113, s98 bitop3:0xa8
v_pk_sub_u16 v108, s83, v108 clamp
v_cmp_ne_u32_e64 vcc, v108, v109
v_perm_b32 v112, s99, v115, v112
v_perm_b32 v113, s99, v115, v113
v_bitop3_b32 v112, v112, v104, s98 bitop3:0x8d
v_bitop3_b32 v113, v113, v105, s98 bitop3:0x8d
v_alignbit_b32 v113, v113, v113, 28
v_or_b32 v114, v112, v113
s_getpc_b64 s[92:93]
s_cbranch_vccnz Lexc
v_mov_b32 v224, v114
v_mov_b32 v225, v106
v_mov_b32 v226, v107
v_and_b32_dpp v52, v21, v9 row_newbcast:15 row_mask:0xf bank_mask:0x1
v_and_b32_dpp v53, v21, v9 row_newbcast:15 row_mask:0xf bank_mask:0x2
v_and_b32_dpp v54, v21, v9 row_newbcast:15 row_mask:0xf bank_mask:0x4
v_and_b32_dpp v55, v21, v9 row_newbcast:15 row_mask:0xf bank_mask:0x8
s_waitcnt vmcnt(10)
s_nop 0
v_mfma_f32_16x16x32_bf16 v[0:3], v[228:231], v[52:55], v[0:3]
s_movk_i32 s94, 15296
s_nop 0
v_perm_b32 v104, v229, v228, s96
v_perm_b32 v105, v231, v230, s96
v_perm_b32 v106, v229, v228, s97
v_perm_b32 v107, v231, v230, s97
v_pk_mad_u16 v108, v228, 2, -1 op_sel_hi:[1,0,0]
v_pk_mad_u16 v109, v229, 2, -1 op_sel_hi:[1,0,0]
v_pk_mad_u16 v110, v230, 2, -1 op_sel_hi:[1,0,0]
v_pk_mad_u16 v111, v231, 2, -1 op_sel_hi:[1,0,0]
v_and_b32 v112, 0x7f7f7f7f, v104
v_and_b32 v113, 0x7f7f7f7f, v105
v_sub_u32 v112, s82, v112
v_sub_u32 v113, s82, v113
v_pk_min_u16 v108, v108, v109
v_pk_min_u16 v110, v110, v111
v_pk_min_u16 v108, v108, v110
v_bitop3_b32 v109, v112, v113, s98 bitop3:0xa8
v_pk_sub_u16 v108, s83, v108 clamp
v_cmp_ne_u32_e64 vcc, v108, v109
v_perm_b32 v112, s99, v115, v112
v_perm_b32 v113, s99, v115, v113
v_bitop3_b32 v112, v112, v104, s98 bitop3:0x8d
v_bitop3_b32 v113, v113, v105, s98 bitop3:0x8d
v_alignbit_b32 v113, v113, v113, 28
v_or_b32 v114, v112, v113
s_getpc_b64 s[92:93]
s_cbranch_vccnz Lexc
v_mov_b32 v227, v114
v_mov_b32 v228, v106
v_mov_b32 v229, v107
v_and_b32_dpp v52, v22, v9 row_newbcast:0 row_mask:0xf bank_mask:0x1
v_and_b32_dpp v53, v22, v9 row_newbcast:0 row_mask:0xf bank_mask:0x2
v_and_b32_dpp v54, v22, v9 row_newbcast:0 row_mask:0xf bank_mask:0x4
v_and_b32_dpp v55, v22, v9 row_newbcast:0 row_mask:0xf bank_mask:0x8
s_waitcnt vmcnt(9)
s_nop 0
v_mfma_f32_16x16x32_bf16 v[0:3], v[236:239], v[52:55], v[0:3]
s_movk_i32 s94, 15360
s_nop 0
v_perm_b32 v104, v237, v236, s96
v_perm_b32 v105, v239, v238, s96
v_perm_b32 v106, v237, v236, s97
v_perm_b32 v107, v239, v238, s97
v_pk_mad_u16 v108, v236, 2, -1 op_sel_hi:[1,0,0]
v_pk_mad_u16 v109, v237, 2, -1 op_sel_hi:[1,0,0]
v_pk_mad_u16 v110, v238, 2, -1 op_sel_hi:[1,0,0]
v_pk_mad_u16 v111, v239, 2, -1 op_sel_hi:[1,0,0]
v_and_b32 v112, 0x7f7f7f7f, v104
v_and_b32 v113, 0x7f7f7f7f, v105
v_sub_u32 v112, s82, v112
v_sub_u32 v113, s82, v113
v_pk_min_u16 v108, v108, v109
v_pk_min_u16 v110, v110, v111
v_pk_min_u16 v108, v108, v110
v_bitop3_b32 v109, v112, v113, s98 bitop3:0xa8
v_pk_sub_u16 v108, s83, v108 clamp
v_cmp_ne_u32_e64 vcc, v108, v109
v_perm_b32 v112, s99, v115, v112
v_perm_b32 v113, s99, v115, v113
v_bitop3_b32 v112, v112, v104, s98 bitop3:0x8d
v_bitop3_b32 v113, v113, v105, s98 bitop3:0x8d
v_alignbit_b32 v113, v113, v113, 28
v_or_b32 v114, v112, v113
s_getpc_b64 s[92:93]
s_cbranch_vccnz Lexc
v_mov_b32 v230, v114
v_mov_b32 v231, v106
v_mov_b32 v236, v107
v_and_b32_dpp v52, v22, v9 row_newbcast:1 row_mask:0xf bank_mask:0x1
v_and_b32_dpp v53, v22, v9 row_newbcast:1 row_mask:0xf bank_mask:0x2
v_and_b32_dpp v54, v22, v9 row_newbcast:1 row_mask:0xf bank_mask:0x4
v_and_b32_dpp v55, v22, v9 row_newbcast:1 row_mask:0xf bank_mask:0x8
s_waitcnt vmcnt(8)
s_nop 0
v_mfma_f32_16x16x32_bf16 v[0:3], v[244:247], v[52:55], v[0:3]
s_movk_i32 s94, 15424
s_nop 0
v_perm_b32 v104, v245, v244, s96
v_perm_b32 v105, v247, v246, s96
v_perm_b32 v106, v245, v244, s97
v_perm_b32 v107, v247, v246, s97
v_pk_mad_u16 v108, v244, 2, -1 op_sel_hi:[1,0,0]
v_pk_mad_u16 v109, v245, 2, -1 op_sel_hi:[1,0,0]
v_pk_mad_u16 v110, v246, 2, -1 op_sel_hi:[1,0,0]
v_pk_mad_u16 v111, v247, 2, -1 op_sel_hi:[1,0,0]
v_and_b32 v112, 0x7f7f7f7f, v104
v_and_b32 v113, 0x7f7f7f7f, v105
v_sub_u32 v112, s82, v112
v_sub_u32 v113, s82, v113
v_pk_min_u16 v108, v108, v109
v_pk_min_u16 v110, v110, v111
v_pk_min_u16 v108, v108, v110
v_bitop3_b32 v109, v112, v113, s98 bitop3:0xa8
v_pk_sub_u16 v108, s83, v108 clamp
v_cmp_ne_u32_e64 vcc, v108, v109
v_perm_b32 v112, s99, v115, v112
v_perm_b32 v113, s99, v115, v113
v_bitop3_b32 v112, v112, v104, s98 bitop3:0x8d
v_bitop3_b32 v113, v113, v105, s98 bitop3:0x8d
v_alignbit_b32 v113, v113, v113, 28
v_or_b32 v114, v112, v113
s_getpc_b64 s[92:93]
s_cbranch_vccnz Lexc
v_mov_b32 v237, v114
v_mov_b32 v238, v106
v_mov_b32 v239, v107
buffer_load_dwordx4 v[244:247], v20, %[rv], s91 offen offset:1024 nt
buffer_load_dwordx4 v[44:47], v20, %[rv], s91 offen offset:2048
buffer_load_dwordx4 v[48:51], v20, %[rv], s91 offen offset:3072
v_and_b32_dpp v52, v22, v9 row_newbcast:2 row_mask:0xf bank_mask:0x1
v_and_b32_dpp v53, v22, v9 row_newbcast:2 row_mask:0xf bank_mask:0x2
v_and_b32_dpp v54, v22, v9 row_newbcast:2 row_mask:0xf bank_mask:0x4
v_and_b32_dpp v55, v22, v9 row_newbcast:2 row_mask:0xf bank_mask:0x8
s_waitcnt vmcnt(10)
s_nop 0
v_mfma_f32_16x16x32_bf16 v[0:3], v[136:139], v[52:55], v[0:3]
)" R"(
s_movk_i32 s94, 15488
s_nop 0
v_perm_b32 v104, v137, v136, s96
v_perm_b32 v105, v139, v138, s96
v_perm_b32 v106, v137, v136, s97
v_perm_b32 v107, v139, v138, s97
v_pk_mad_u16 v108, v136, 2, -1 op_sel_hi:[1,0,0]
v_pk_mad_u16 v109, v137, 2, -1 op_sel_hi:[1,0,0]
v_pk_mad_u16 v110, v138, 2, -1 op_sel_hi:[1,0,0]
v_pk_mad_u16 v111, v139, 2, -1 op_sel_hi:[1,0,0]
v_and_b32 v112, 0x7f7f7f7f, v104
v_and_b32 v113, 0x7f7f7f7f, v105
v_sub_u32 v112, s82, v112
v_sub_u32 v113, s82, v113
v_pk_min_u16 v108, v108, v109
v_pk_min_u16 v110, v110, v111
v_pk_min_u16 v108, v108, v110
v_bitop3_b32 v109, v112, v113, s98 bitop3:0xa8
v_pk_sub_u16 v108, s83, v108 clamp
v_cmp_ne_u32_e64 vcc, v108, v109
v_perm_b32 v112, s99, v115, v112
v_perm_b32 v113, s99, v115, v113
v_bitop3_b32 v112, v112, v104, s98 bitop3:0x8d
v_bitop3_b32 v113, v113, v105, s98 bitop3:0x8d
v_alignbit_b32 v113, v113, v113, 28
v_or_b32 v114, v112, v113
s_getpc_b64 s[92:93]
s_cbranch_vccnz Lexc
v_mov_b32 v136, v114
v_mov_b32 v137, v106
v_mov_b32 v138, v107
v_and_b32_dpp v52, v22, v9 row_newbcast:3 row_mask:0xf bank_mask:0x1
v_and_b32_dpp v53, v22, v9 row_newbcast:3 row_mask:0xf bank_mask:0x2
v_and_b32_dpp v54, v22, v9 row_newbcast:3 row_mask:0xf bank_mask:0x4
v_and_b32_dpp v55, v22, v9 row_newbcast:3 row_mask:0xf bank_mask:0x8
s_waitcnt vmcnt(9)
s_nop 0
v_mfma_f32_16x16x32_bf16 v[0:3], v[140:143], v[52:55], v[0:3]
s_movk_i32 s94, 15552
s_nop 0
v_perm_b32 v104, v141, v140, s96
v_perm_b32 v105, v143, v142, s96
v_perm_b32 v106, v141, v140, s97
v_perm_b32 v107, v143, v142, s97
v_pk_mad_u16 v108, v140, 2, -1 op_sel_hi:[1,0,0]
v_pk_mad_u16 v109, v141, 2, -1 op_sel_hi:[1,0,0]
v_pk_mad_u16 v110, v142, 2, -1 op_sel_hi:[1,0,0]
v_pk_mad_u16 v111, v143, 2, -1 op_sel_hi:[1,0,0]
v_and_b32 v112, 0x7f7f7f7f, v104
v_and_b32 v113, 0x7f7f7f7f, v105
v_sub_u32 v112, s82, v112
v_sub_u32 v113, s82, v113
v_pk_min_u16 v108, v108, v109
v_pk_min_u16 v110, v110, v111
v_pk_min_u16 v108, v108, v110
v_bitop3_b32 v109, v112, v113, s98 bitop3:0xa8
v_pk_sub_u16 v108, s83, v108 clamp
v_cmp_ne_u32_e64 vcc, v108, v109
v_perm_b32 v112, s99, v115, v112
v_perm_b32 v113, s99, v115, v113
v_bitop3_b32 v112, v112, v104, s98 bitop3:0x8d
v_bitop3_b32 v113, v113, v105, s98 bitop3:0x8d
v_alignbit_b32 v113, v113, v113, 28
v_or_b32 v114, v112, v113
s_getpc_b64 s[92:93]
s_cbranch_vccnz Lexc
v_mov_b32 v139, v114
v_mov_b32 v140, v106
v_mov_b32 v141, v107
v_and_b32_dpp v52, v22, v9 row_newbcast:4 row_mask:0xf bank_mask:0x1
v_and_b32_dpp v53, v22, v9 row_newbcast:4 row_mask:0xf bank_mask:0x2
v_and_b32_dpp v54, v22, v9 row_newbcast:4 row_mask:0xf bank_mask:0x4
v_and_b32_dpp v55, v22, v9 row_newbcast:4 row_mask:0xf bank_mask:0x8
s_waitcnt vmcnt(7)
s_nop 0
v_mfma_f32_16x16x32_bf16 v[0:3], v[148:151], v[52:55], v[0:3]
s_movk_i32 s94, 15616
s_nop 0
v_perm_b32 v104, v149, v148, s96
v_perm_b32 v105, v151, v150, s96
v_perm_b32 v106, v149, v148, s97
v_perm_b32 v107, v151, v150, s97
v_pk_mad_u16 v108, v148, 2, -1 op_sel_hi:[1,0,0]
v_pk_mad_u16 v109, v149, 2, -1 op_sel_hi:[1,0,0]
v_pk_mad_u16 v110, v150, 2, -1 op_sel_hi:[1,0,0]
v_pk_mad_u16 v111, v151, 2, -1 op_sel_hi:[1,0,0]
v_and_b32 v112, 0x7f7f7f7f, v104
v_and_b32 v113, 0x7f7f7f7f, v105
v_sub_u32 v112, s82, v112
v_sub_u32 v113, s82, v113
v_pk_min_u16 v108, v108, v109
v_pk_min_u16 v110, v110, v111
v_pk_min_u16 v108, v108, v110
v_bitop3_b32 v109, v112, v113, s98 bitop3:0xa8
v_pk_sub_u16 v108, s83, v108 clamp
v_cmp_ne_u32_e64 vcc, v108, v109
v_perm_b32 v112, s99, v115, v112
v_perm_b32 v113, s99, v115, v113
v_bitop3_b32 v112, v112, v104, s98 bitop3:0x8d
v_bitop3_b32 v113, v113, v105, s98 bitop3:0x8d
v_alignbit_b32 v113, v113, v113, 28
v_or_b32 v114, v112, v113
s_getpc_b64 s[92:93]
s_cbranch_vccnz Lexc
v_mov_b32 v142, v114
v_mov_b32 v143, v106
v_mov_b32 v148, v107
v_and_b32_dpp v52, v22, v9 row_newbcast:5 row_mask:0xf bank_mask:0x1
v_and_b32_dpp v53, v22, v9 row_newbcast:5 row_mask:0xf bank_mask:0x2
v_and_b32_dpp v54, v22, v9 row_newbcast:5 row_mask:0xf bank_mask:0x4
v_and_b32_dpp v55, v22, v9 row_newbcast:5 row_mask:0xf bank_mask:0x8
s_waitcnt vmcnt(6)
s_nop 0
v_mfma_f32_16x16x32_bf16 v[0:3], v[168:171], v[52:55], v[0:3]
s_movk_i32 s94, 15680
s_nop 0
v_perm_b32 v104, v169, v168, s96
v_perm_b32 v105, v171, v170, s96
v_perm_b32 v106, v169, v168, s97
v_perm_b32 v107, v171, v170, s97
v_pk_mad_u16 v108, v168, 2, -1 op_sel_hi:[1,0,0]
v_pk_mad_u16 v109, v169, 2, -1 op_sel_hi:[1,0,0]
v_pk_mad_u16 v110, v170, 2, -1 op_sel_hi:[1,0,0]
v_pk_mad_u16 v111, v171, 2, -1 op_sel_hi:[1,0,0]
v_and_b32 v112, 0x7f7f7f7f, v104
v_and_b32 v113, 0x7f7f7f7f, v105
v_sub_u32 v112, s82, v112
v_sub_u32 v113, s82, v113
v_pk_min_u16 v108, v108, v109
v_pk_min_u16 v110, v110, v111
v_pk_min_u16 v108, v108, v110
v_bitop3_b32 v109, v112, v113, s98 bitop3:0xa8
v_pk_sub_u16 v108, s83, v108 clamp
v_cmp_ne_u32_e64 vcc, v108, v109
v_perm_b32 v112, s99, v115, v112
v_perm_b32 v113, s99, v115, v113
v_bitop3_b32 v112, v112, v104, s98 bitop3:0x8d
v_bitop3_b32 v113, v113, v105, s98 bitop3:0x8d
v_alignbit_b32 v113, v113, v113, 28
v_or_b32 v114, v112, v113
s_getpc_b64 s[92:93]
s_cbranch_vccnz Lexc
v_mov_b32 v149, v114
v_mov_b32 v150, v106
v_mov_b32 v151, v107
s_branch LP62_%=b
LP62_%=:
s_sleep 1
LP62_%=b:
s_memrealtime vcc
s_waitcnt lgkmcnt(0)
s_sub_u32 s91, vcc_lo, %[t0]
s_cmp_lt_u32 s91, 4045
s_cbranch_scc1 LP62_%=
s_cmp_lt_u32 s91, 4075
s_cbranch_scc0 LP62_%=f
s_waitcnt vmcnt(4)
s_nop 0
LP62_%=f:
s_mov_b32 s91, 126976
buffer_load_dwordx4 v[168:171], v20, %[rv], s91 offen offset:0 nt
buffer_load_dwordx4 v[104:107], v20, %[rv], s91 offen offset:1024 nt
buffer_load_dwordx4 v[108:111], v20, %[rv], s91 offen offset:2048 nt
v_and_b32_dpp v52, v22, v9 row_newbcast:6 row_mask:0xf bank_mask:0x1
v_and_b32_dpp v53, v22, v9 row_newbcast:6 row_mask:0xf bank_mask:0x2
v_and_b32_dpp v54, v22, v9 row_newbcast:6 row_mask:0xf bank_mask:0x4
v_and_b32_dpp v55, v22, v9 row_newbcast:6 row_mask:0xf bank_mask:0x8
s_waitcnt vmcnt(8)
s_nop 0
v_mfma_f32_16x16x32_bf16 v[0:3], v[36:39], v[52:55], v[0:3]
v_and_b32_dpp v52, v22, v9 row_newbcast:7 row_mask:0xf bank_mask:0x1
v_and_b32_dpp v53, v22, v9 row_newbcast:7 row_mask:0xf bank_mask:0x2
v_and_b32_dpp v54, v22, v9 row_newbcast:7 row_mask:0xf bank_mask:0x4
v_and_b32_dpp v55, v22, v9 row_newbcast:7 row_mask:0xf bank_mask:0x8
s_waitcnt vmcnt(7)
s_nop 0
v_mfma_f32_16x16x32_bf16 v[0:3], v[40:43], v[52:55], v[0:3]
buffer_load_dwordx4 v[112:115], v20, %[rv], s91 offen offset:3072 nt
v_and_b32_dpp v52, v22, v9 row_newbcast:8 row_mask:0xf bank_mask:0x1
v_and_b32_dpp v53, v22, v9 row_newbcast:8 row_mask:0xf bank_mask:0x2
v_and_b32_dpp v54, v22, v9 row_newbcast:8 row_mask:0xf bank_mask:0x4
v_and_b32_dpp v55, v22, v9 row_newbcast:8 row_mask:0xf bank_mask:0x8
s_waitcnt vmcnt(7)
s_nop 0
v_mfma_f32_16x16x32_bf16 v[0:3], v[188:191], v[52:55], v[0:3]
v_and_b32_dpp v52, v22, v9 row_newbcast:9 row_mask:0xf bank_mask:0x1
v_and_b32_dpp v53, v22, v9 row_newbcast:9 row_mask:0xf bank_mask:0x2
v_and_b32_dpp v54, v22, v9 row_newbcast:9 row_mask:0xf bank_mask:0x4
v_and_b32_dpp v55, v22, v9 row_newbcast:9 row_mask:0xf bank_mask:0x8
s_waitcnt vmcnt(6)
s_nop 0
v_mfma_f32_16x16x32_bf16 v[0:3], v[244:247], v[52:55], v[0:3]
v_and_b32_dpp v52, v22, v9 row_newbcast:10 row_mask:0xf bank_mask:0x1
v_and_b32_dpp v53, v22, v9 row_newbcast:10 row_mask:0xf bank_mask:0x2
v_and_b32_dpp v54, v22, v9 row_newbcast:10 row_mask:0xf bank_mask:0x4
v_and_b32_dpp v55, v22, v9 row_newbcast:10 row_mask:0xf bank_mask:0x8
s_waitcnt vmcnt(5)
s_nop 0
v_mfma_f32_16x16x32_bf16 v[0:3], v[44:47], v[52:55], v[0:3]
v_and_b32_dpp v52, v22, v9 row_newbcast:11 row_mask:0xf bank_mask:0x1
v_and_b32_dpp v53, v22, v9 row_newbcast:11 row_mask:0xf bank_mask:0x2
v_and_b32_dpp v54, v22, v9 row_newbcast:11 row_mask:0xf bank_mask:0x4
v_and_b32_dpp v55, v22, v9 row_newbcast:11 row_mask:0xf bank_mask:0x8
s_waitcnt vmcnt(4)
s_nop 0
v_mfma_f32_16x16x32_bf16 v[0:3], v[48:51], v[52:55], v[0:3]
v_and_b32_dpp v52, v22, v9 row_newbcast:12 row_mask:0xf bank_mask:0x1
v_and_b32_dpp v53, v22, v9 row_newbcast:12 row_mask:0xf bank_mask:0x2
v_and_b32_dpp v54, v22, v9 row_newbcast:12 row_mask:0xf bank_mask:0x4
v_and_b32_dpp v55, v22, v9 row_newbcast:12 row_mask:0xf bank_mask:0x8
s_waitcnt vmcnt(3)
s_nop 0
v_mfma_f32_16x16x32_bf16 v[0:3], v[168:171], v[52:55], v[0:3]
v_and_b32_dpp v52, v22, v9 row_newbcast:13 row_mask:0xf bank_mask:0x1
v_and_b32_dpp v53, v22, v9 row_newbcast:13 row_mask:0xf bank_mask:0x2
v_and_b32_dpp v54, v22, v9 row_newbcast:13 row_mask:0xf bank_mask:0x4
v_and_b32_dpp v55, v22, v9 row_newbcast:13 row_mask:0xf bank_mask:0x8
s_waitcnt vmcnt(2)
s_nop 0
v_mfma_f32_16x16x32_bf16 v[0:3], v[104:107], v[52:55], v[0:3]
v_and_b32_dpp v52, v22, v9 row_newbcast:14 row_mask:0xf bank_mask:0x1
v_and_b32_dpp v53, v22, v9 row_newbcast:14 row_mask:0xf bank_mask:0x2
v_and_b32_dpp v54, v22, v9 row_newbcast:14 row_mask:0xf bank_mask:0x4
v_and_b32_dpp v55, v22, v9 row_newbcast:14 row_mask:0xf bank_mask:0x8
s_waitcnt vmcnt(1)
s_nop 0
v_mfma_f32_16x16x32_bf16 v[0:3], v[108:111], v[52:55], v[0:3]
v_and_b32_dpp v52, v22, v9 row_newbcast:15 row_mask:0xf bank_mask:0x1
v_and_b32_dpp v53, v22, v9 row_newbcast:15 row_mask:0xf bank_mask:0x2
v_and_b32_dpp v54, v22, v9 row_newbcast:15 row_mask:0xf bank_mask:0x4
v_and_b32_dpp v55, v22, v9 row_newbcast:15 row_mask:0xf bank_mask:0x8
s_waitcnt vmcnt(0)
s_nop 0
v_mfma_f32_16x16x32_bf16 v[0:3], v[112:115], v[52:55], v[0:3]
s_mov_b32 exec_lo, 0x33333333
s_mov_b32 exec_hi, 0x33333333
s_nop 5
v_pk_mul_f32 v[0:1], v[0:1], v[4:5] op_sel_hi:[1,0]
v_pk_mul_f32 v[2:3], v[2:3], v[4:5] op_sel_hi:[1,0]
ds_write_b128 v10, v[0:3]
s_mov_b64 exec, 0xff
s_waitcnt lgkmcnt(0)
s_barrier
ds_read_b128 v[36:39], v11 offset:0
ds_read_b128 v[40:43], v11 offset:1024
ds_read_b128 v[44:47], v11 offset:2048
ds_read_b128 v[48:51], v11 offset:3072
s_waitcnt lgkmcnt(2)
v_pk_add_f32 v[36:37], v[36:37], v[40:41]
v_pk_add_f32 v[38:39], v[38:39], v[42:43]
s_waitcnt lgkmcnt(0)
v_pk_add_f32 v[44:45], v[44:45], v[48:49]
v_pk_add_f32 v[46:47], v[46:47], v[50:51]
v_pk_add_f32 v[36:37], v[36:37], v[44:45]
v_pk_add_f32 v[38:39], v[38:39], v[46:47]
v_min_u32 v52, -2, v36
v_min_u32 v53, -2, v37
v_min_u32 v54, -2, v38
v_min_u32_e64 v55, -2, v39
buffer_store_dwordx4 v[52:55], v12, %[rw], s68 offen sc1
s_mov_b64 exec, -1
s_add_u32 s87, s69, 0
s_add_u32 s88, s69, 4096
s_movk_i32 s84, 31
s_branch LmR2_%=p
LmR2_%=r:
s_mov_b64 exec, vcc
s_sub_u32 s84, s84, 1
s_cbranch_scc1 LmP4_%=
buffer_load_dwordx4 v[36:39], v32, %[rw], s87 offen offset:0 sc1
buffer_load_dwordx4 v[40:43], v32, %[rw], s87 offen offset:2048 sc1
buffer_load_dwordx4 v[44:47], v32, %[rw], s88 offen offset:0 sc1
buffer_load_dwordx4 v[48:51], v32, %[rw], s88 offen offset:2048 sc1
s_branch LmR2_%=w
LmR2_%=p:
s_add_u32 %[g], %[g], 4245
s_memrealtime s[92:93]
s_waitcnt lgkmcnt(0)
s_sub_u32 s92, s92, %[g]
s_cmp_lt_i32 s92, 12
s_cbranch_scc1 LmW1_%=a
s_branch LmR2_%=
LmW1_%=:
s_memrealtime s[92:93]
s_waitcnt lgkmcnt(0)
s_sub_u32 s92, s92, %[g]
LmW1_%=a:
s_cmp_lt_i32 s92, 0
s_cbranch_scc1 LmW1_%=
LmR2_%=:
buffer_load_dwordx4 v[36:39], v32, %[rw], s87 offen offset:0 sc1
buffer_load_dwordx4 v[40:43], v32, %[rw], s87 offen offset:2048 sc1
buffer_load_dwordx4 v[44:47], v32, %[rw], s88 offen offset:0 sc1
buffer_load_dwordx4 v[48:51], v32, %[rw], s88 offen offset:2048 sc1
v_mov_b32 v7, v16
v_mov_b32 v52, v17
v_mov_b32 v53, v18
v_mov_b32_e64 v54, v19
s_mov_b32 s85, 0x05040100
s_mov_b32 s86, 0x07060302
s_mov_b32 s89, 0x3b000000
v_mov_b32 v4, 0x3f803f80
v_mov_b32_e64 v55, 0
s_mov_b32 s90, 0x3727c5ac
v_writelane_b32 v55, s90, 32
v_lshlrev_b32_e64 v0, 16, v5
v_and_b32 v1, 0xffff0000, v5
v_perm_b32 v2, v28, v24, s85
v_perm_b32 v28, v28, v24, s86
v_mov_b32_e64 v24, v2
v_perm_b32 v2, v29, v25, s85
v_perm_b32 v29, v29, v25, s86
v_mov_b32_e64 v25, v2
v_perm_b32 v2, v30, v26, s85
v_perm_b32 v30, v30, v26, s86
v_mov_b32_e64 v26, v2
v_perm_b32 v2, v31, v27, s85
v_perm_b32 v31, v31, v27, s86
v_mov_b32_e64 v27, v2
v_perm_b32 v2, v92, v88, s85
v_perm_b32 v92, v92, v88, s86
v_mov_b32_e64 v88, v2
v_perm_b32 v2, v93, v89, s85
v_perm_b32 v93, v93, v89, s86
v_mov_b32_e64 v89, v2
)" R"(
v_perm_b32 v2, v94, v90, s85
v_perm_b32 v94, v94, v90, s86
v_mov_b32_e64 v90, v2
v_perm_b32 v2, v95, v91, s85
v_perm_b32 v95, v95, v91, s86
v_mov_b32_e64 v91, v2
v_perm_b32 v2, v100, v96, s85
v_perm_b32 v100, v100, v96, s86
v_mov_b32_e64 v96, v2
v_perm_b32 v2, v101, v97, s85
v_perm_b32 v101, v101, v97, s86
v_mov_b32_e64 v97, v2
v_perm_b32 v2, v102, v98, s85
v_perm_b32 v102, v102, v98, s86
v_mov_b32_e64 v98, v2
v_perm_b32 v2, v103, v99, s85
v_perm_b32 v103, v103, v99, s86
v_mov_b32 v99, v2
LmR2_%=w:
s_waitcnt vmcnt(2)
v_pk_add_f32 v[8:9], v[36:37], v[40:41]
v_pk_add_f32 v[10:11], v[38:39], v[42:43]
s_waitcnt vmcnt(0)
s_nop 0
v_pk_add_f32 v[12:13], v[44:45], v[48:49]
v_pk_add_f32 v[14:15], v[46:47], v[50:51]
v_pk_add_f32 v[8:9], v[8:9], v[12:13]
v_pk_add_f32 v[10:11], v[10:11], v[14:15]
v_pk_add_f32 v[12:13], v[8:9], v[10:11]
v_cmp_u_f32 vcc, v12, v13
s_cbranch_vccnz LmR2_%=r
s_mov_b64 exec, -1
s_nop 0
LmD3_%=:
v_add_f32_dpp v16, v8, v8 row_ror:8 row_mask:0xf bank_mask:0x3
v_add_f32_dpp v17, v9, v9 row_ror:8 row_mask:0xf bank_mask:0x3
v_add_f32_dpp v16, v10, v10 row_ror:8 row_mask:0xf bank_mask:0xc
v_add_f32_dpp v17, v11, v11 row_ror:8 row_mask:0xf bank_mask:0xc
v_cvt_pk_bf16_f32 v18, v16, v17
s_nop 1
v_mov_b32_dpp v20, v18 quad_perm:[0,0,0,0] row_mask:0xf bank_mask:0xf
v_mov_b32_dpp v21, v18 quad_perm:[1,1,1,1] row_mask:0xf bank_mask:0xf
v_mov_b32_dpp v22, v18 quad_perm:[2,2,2,2] row_mask:0xf bank_mask:0xf
v_mov_b32_dpp v23, v18 quad_perm:[3,3,3,3] row_mask:0xf bank_mask:0xf
s_nop 1
v_mfma_f32_16x16x32_bf16 v[8:11], v[20:23], v[72:75], 0
v_mfma_f32_16x16x32_bf16 v[12:15], v[20:23], v[76:79], 0
v_mfma_f32_16x16x32_bf16 v[16:19], v[20:23], v[80:83], 0
v_mfma_f32_16x16x32_bf16 v[20:23], v[20:23], v[84:87], 0
s_mov_b32 exec_lo, 0x00f0000f
s_mov_b32 exec_hi, 0xf0000f00
s_nop 3
ds_write2_b32 v33, v8, v12 offset0:192 offset1:196
s_nop 0
ds_write2_b32 v33, v16, v20 offset0:200 offset1:204
s_mov_b32 exec_lo, -1
s_mov_b32 exec_hi, 0
s_waitcnt lgkmcnt(0)
s_barrier
ds_read_b128 v[8:11], v34 offset:0
ds_read_b128 v[12:15], v34 offset:1024
ds_read_b128 v[16:19], v34 offset:2048
ds_read_b128 v[20:23], v34 offset:3072
s_waitcnt lgkmcnt(2)
v_pk_add_f32 v[8:9], v[8:9], v[12:13]
v_pk_add_f32 v[10:11], v[10:11], v[14:15]
s_waitcnt lgkmcnt(0)
v_pk_add_f32 v[16:17], v[16:17], v[20:21]
v_pk_add_f32 v[18:19], v[18:19], v[22:23]
v_pk_add_f32 v[8:9], v[8:9], v[10:11]
v_pk_add_f32 v[16:17], v[16:17], v[18:19]
v_pk_add_f32 v[8:9], v[8:9], v[16:17]
v_add_f32 v8, v8, v9
v_min_u32 v8, -2, v8
buffer_store_dword v8, v35, %[rw], s70 offen sc1
s_mov_b32 exec_hi, -1
s_add_u32 s87, s71, 0
s_add_u32 s88, s71, 4096
s_movk_i32 s84, 32
s_mov_b64 vcc, -1
s_add_u32 %[g], %[g], 78
s_memrealtime s[92:93]
s_waitcnt lgkmcnt(0)
s_sub_u32 s92, s92, %[g]
s_cmp_lt_i32 s92, 12
s_cbranch_scc1 LmW5_%=a
s_sleep 6
s_branch LmR6_%=
LmW5_%=:
s_memrealtime s[92:93]
s_waitcnt lgkmcnt(0)
s_sub_u32 s92, s92, %[g]
LmW5_%=a:
s_cmp_lt_i32 s92, 0
s_cbranch_scc1 LmW5_%=
LmR6_%=:
s_mov_b64 exec, vcc
s_sub_u32 s84, s84, 1
s_cbranch_scc1 LmP8_%=
buffer_load_dwordx4 v[36:39], v32, %[rw], s87 offen offset:0 sc1
buffer_load_dwordx4 v[40:43], v32, %[rw], s87 offen offset:2048 sc1
buffer_load_dwordx4 v[44:47], v32, %[rw], s88 offen offset:0 sc1
buffer_load_dwordx4 v[48:51], v32, %[rw], s88 offen offset:2048 sc1
s_waitcnt vmcnt(2)
v_pk_add_f32 v[8:9], v[36:37], v[40:41]
v_pk_add_f32 v[10:11], v[38:39], v[42:43]
s_waitcnt vmcnt(0)
s_nop 0
v_pk_add_f32 v[12:13], v[44:45], v[48:49]
v_pk_add_f32 v[14:15], v[46:47], v[50:51]
v_pk_add_f32 v[8:9], v[8:9], v[12:13]
v_pk_add_f32 v[10:11], v[10:11], v[14:15]
v_pk_add_f32 v[12:13], v[8:9], v[10:11]
v_cmp_u_f32 vcc, v12, v13
s_cbranch_vccnz LmR6_%=
s_mov_b64 exec, -1
s_nop 0
LmD7_%=:
s_mov_b32 vcc_lo, 0x22222222
s_mov_b32 vcc_hi, 0x22222222
v_add_f32_dpp v16, v8, v8 row_ror:8 row_mask:0xf bank_mask:0x3
v_add_f32_dpp v17, v9, v9 row_ror:8 row_mask:0xf bank_mask:0x3
v_add_f32_dpp v16, v10, v10 row_ror:8 row_mask:0xf bank_mask:0xc
v_add_f32_dpp v17, v11, v11 row_ror:8 row_mask:0xf bank_mask:0xc
v_cvt_pk_bf16_f32 v18, v16, v17
v_lshlrev_b32_e64 v16, 16, v18
v_and_b32 v17, 0xffff0000, v18
v_pk_add_f32 v[16:17], v[16:17], v[0:1]
v_cvt_pk_bf16_f32 v5, v16, v17
s_nop 1
v_cndmask_b32_dpp v20, v5, v4, vcc quad_perm:[0,0,0,0] row_mask:0xf bank_mask:0xf
v_cndmask_b32_dpp v21, v5, v4, vcc quad_perm:[1,1,1,1] row_mask:0xf bank_mask:0xf
v_cndmask_b32_dpp v22, v5, v4, vcc quad_perm:[2,2,2,2] row_mask:0xf bank_mask:0xf
v_cndmask_b32_dpp v23, v5, v4, vcc quad_perm:[3,3,3,3] row_mask:0xf bank_mask:0xf
s_nop 1
v_mfma_f32_16x16x32_bf16 v[0:3], v[20:23], v[20:23], 0
v_mfma_f32_16x16x32_bf16 v[8:11], v[20:23], v[88:91], 0
v_mfma_f32_16x16x32_bf16 v[12:15], v[20:23], v[92:95], 0
v_mfma_f32_16x16x32_bf16 v[16:19], v[20:23], v[96:99], 0
v_mfma_f32_16x16x32_bf16 v[20:23], v[20:23], v[100:103], 0
s_cmp_eq_u32 s76, 0
s_mov_b32 exec_lo, 0x00f0000f
s_mov_b32 exec_hi, 0xf0000f00
s_nop 0
ds_write2_b32 v33, v0, v1 offset0:128 offset1:132
ds_write2_b32 v33, v8, v9 offset0:0 offset1:64
ds_write2_b32 v33, v12, v13 offset0:4 offset1:68
ds_write2_b32 v33, v16, v17 offset0:8 offset1:72
ds_write2_b32 v33, v20, v21 offset0:12 offset1:76
s_mov_b64 exec, -1
v_mbcnt_lo_u32_b32 v2, -1, 0
s_waitcnt lgkmcnt(0)
s_barrier
s_cbranch_scc1 LmS9_%=
ds_read_b128 v[8:11], v53 offset:0
ds_read_b128 v[12:15], v53 offset:1024
ds_read_b128 v[16:19], v53 offset:2048
ds_read_b128 v[20:23], v53 offset:3072
s_waitcnt lgkmcnt(2)
v_pk_add_f32 v[8:9], v[8:9], v[12:13]
v_pk_add_f32 v[10:11], v[10:11], v[14:15]
s_waitcnt lgkmcnt(0)
v_pk_add_f32 v[16:17], v[16:17], v[20:21]
v_pk_add_f32 v[18:19], v[18:19], v[22:23]
v_pk_add_f32 v[8:9], v[8:9], v[10:11]
v_pk_add_f32 v[16:17], v[16:17], v[18:19]
v_pk_add_f32 v[8:9], v[8:9], v[16:17]
v_add_f32_e64 v8, v8, v9
v_fma_f32 v10, v8, s89, v55
v_mov_b32_e64 v11, v8
v_readlane_b32 s87, v10, 33
v_readlane_b32 s88, v10, 32
v_permlane16_swap_b32 v8, v11
v_mul_f32 v12, s87, s87
v_sub_f32 v12, s88, v12
v_fma_f32 v8, -s87, v11, v8
v_max_f32 v12, 0x3727c5ac, v12
v_rsq_f32 v12, v12
s_nop 0
v_mul_f32_e64 v8, v8, v12
v_cvt_pk_bf16_f32 v8, v8, v8
v_lshlrev_b32_e64 v8, 16, v8
v_mul_f32 v9, 0xbfb8aa3b, v8
v_exp_f32 v9, v9
s_nop 0
v_add_f32 v9, 1.0, v9
v_rcp_f32 v9, v9
s_nop 0
v_cvt_pk_bf16_f32 v9, v9, v9
v_lshlrev_b32 v9, 16, v9
v_mul_f32_dpp v9, v8, v9 quad_perm:[2,3,2,3] row_mask:0xf bank_mask:0xf
s_nop 1
v_mov_b32_dpp v8, v9 quad_perm:[1,1,1,1] row_mask:0xf bank_mask:0xf
v_cvt_pk_bf16_f32 v9, v9, v8
v_min_u32 v9, -2, v9
s_mov_b64 exec, 0x1111
buffer_store_dword v9, v2, %[rw], s72 offen offset:0 sc1
buffer_store_dword v9, v2, %[rw], s72 offen offset:2048 sc1
s_mov_b64 exec, -1
s_branch LmJ10_%=
LmS9_%=:
s_sleep 4
LmJ10_%=:
s_mov_b32 s84, 65535
s_branch LmP14_%=
LmR12_%=r:
s_mov_b64 exec, vcc
s_sub_u32 s84, s84, 1
s_cbranch_scc1 LmD13_%=
buffer_load_dwordx4 v[36:39], v7, %[rw], s73 offen sc1
s_waitcnt vmcnt(0)
s_branch LmR12_%=w
LmP14_%=:
s_add_u32 %[g], %[g], 85
s_memrealtime s[92:93]
s_waitcnt lgkmcnt(0)
s_sub_u32 s92, s92, %[g]
s_cmp_lt_i32 s92, 12
s_cbranch_scc1 LmW11_%=a
s_sleep 6
s_branch LmR12_%=
LmW11_%=:
s_memrealtime s[92:93]
s_waitcnt lgkmcnt(0)
s_sub_u32 s92, s92, %[g]
LmW11_%=a:
s_cmp_lt_i32 s92, 0
s_cbranch_scc1 LmW11_%=
LmR12_%=:
buffer_load_dwordx4 v[36:39], v7, %[rw], s73 offen sc1
v_lshlrev_b32 v0, 16, v5
v_and_b32 v1, 0xffff0000, v5
s_add_u32 s88, s70, 262144
s_and_b32 s85, %[b], 7
s_lshr_b32 s86, %[b], 3
s_and_b32 s87, %[b], 1
s_lshr_b32 s92, s85, 1
s_cmp_eq_u32 %[w], s92
s_cselect_b32 s35, 1, 0
s_cmp_lt_u32 s86, 16
s_cselect_b32 s35, s35, 0
s_mul_i32 s90, s92, 43
s_mul_i32 s93, s87, 22
s_add_u32 s90, s90, s93
s_add_u32 s90, s90, s86
s_lshl_b32 s92, s90, 11
s_lshl_b32 s93, %[w], 9
s_add_u32 s92, s92, s93
s_add_u32 s32, s92, 1556480
s_lshl_b32 s92, s90, 13
s_lshl_b32 s93, %[w], 8
s_add_u32 s92, s92, s93
s_add_u32 s93, s86, s87
s_cmp_lt_u32 s93, 22
s_cselect_b32 s34, 1, 0
s_cselect_b32 s38, s92, 0x7ff00000
s_lshl_b32 s92, s85, 7
s_lshl_b32 s93, s86, 3
s_add_u32 s37, s92, s93
s_lshl_b32 s92, s37, 1
s_add_u32 s33, s92, 1556480
s_lshl_b32 s92, s87, 4
s_add_u32 s92, s92, s86
s_lshl_b32 s93, s92, 1
s_and_b32 s93, s93, 48
s_and_b32 s92, s92, 7
s_or_b32 s36, s92, s93
s_sub_u32 s39, s75, 16384
s_lshr_b32 s92, %[b], 5
s_lshl_b32 s92, s92, 2
s_add_u32 s92, s92, %[w]
s_lshl_b32 s40, s92, 4
s_add_u32 s41, %[ep], 2
s_lshl_b32 s92, %[w], 10
s_add_u32 s43, s92, 159744
s_lshl_b32 s92, %[b], 2
s_add_u32 s92, s92, %[w]
s_lshl_b32 s93, s92, 3
s_add_u32 s93, s93, 32768
s_cmp_eq_u32 s92, 0
s_cselect_b32 s42, 0, s93
s_lshl_b32 s93, s85, 4
s_add_u32 s93, s93, 2560
s_and_b32 s92, s92, 0x3e3
s_cmp_eq_u32 s92, 0x3e0
s_cselect_b32 s42, s93, s42
s_mov_b32 s8, 4096
s_mov_b32 s9, 8192
s_mov_b32 s10, 12288
s_mov_b32 s11, 16384
s_mov_b32 s12, 20480
s_mov_b32 s13, 24576
s_mov_b32 s14, 69632
s_mov_b32 s15, 73728
s_mov_b32 s16, 77824
s_mov_b32 s17, 81920
s_mov_b32 s18, 86016
s_mov_b32 s19, 90112
s_mov_b32 s20, 94208
s_mov_b32 s21, 98304
s_mov_b32 s22, 102400
s_mov_b32 s23, 106496
s_mov_b32 s24, 118784
s_mov_b32 s25, 122880
s_waitcnt vmcnt(0)
LmR12_%=w:
v_max3_u32 v2, v36, v37, v38
v_max_u32 v2, v2, v39
v_cmp_eq_u32 vcc, -1, v2
s_cbranch_vccnz LmR12_%=r
LmD13_%=:
s_mov_b64 exec, -1
v_mfma_f32_16x16x32_bf16 v[8:11], v[36:39], v[24:27], 0
v_mfma_f32_16x16x32_bf16 v[12:15], v[36:39], v[28:31], 0
s_mov_b32 exec_lo, 0x00f0000f
s_mov_b32 exec_hi, 0xf0000f00
s_nop 5
ds_write2_b32 v52, v8, v12 offset0:0 offset1:4
s_mov_b32 exec_lo, -1
s_mov_b32 exec_hi, 0
s_waitcnt lgkmcnt(0)
s_barrier
ds_read_b128 v[16:19], v34 offset:0
ds_read_b128 v[20:23], v34 offset:1024
s_waitcnt lgkmcnt(0)
v_pk_add_f32 v[16:17], v[16:17], v[20:21]
v_pk_add_f32 v[18:19], v[18:19], v[22:23]
v_pk_add_f32 v[16:17], v[16:17], v[18:19]
v_add_f32 v16, v16, v17
v_min_u32_e64 v16, -2, v16
buffer_store_dword v16, v35, %[rw], s88 offen sc1
s_mov_b32 exec_hi, -1
s_add_u32 s87, s71, 262144
s_add_u32 s88, s71, 266240
s_movk_i32 s84, 32
s_mov_b64 vcc, -1
s_add_u32 %[g], %[g], 60
s_memrealtime s[92:93]
s_waitcnt lgkmcnt(0)
s_sub_u32 s92, s92, %[g]
s_cmp_lt_i32 s92, 12
s_cbranch_scc1 LmW15_%=a
s_sleep 6
s_branch LmR16_%=
LmW15_%=:
s_memrealtime s[92:93]
s_waitcnt lgkmcnt(0)
s_sub_u32 s92, s92, %[g]
LmW15_%=a:
s_cmp_lt_i32 s92, 0
s_cbranch_scc1 LmW15_%=
LmR16_%=:
s_mov_b64 exec, vcc
s_sub_u32 s84, s84, 1
s_cbranch_scc1 LmP18_%=
buffer_load_dwordx4 v[36:39], v32, %[rw], s87 offen offset:0 sc1
buffer_load_dwordx4 v[40:43], v32, %[rw], s87 offen offset:2048 sc1
buffer_load_dwordx4 v[44:47], v32, %[rw], s88 offen offset:0 sc1
buffer_load_dwordx4 v[48:51], v32, %[rw], s88 offen offset:2048 sc1
s_waitcnt vmcnt(2)
v_pk_add_f32 v[8:9], v[36:37], v[40:41]
v_pk_add_f32 v[10:11], v[38:39], v[42:43]
s_waitcnt vmcnt(0)
s_nop 0
v_pk_add_f32 v[12:13], v[44:45], v[48:49]
v_pk_add_f32 v[14:15], v[46:47], v[50:51]
v_pk_add_f32 v[8:9], v[8:9], v[12:13]
v_pk_add_f32 v[10:11], v[10:11], v[14:15]
v_pk_add_f32 v[12:13], v[8:9], v[10:11]
v_cmp_u_f32 vcc, v12, v13
s_cbranch_vccnz LmR16_%=
s_mov_b64 exec, -1
s_nop 0
LmD17_%=:
s_mov_b32 vcc_lo, 0x22222222
s_mov_b32 vcc_hi, 0x22222222
v_add_f32_dpp v16, v8, v8 row_ror:8 row_mask:0xf bank_mask:0x3
v_add_f32_dpp v17, v9, v9 row_ror:8 row_mask:0xf bank_mask:0x3
v_add_f32_dpp v16, v10, v10 row_ror:8 row_mask:0xf bank_mask:0xc
v_add_f32_dpp v17, v11, v11 row_ror:8 row_mask:0xf bank_mask:0xc
v_cvt_pk_bf16_f32 v18, v16, v17
v_lshlrev_b32_e64 v16, 16, v18
v_and_b32 v17, 0xffff0000, v18
v_pk_add_f32 v[16:17], v[16:17], v[0:1]
v_cvt_pk_bf16_f32 v5, v16, v17
s_nop 1
v_cndmask_b32_dpp v20, v5, v4, vcc quad_perm:[0,0,0,0] row_mask:0xf bank_mask:0xf
v_cndmask_b32_dpp v21, v5, v4, vcc quad_perm:[1,1,1,1] row_mask:0xf bank_mask:0xf
v_cndmask_b32_dpp v22, v5, v4, vcc quad_perm:[2,2,2,2] row_mask:0xf bank_mask:0xf
v_cndmask_b32_dpp v23, v5, v4, vcc quad_perm:[3,3,3,3] row_mask:0xf bank_mask:0xf
s_nop 1
v_mfma_f32_16x16x32_bf16 v[0:3], v[20:23], v[20:23], 0
v_mfma_f32_16x16x32_bf16 v[8:11], v[20:23], v[56:59], 0
v_mfma_f32_16x16x32_bf16 v[12:15], v[20:23], v[60:63], 0
v_mfma_f32_16x16x32_bf16 v[16:19], v[20:23], v[64:67], 0
)" R"(
v_mfma_f32_16x16x32_bf16 v[20:23], v[20:23], v[68:71], 0
s_cmp_eq_u32 s77, 0
s_mov_b32 exec_lo, 0x00f0000f
s_mov_b32 exec_hi, 0xf0000f00
s_nop 0
ds_write2_b32 v33, v0, v1 offset0:128 offset1:132
ds_write2_b32 v33, v8, v9 offset0:0 offset1:64
ds_write2_b32 v33, v12, v13 offset0:4 offset1:68
ds_write2_b32 v33, v16, v17 offset0:8 offset1:72
ds_write2_b32 v33, v20, v21 offset0:12 offset1:76
s_mov_b64 exec, -1
v_mbcnt_lo_u32_b32 v2, -1, 0
v_lshlrev_b32 v2, 1, v2
s_waitcnt lgkmcnt(0)
s_barrier
s_cbranch_scc1 LmS19_%=
ds_read_b128 v[8:11], v53 offset:0
ds_read_b128 v[12:15], v53 offset:1024
ds_read_b128 v[16:19], v53 offset:2048
ds_read_b128 v[20:23], v53 offset:3072
s_waitcnt lgkmcnt(2)
v_pk_add_f32 v[8:9], v[8:9], v[12:13]
v_pk_add_f32 v[10:11], v[10:11], v[14:15]
s_waitcnt lgkmcnt(0)
s_nop 0
v_pk_add_f32 v[16:17], v[16:17], v[20:21]
v_pk_add_f32 v[18:19], v[18:19], v[22:23]
v_pk_add_f32 v[8:9], v[8:9], v[10:11]
v_pk_add_f32 v[16:17], v[16:17], v[18:19]
v_pk_add_f32 v[8:9], v[8:9], v[16:17]
v_add_f32_e64 v8, v8, v9
v_fma_f32 v10, v8, s89, v55
v_mov_b32_e64 v11, v8
v_readlane_b32 s87, v10, 33
v_readlane_b32 s88, v10, 32
v_permlane16_swap_b32 v8, v11
v_mul_f32 v12, s87, s87
v_sub_f32 v12, s88, v12
v_fma_f32 v8, -s87, v11, v8
v_max_f32 v12, 0x3727c5ac, v12
v_rsq_f32 v12, v12
s_nop 0
v_mul_f32 v8, v8, v12
s_nop 1
v_mov_b32_dpp v9, v8 quad_perm:[1,1,3,3] row_mask:0xf bank_mask:0xf
v_cvt_pk_bf16_f32 v8, v8, v9
v_min_u32_e64 v8, -2, v8
s_mov_b64 exec, 0x5555
buffer_store_dword v8, v2, %[rw], s74 offen sc1
s_mov_b64 exec, -1
s_branch LmJ20_%=
LmS19_%=:
s_sleep 4
LmJ20_%=:
s_mov_b32 s84, 65535
s_branch LmP24_%=
LmR22_%=r:
s_mov_b64 exec, vcc
s_sub_u32 s84, s84, 1
s_cbranch_scc1 LmD23_%=
buffer_load_dwordx4 v[0:3], v13, %[rw], s75 offen sc1
s_waitcnt vmcnt(0)
s_branch LmR22_%=w
LmP24_%=:
s_mov_b32 s85, 0
s_add_u32 %[g], %[g], 82
s_memrealtime s[92:93]
s_waitcnt lgkmcnt(0)
s_sub_u32 s92, s92, %[g]
s_cmp_lt_i32 s92, 12
s_cbranch_scc1 LmW21_%=a
s_sleep 4
s_branch LmR22_%=
LmW21_%=:
s_memrealtime s[92:93]
s_waitcnt lgkmcnt(0)
s_sub_u32 s92, s92, %[g]
LmW21_%=a:
s_cmp_lt_i32 s92, 0
s_cbranch_scc1 LmW21_%=
LmR22_%=:
buffer_load_dwordx4 v[0:3], v54, %[rw], s75 offen sc1
v_mov_b32_e64 v13, v54
v_mbcnt_lo_u32_b32 v14, -1, 0
v_mbcnt_hi_u32_b32 v14, -1, v14
v_lshlrev_b32_e64 v7, 4, v14
s_sub_u32 s85, s79, 38912
v_add_u32_e64 v53, s85, v7
v_lshl_add_u32 v15, v14, 2, s79
ds_read_b128 v[36:39], v53 offset:38912
ds_read_b32 v8, v15 offset:768
v_mov_b32 v56, 0
v_mov_b32 v57, 0
v_mov_b32 v58, 0
v_mov_b32 v59, 0
v_mov_b32 v60, 0
v_mov_b32 v61, 0
v_mov_b32 v62, 0
v_mov_b32 v63, 0
v_mov_b32 v64, 0
v_mov_b32 v65, 0
v_mov_b32 v66, 0
v_mov_b32 v67, 0
v_mov_b32 v68, 0
v_mov_b32 v69, 0
v_mov_b32 v70, 0
v_mov_b32 v71, 0
s_mov_b32 s96, 0x05010400
s_mov_b32 s97, 0x07030602
s_mov_b32 s98, 0x87878787
s_mov_b32 s88, 0xaaaaaaaa
s_mov_b32 s89, 0xaaaaaaaa
s_mov_b32 s94, 0xcccccccc
s_mov_b32 s95, 0xcccccccc
s_add_u32 s68, s68, 1048576
s_add_u32 s69, s69, 1048576
s_add_u32 s70, s70, 1048576
s_add_u32 s71, s71, 1048576
s_and_b32 s93, s80, 0xff
s_mul_i32 s92, s93, 0x01000100
s_sub_u32 s92, s92, 0x07000700
v_mov_b32_e64 v4, s92
s_mul_i32 s93, s93, 0x02000200
s_sub_u32 s74, 0xd000cff, s93
s_lshl_b32 s92, s82, 24
s_add_u32 s92, s92, 0x38800000
v_mov_b32 v54, s92
v_and_b32_e64 v9, 16, v7
s_mov_b32 s99, 0xffff
v_lshrrev_b32_e64 v10, 1, v7
v_and_b32 v10, 0x1e0, v10
v_add3_u32 v52, v9, v10, s43
v_add_u32_e64 v12, s43, v7
v_cmp_gt_u32 s[92:93], s78, v14
s_cmp_gt_u32 s78, 48
s_cselect_b64 s[86:87], -1, 0
s_waitcnt lgkmcnt(0)
s_nop 0
ds_write_b128 v53, v[56:59] offset:38912
v_perm_b32 v16, v36, v38, s96
v_perm_b32 v17, v36, v38, s97
v_perm_b32 v18, v37, v39, s96
v_perm_b32 v19, v37, v39, s97
s_andn2_b64 s[92:93], s[92:93], s[86:87]
v_lshrrev_b32 v9, 2, v8
v_lshrrev_b32_e64 v10, 8, v8
v_and_b32 v11, 0x400, v9
v_and_b32 v9, 0xf0, v9
v_and_or_b32 v9, v10, 12, v9
v_and_b32_e64 v10, 48, v8
v_lshl_add_u32 v10, v10, 4, v9
v_add3_u32 v20, v10, v11, s85
v_cmp_ne_u32 vcc, 0, v11
v_mov_b32 v10, s43
v_mov_b32 v11, s79
v_cndmask_b32 v11, v10, v11, vcc
v_and_b32_e64 v10, 12, v8
v_lshl_add_u32 v9, v10, 6, v9
v_add_u32 v9, v9, v11
v_and_b32 v11, 3, v8
v_lshl_add_u32 v11, v11, 2, s43
v_lshl_add_u32 v21, v10, 5, v11
v_cmp_gt_u32 vcc, 8192, v8
v_and_b32 v10, 63, v8
v_lshlrev_b32 v10, 2, v10
v_cndmask_b32 v20, v20, v10, vcc
v_cndmask_b32 v21, v21, v9, vcc
s_and_b64 s[72:73], s[92:93], vcc
s_andn2_b64 s[76:77], s[92:93], vcc
v_accvgpr_read_b32 v22, a254
v_accvgpr_read_b32 v25, a255
v_accvgpr_read_b32 v27, a0
v_alignbit_b32 v23, v22, v22, 4
v_and_b32 v22, s98, v22
v_and_b32 v23, s98, v23
v_perm_b32 v24, v22, v25, s96
v_perm_b32 v25, v22, v25, s97
v_perm_b32 v26, v23, v27, s96
v_perm_b32 v27, v23, v27, s97
v_accvgpr_read_b32 v22, a1
v_accvgpr_read_b32 v29, a2
v_accvgpr_read_b32 v31, a3
v_alignbit_b32 v23, v22, v22, 4
v_and_b32 v22, s98, v22
v_and_b32 v23, s98, v23
v_perm_b32 v28, v22, v29, s96
v_perm_b32 v29, v22, v29, s97
v_perm_b32 v30, v23, v31, s96
v_perm_b32 v31, v23, v31, s97
s_waitcnt vmcnt(0)
LmR22_%=w:
v_max3_u32 v22, v0, v1, v2
v_max_u32 v22, v22, v3
v_cmp_eq_u32 vcc, -1, v22
s_cbranch_vccnz LmR22_%=r
LmD23_%=:
s_mov_b64 exec, -1
s_nop 0
s_memrealtime s[92:93]
ds_write_b128 v12, v[68:71]
v_pk_add_u16 v36, v0, v0
v_pk_add_u16 v37, v1, v1
v_pk_add_u16 v38, v2, v2
v_pk_add_u16 v39, v3, v3
v_pk_max_u16 v36, v36, v37
v_pk_max_u16 v38, v38, v39
v_pk_max_u16 v36, v36, v38
v_pk_sub_u16 v36, v36, s74 clamp
v_cmp_ne_u32 vcc, 0, v36
s_or_b64 s[86:87], s[86:87], vcc
s_cbranch_scc1 Lslowk
s_nop 0
Lslowkr:
v_add_u32_dpp v56, v0, v4 quad_perm:[0,1,2,3] row_mask:0xf bank_mask:0x1
v_add_u32_dpp v57, v1, v4 quad_perm:[0,1,2,3] row_mask:0xf bank_mask:0x1
v_add_u32_dpp v58, v2, v4 quad_perm:[0,1,2,3] row_mask:0xf bank_mask:0x1
v_add_u32_dpp v59, v3, v4 quad_perm:[0,1,2,3] row_mask:0xf bank_mask:0x1
v_add_u32_dpp v60, v0, v4 quad_perm:[0,1,2,3] row_mask:0xf bank_mask:0x2
v_add_u32_dpp v61, v1, v4 quad_perm:[0,1,2,3] row_mask:0xf bank_mask:0x2
v_add_u32_dpp v62, v2, v4 quad_perm:[0,1,2,3] row_mask:0xf bank_mask:0x2
v_add_u32_dpp v63, v3, v4 quad_perm:[0,1,2,3] row_mask:0xf bank_mask:0x2
v_mfma_f32_16x16x32_bf16 v[36:39], v[24:27], v[56:59], 0
s_waitcnt lgkmcnt(0)
s_mov_b32 s90, s92
ds_bpermute_b32 v8, v20, v0
buffer_load_dwordx4 a[0:3], v7, %[rv], s17 offen offset:3072 nt
v_accvgpr_read_b32 v22, a4
v_accvgpr_read_b32 v25, a5
v_accvgpr_read_b32 v27, a6
v_alignbit_b32 v23, v22, v22, 4
v_and_b32 v22, s98, v22
v_and_b32 v23, s98, v23
v_perm_b32 v24, v22, v25, s96
v_perm_b32 v25, v22, v25, s97
v_perm_b32 v26, v23, v27, s96
v_perm_b32 v27, v23, v27, s97
v_mfma_f32_16x16x32_bf16 v[40:43], v[28:31], v[56:59], 0
ds_bpermute_b32 v9, v20, v1
ds_bpermute_b32 v10, v20, v2
v_accvgpr_read_b32 v22, a7
v_accvgpr_read_b32 v29, a8
v_accvgpr_read_b32 v31, a9
v_alignbit_b32 v23, v22, v22, 4
v_and_b32 v22, s98, v22
v_and_b32 v23, s98, v23
v_perm_b32 v28, v22, v29, s96
v_perm_b32 v29, v22, v29, s97
v_perm_b32 v30, v23, v31, s96
v_perm_b32 v31, v23, v31, s97
v_mfma_f32_16x16x32_bf16 v[44:47], v[24:27], v[56:59], 0
ds_bpermute_b32 v11, v20, v3
buffer_load_dwordx4 a[4:7], v7, %[rv], s17 offen offset:2048 nt
v_accvgpr_read_b32 v22, a10
v_accvgpr_read_b32 v25, a11
v_accvgpr_read_b32 v27, a12
v_alignbit_b32 v23, v22, v22, 4
v_and_b32 v22, s98, v22
v_and_b32 v23, s98, v23
v_perm_b32 v24, v22, v25, s96
v_perm_b32 v25, v22, v25, s97
v_perm_b32 v26, v23, v27, s96
v_perm_b32 v27, v23, v27, s97
v_mfma_f32_16x16x32_bf16 v[48:51], v[28:31], v[56:59], 0
buffer_load_dwordx4 a[8:11], v7, %[rv], s17 offen offset:1024 nt
v_add_u32_dpp v64, v0, v4 quad_perm:[0,1,2,3] row_mask:0xf bank_mask:0x4
v_add_u32_dpp v65, v1, v4 quad_perm:[0,1,2,3] row_mask:0xf bank_mask:0x4
v_add_u32_dpp v66, v2, v4 quad_perm:[0,1,2,3] row_mask:0xf bank_mask:0x4
v_add_u32_dpp v67, v3, v4 quad_perm:[0,1,2,3] row_mask:0xf bank_mask:0x4
v_add_u32_dpp v68, v0, v4 quad_perm:[0,1,2,3] row_mask:0xf bank_mask:0x8
v_add_u32_dpp v69, v1, v4 quad_perm:[0,1,2,3] row_mask:0xf bank_mask:0x8
v_add_u32_dpp v70, v2, v4 quad_perm:[0,1,2,3] row_mask:0xf bank_mask:0x8
v_add_u32_dpp v71, v3, v4 quad_perm:[0,1,2,3] row_mask:0xf bank_mask:0x8
v_accvgpr_read_b32 v22, a13
v_accvgpr_read_b32 v29, a14
v_accvgpr_read_b32 v31, a15
v_alignbit_b32 v23, v22, v22, 4
v_and_b32 v22, s98, v22
v_and_b32 v23, s98, v23
v_perm_b32 v28, v22, v29, s96
v_perm_b32 v29, v22, v29, s97
v_perm_b32 v30, v23, v31, s96
v_perm_b32 v31, v23, v31, s97
v_mfma_f32_16x16x32_bf16 v[36:39], v[24:27], v[60:63], v[36:39]
buffer_load_dwordx4 a[12:15], v7, %[rv], s17 offen offset:0 nt
v_accvgpr_read_b32 v22, a16
v_accvgpr_read_b32 v25, a17
v_accvgpr_read_b32 v27, a18
v_alignbit_b32 v23, v22, v22, 4
v_and_b32 v22, s98, v22
v_and_b32 v23, s98, v23
v_perm_b32 v24, v22, v25, s96
v_perm_b32 v25, v22, v25, s97
v_perm_b32 v26, v23, v27, s96
v_perm_b32 v27, v23, v27, s97
v_mfma_f32_16x16x32_bf16 v[40:43], v[28:31], v[60:63], v[40:43]
s_waitcnt lgkmcnt(0)
s_nop 0
v_dot2_f32_bf16 v13, v16, v8, 0
v_accvgpr_read_b32 v22, a19
v_accvgpr_read_b32 v29, a20
v_accvgpr_read_b32 v31, a21
v_alignbit_b32 v23, v22, v22, 4
v_and_b32 v22, s98, v22
v_and_b32 v23, s98, v23
v_perm_b32 v28, v22, v29, s96
v_perm_b32 v29, v22, v29, s97
v_perm_b32 v30, v23, v31, s96
v_perm_b32 v31, v23, v31, s97
v_mfma_f32_16x16x32_bf16 v[44:47], v[24:27], v[60:63], v[44:47]
v_dot2_f32_bf16 v14, v18, v10, 0
v_dot2_f32_bf16 v13, v17, v9, v13
buffer_load_dwordx4 a[16:19], v7, %[rv], s25 offen offset:3072 nt
v_accvgpr_read_b32 v22, a22
v_accvgpr_read_b32 v25, a23
v_accvgpr_read_b32 v27, a24
v_alignbit_b32 v23, v22, v22, 4
v_and_b32 v22, s98, v22
v_and_b32 v23, s98, v23
v_perm_b32 v24, v22, v25, s96
v_perm_b32 v25, v22, v25, s97
v_perm_b32 v26, v23, v27, s96
v_perm_b32 v27, v23, v27, s97
v_mfma_f32_16x16x32_bf16 v[48:51], v[28:31], v[60:63], v[48:51]
v_dot2_f32_bf16 v14, v19, v11, v14
v_accvgpr_read_b32 v22, a25
v_accvgpr_read_b32 v29, a26
v_accvgpr_read_b32 v31, a27
v_alignbit_b32 v23, v22, v22, 4
v_and_b32 v22, s98, v22
v_and_b32 v23, s98, v23
v_perm_b32 v28, v22, v29, s96
v_perm_b32 v29, v22, v29, s97
v_perm_b32 v30, v23, v31, s96
v_perm_b32 v31, v23, v31, s97
v_mfma_f32_16x16x32_bf16 v[36:39], v[24:27], v[64:67], v[36:39]
v_add_f32 v13, v13, v14
s_mov_b64 exec, s[72:73]
ds_add_f32 v21, v13
s_mov_b64 exec, -1
s_nop 0
buffer_load_dwordx4 a[20:23], v7, %[rv], s25 offen offset:2048 nt
v_accvgpr_read_b32 v22, a28
v_accvgpr_read_b32 v25, a29
v_accvgpr_read_b32 v27, a30
v_alignbit_b32 v23, v22, v22, 4
v_and_b32 v22, s98, v22
v_and_b32 v23, s98, v23
v_perm_b32 v24, v22, v25, s96
v_perm_b32 v25, v22, v25, s97
v_perm_b32 v26, v23, v27, s96
v_perm_b32 v27, v23, v27, s97
v_mfma_f32_16x16x32_bf16 v[40:43], v[28:31], v[64:67], v[40:43]
ds_read_b128 v[8:11], v12
ds_read_b128 v[12:15], v53 offset:38912
buffer_load_dwordx4 a[24:27], v7, %[rv], s16 offen offset:3072 nt
v_accvgpr_read_b32 v22, a31
v_accvgpr_read_b32 v29, a32
v_accvgpr_read_b32 v31, a33
v_alignbit_b32 v23, v22, v22, 4
v_and_b32 v22, s98, v22
v_and_b32 v23, s98, v23
v_perm_b32 v28, v22, v29, s96
v_perm_b32 v29, v22, v29, s97
v_perm_b32 v30, v23, v31, s96
v_perm_b32 v31, v23, v31, s97
v_mfma_f32_16x16x32_bf16 v[44:47], v[24:27], v[64:67], v[44:47]
v_accvgpr_read_b32 v22, a34
v_accvgpr_read_b32 v25, a35
v_accvgpr_read_b32 v27, a36
v_alignbit_b32 v23, v22, v22, 4
v_and_b32 v22, s98, v22
v_and_b32 v23, s98, v23
v_perm_b32 v24, v22, v25, s96
v_perm_b32 v25, v22, v25, s97
v_perm_b32 v26, v23, v27, s96
v_perm_b32 v27, v23, v27, s97
v_mfma_f32_16x16x32_bf16 v[48:51], v[28:31], v[64:67], v[48:51]
v_accvgpr_read_b32 v22, a37
v_accvgpr_read_b32 v29, a38
v_accvgpr_read_b32 v31, a39
v_alignbit_b32 v23, v22, v22, 4
v_and_b32 v22, s98, v22
v_and_b32 v23, s98, v23
v_perm_b32 v28, v22, v29, s96
v_perm_b32 v29, v22, v29, s97
v_perm_b32 v30, v23, v31, s96
v_perm_b32 v31, v23, v31, s97
v_mfma_f32_16x16x32_bf16 v[36:39], v[24:27], v[68:71], v[36:39]
v_accvgpr_read_b32 v22, a40
v_accvgpr_read_b32 v25, a41
v_accvgpr_read_b32 v27, a42
v_alignbit_b32 v23, v22, v22, 4
v_and_b32 v22, s98, v22
v_and_b32 v23, s98, v23
v_perm_b32 v24, v22, v25, s96
)" R"(
v_perm_b32 v25, v22, v25, s97
v_perm_b32 v26, v23, v27, s96
v_perm_b32 v27, v23, v27, s97
v_mfma_f32_16x16x32_bf16 v[40:43], v[28:31], v[68:71], v[40:43]
buffer_load_dwordx4 a[28:31], v7, %[rv], s16 offen offset:2048 nt
v_accvgpr_read_b32 v22, a43
v_accvgpr_read_b32 v29, a44
v_accvgpr_read_b32 v31, a45
v_alignbit_b32 v23, v22, v22, 4
v_and_b32 v22, s98, v22
v_and_b32 v23, s98, v23
v_perm_b32 v28, v22, v29, s96
v_perm_b32 v29, v22, v29, s97
v_perm_b32 v30, v23, v31, s96
v_perm_b32 v31, v23, v31, s97
v_mfma_f32_16x16x32_bf16 v[44:47], v[24:27], v[68:71], v[44:47]
v_accvgpr_read_b32 v22, a46
v_accvgpr_read_b32 v25, a47
v_accvgpr_read_b32 v27, a48
v_alignbit_b32 v23, v22, v22, 4
v_and_b32 v22, s98, v22
v_and_b32 v23, s98, v23
v_perm_b32 v24, v22, v25, s96
v_perm_b32 v25, v22, v25, s97
v_perm_b32 v26, v23, v27, s96
v_perm_b32 v27, v23, v27, s97
v_mfma_f32_16x16x32_bf16 v[48:51], v[28:31], v[68:71], v[48:51]
v_accvgpr_read_b32 v22, a49
v_accvgpr_read_b32 v29, a50
v_accvgpr_read_b32 v31, a51
v_alignbit_b32 v23, v22, v22, 4
v_and_b32 v22, s98, v22
v_and_b32 v23, s98, v23
v_perm_b32 v28, v22, v29, s96
v_perm_b32 v29, v22, v29, s97
v_perm_b32 v30, v23, v31, s96
v_perm_b32 v31, v23, v31, s97
s_waitcnt lgkmcnt(0)
s_nop 0
v_cndmask_b32 v36, v36, v38, s[94:95]
v_cndmask_b32 v37, v37, v39, s[94:95]
v_cndmask_b32 v40, v40, v42, s[94:95]
v_cndmask_b32 v41, v41, v43, s[94:95]
v_cndmask_b32 v44, v44, v46, s[94:95]
v_cndmask_b32 v45, v45, v47, s[94:95]
v_cndmask_b32 v48, v48, v50, s[94:95]
v_cndmask_b32 v49, v49, v51, s[94:95]
v_cndmask_b32 v38, v40, v37, s[88:89]
v_cndmask_b32 v39, v36, v41, s[88:89]
v_cndmask_b32 v46, v48, v45, s[88:89]
v_cndmask_b32 v47, v44, v49, s[88:89]
v_add_f32_dpp v39, v38, v39 quad_perm:[1,0,3,2] row_mask:0xf bank_mask:0xf
v_add_f32_dpp v47, v46, v47 quad_perm:[1,0,3,2] row_mask:0xf bank_mask:0xf
v_cndmask_b32 v40, v47, v39, s[94:95]
v_cndmask_b32 v41, v39, v47, s[94:95]
v_add_f32_e64 v41, v41, v12
v_add_f32_dpp v12, v40, v41 quad_perm:[2,3,0,1] row_mask:0xf bank_mask:0xf
v_mfma_f32_16x16x32_bf16 v[36:39], v[24:27], v[56:59], 0
v_cvt_pk_bf16_f32 v12, 0, v12
v_mul_f32 v12, 0x3db504f3, v12
buffer_load_dwordx4 a[32:35], v7, %[rv], s24 offen offset:3072 nt
v_accvgpr_read_b32 v22, a56
v_accvgpr_read_b32 v25, a57
v_accvgpr_read_b32 v27, a58
v_alignbit_b32 v23, v22, v22, 4
v_and_b32 v22, s98, v22
v_and_b32 v23, s98, v23
v_perm_b32 v24, v22, v25, s96
v_perm_b32 v25, v22, v25, s97
v_perm_b32 v26, v23, v27, s96
v_perm_b32 v27, v23, v27, s97
v_mfma_f32_16x16x32_bf16 v[40:43], v[28:31], v[56:59], 0
v_cvt_pk_bf16_f32 v12, 0, v12
v_mul_f32 v12, 0x3fb8aa3b, v12
buffer_load_dwordx4 a[36:39], v7, %[rv], s24 offen offset:2048 nt
v_accvgpr_read_b32 v22, a59
v_accvgpr_read_b32 v29, a60
v_accvgpr_read_b32 v31, a61
v_alignbit_b32 v23, v22, v22, 4
v_and_b32 v22, s98, v22
v_and_b32 v23, s98, v23
v_perm_b32 v28, v22, v29, s96
v_perm_b32 v29, v22, v29, s97
v_perm_b32 v30, v23, v31, s96
v_perm_b32 v31, v23, v31, s97
v_mfma_f32_16x16x32_bf16 v[44:47], v[24:27], v[56:59], 0
v_accvgpr_read_b32 v22, a62
v_accvgpr_read_b32 v25, a63
v_accvgpr_read_b32 v27, a68
v_alignbit_b32 v23, v22, v22, 4
v_and_b32 v22, s98, v22
v_and_b32 v23, s98, v23
v_perm_b32 v24, v22, v25, s96
v_perm_b32 v25, v22, v25, s97
v_perm_b32 v26, v23, v27, s96
v_perm_b32 v27, v23, v27, s97
v_mfma_f32_16x16x32_bf16 v[48:51], v[28:31], v[56:59], 0
buffer_load_dwordx4 a[40:43], v7, %[rv], s16 offen offset:1024 nt
v_accvgpr_read_b32 v22, a69
v_accvgpr_read_b32 v29, a70
v_accvgpr_read_b32 v31, a71
v_alignbit_b32 v23, v22, v22, 4
v_and_b32 v22, s98, v22
v_and_b32 v23, s98, v23
v_perm_b32 v28, v22, v29, s96
v_perm_b32 v29, v22, v29, s97
v_perm_b32 v30, v23, v31, s96
v_perm_b32 v31, v23, v31, s97
v_mfma_f32_16x16x32_bf16 v[36:39], v[24:27], v[60:63], v[36:39]
v_alignbit_b32 v23, v252, v252, 4
v_and_b32 v22, s98, v252
v_and_b32 v23, s98, v23
v_perm_b32 v24, v22, v253, s96
v_perm_b32 v25, v22, v253, s97
v_perm_b32 v26, v23, v254, s96
v_perm_b32 v27, v23, v254, s97
v_mfma_f32_16x16x32_bf16 v[40:43], v[28:31], v[60:63], v[40:43]
v_alignbit_b32 v23, v255, v255, 4
v_and_b32 v22, s98, v255
v_and_b32 v23, s98, v23
v_perm_b32 v28, v22, v248, s96
v_perm_b32 v29, v22, v248, s97
v_perm_b32 v30, v23, v249, s96
v_perm_b32 v31, v23, v249, s97
v_mfma_f32_16x16x32_bf16 v[44:47], v[24:27], v[60:63], v[44:47]
ds_read_b128 v[252:255], v53 offset:36864
v_alignbit_b32 v23, v250, v250, 4
v_and_b32 v22, s98, v250
v_and_b32 v23, s98, v23
v_perm_b32 v24, v22, v251, s96
v_perm_b32 v25, v22, v251, s97
v_perm_b32 v26, v23, v116, s96
v_perm_b32 v27, v23, v116, s97
v_mfma_f32_16x16x32_bf16 v[48:51], v[28:31], v[60:63], v[48:51]
buffer_load_dwordx4 a[44:47], v7, %[rv], s16 offen offset:0 nt
ds_read_b128 v[248:251], v53 offset:37888
v_alignbit_b32 v23, v117, v117, 4
v_and_b32 v22, s98, v117
v_and_b32 v23, s98, v23
v_perm_b32 v28, v22, v118, s96
v_perm_b32 v29, v22, v118, s97
v_perm_b32 v30, v23, v119, s96
v_perm_b32 v31, v23, v119, s97
v_mfma_f32_16x16x32_bf16 v[36:39], v[24:27], v[64:67], v[36:39]
ds_read_b128 v[116:119], v53 offset:0
v_alignbit_b32 v23, v120, v120, 4
v_and_b32 v22, s98, v120
v_and_b32 v23, s98, v23
v_perm_b32 v24, v22, v121, s96
v_perm_b32 v25, v22, v121, s97
v_perm_b32 v26, v23, v122, s96
v_perm_b32 v27, v23, v122, s97
v_mfma_f32_16x16x32_bf16 v[40:43], v[28:31], v[64:67], v[40:43]
v_alignbit_b32 v23, v123, v123, 4
v_and_b32 v22, s98, v123
v_and_b32 v23, s98, v23
v_perm_b32 v28, v22, v124, s96
v_perm_b32 v29, v22, v124, s97
v_perm_b32 v30, v23, v125, s96
v_perm_b32 v31, v23, v125, s97
v_mfma_f32_16x16x32_bf16 v[44:47], v[24:27], v[64:67], v[44:47]
ds_read_b128 v[120:123], v53 offset:1024
v_alignbit_b32 v23, v126, v126, 4
v_and_b32 v22, s98, v126
v_and_b32 v23, s98, v23
v_perm_b32 v24, v22, v127, s96
v_perm_b32 v25, v22, v127, s97
v_perm_b32 v26, v23, v128, s96
v_perm_b32 v27, v23, v128, s97
v_mfma_f32_16x16x32_bf16 v[48:51], v[28:31], v[64:67], v[48:51]
buffer_load_dwordx4 a[48:51], v7, %[rv], s23 offen offset:3072 nt
v_alignbit_b32 v23, v129, v129, 4
v_and_b32 v22, s98, v129
v_and_b32 v23, s98, v23
v_perm_b32 v28, v22, v130, s96
v_perm_b32 v29, v22, v130, s97
v_perm_b32 v30, v23, v131, s96
v_perm_b32 v31, v23, v131, s97
v_mfma_f32_16x16x32_bf16 v[36:39], v[24:27], v[68:71], v[36:39]
v_accvgpr_read_b32 v22, a112
v_accvgpr_read_b32 v25, a113
v_accvgpr_read_b32 v27, a114
v_alignbit_b32 v23, v22, v22, 4
v_and_b32 v22, s98, v22
v_and_b32 v23, s98, v23
v_perm_b32 v24, v22, v25, s96
v_perm_b32 v25, v22, v25, s97
v_perm_b32 v26, v23, v27, s96
v_perm_b32 v27, v23, v27, s97
v_mfma_f32_16x16x32_bf16 v[40:43], v[28:31], v[68:71], v[40:43]
v_accvgpr_read_b32 v22, a115
v_accvgpr_read_b32 v29, a120
v_accvgpr_read_b32 v31, a121
v_alignbit_b32 v23, v22, v22, 4
v_and_b32 v22, s98, v22
v_and_b32 v23, s98, v23
v_perm_b32 v28, v22, v29, s96
v_perm_b32 v29, v22, v29, s97
v_perm_b32 v30, v23, v31, s96
v_perm_b32 v31, v23, v31, s97
v_mfma_f32_16x16x32_bf16 v[44:47], v[24:27], v[68:71], v[44:47]
buffer_load_dwordx4 a[56:59], v7, %[rv], s23 offen offset:2048 nt
v_accvgpr_read_b32 v22, a122
v_accvgpr_read_b32 v25, a123
v_accvgpr_read_b32 v27, a128
v_alignbit_b32 v23, v22, v22, 4
v_and_b32 v22, s98, v22
v_and_b32 v23, s98, v23
v_perm_b32 v24, v22, v25, s96
v_perm_b32 v25, v22, v25, s97
v_perm_b32 v26, v23, v27, s96
v_perm_b32 v27, v23, v27, s97
v_mfma_f32_16x16x32_bf16 v[48:51], v[28:31], v[68:71], v[48:51]
buffer_load_dwordx4 a[60:63], v7, %[rv], s15 offen offset:3072 nt
v_accvgpr_read_b32 v22, a129
v_accvgpr_read_b32 v29, a130
v_accvgpr_read_b32 v31, a131
v_alignbit_b32 v23, v22, v22, 4
v_and_b32 v22, s98, v22
v_and_b32 v23, s98, v23
v_perm_b32 v28, v22, v29, s96
v_perm_b32 v29, v22, v29, s97
v_perm_b32 v30, v23, v31, s96
v_perm_b32 v31, v23, v31, s97
v_cndmask_b32 v36, v36, v38, s[94:95]
v_cndmask_b32 v37, v37, v39, s[94:95]
v_cndmask_b32 v40, v40, v42, s[94:95]
v_cndmask_b32 v41, v41, v43, s[94:95]
v_cndmask_b32 v44, v44, v46, s[94:95]
v_cndmask_b32 v45, v45, v47, s[94:95]
v_cndmask_b32 v48, v48, v50, s[94:95]
v_cndmask_b32 v49, v49, v51, s[94:95]
v_cndmask_b32 v38, v40, v37, s[88:89]
v_cndmask_b32 v39, v36, v41, s[88:89]
v_cndmask_b32 v46, v48, v45, s[88:89]
v_cndmask_b32 v47, v44, v49, s[88:89]
v_add_f32_dpp v39, v38, v39 quad_perm:[1,0,3,2] row_mask:0xf bank_mask:0xf
v_add_f32_dpp v47, v46, v47 quad_perm:[1,0,3,2] row_mask:0xf bank_mask:0xf
v_cndmask_b32 v40, v47, v39, s[94:95]
v_cndmask_b32 v41, v39, v47, s[94:95]
v_add_f32_e64 v41, v41, v13
v_add_f32_dpp v13, v40, v41 quad_perm:[2,3,0,1] row_mask:0xf bank_mask:0xf
v_mfma_f32_16x16x32_bf16 v[36:39], v[24:27], v[56:59], 0
v_cvt_pk_bf16_f32 v13, 0, v13
v_mul_f32 v13, 0x3db504f3, v13
v_accvgpr_read_b32 v22, a140
v_accvgpr_read_b32 v25, a141
v_accvgpr_read_b32 v27, a142
v_alignbit_b32 v23, v22, v22, 4
v_and_b32 v22, s98, v22
v_and_b32 v23, s98, v23
v_perm_b32 v24, v22, v25, s96
v_perm_b32 v25, v22, v25, s97
v_perm_b32 v26, v23, v27, s96
v_perm_b32 v27, v23, v27, s97
v_mfma_f32_16x16x32_bf16 v[40:43], v[28:31], v[56:59], 0
v_cvt_pk_bf16_f32 v13, 0, v13
v_mul_f32 v13, 0x3fb8aa3b, v13
buffer_load_dwordx4 a[68:71], v7, %[rv], s15 offen offset:2048 nt
v_accvgpr_read_b32 v22, a143
v_accvgpr_read_b32 v29, a148
v_accvgpr_read_b32 v31, a149
v_alignbit_b32 v23, v22, v22, 4
v_and_b32 v22, s98, v22
v_and_b32 v23, s98, v23
v_perm_b32 v28, v22, v29, s96
v_perm_b32 v29, v22, v29, s97
v_perm_b32 v30, v23, v31, s96
v_perm_b32 v31, v23, v31, s97
v_mfma_f32_16x16x32_bf16 v[44:47], v[24:27], v[56:59], 0
v_max_f32_e64 v4, v12, v13
v_accvgpr_read_b32 v22, a150
v_accvgpr_read_b32 v25, a151
v_accvgpr_read_b32 v27, a156
v_alignbit_b32 v23, v22, v22, 4
v_and_b32 v22, s98, v22
v_and_b32 v23, s98, v23
v_perm_b32 v24, v22, v25, s96
v_perm_b32 v25, v22, v25, s97
v_perm_b32 v26, v23, v27, s96
v_perm_b32 v27, v23, v27, s97
v_mfma_f32_16x16x32_bf16 v[48:51], v[28:31], v[56:59], 0
v_accvgpr_read_b32 v22, a157
v_accvgpr_read_b32 v29, a158
v_accvgpr_read_b32 v31, a159
v_alignbit_b32 v23, v22, v22, 4
v_and_b32 v22, s98, v22
v_and_b32 v23, s98, v23
v_perm_b32 v28, v22, v29, s96
v_perm_b32 v29, v22, v29, s97
v_perm_b32 v30, v23, v31, s96
v_perm_b32 v31, v23, v31, s97
v_mfma_f32_16x16x32_bf16 v[36:39], v[24:27], v[60:63], v[36:39]
v_accvgpr_read_b32 v22, a168
v_accvgpr_read_b32 v25, a169
v_accvgpr_read_b32 v27, a170
v_alignbit_b32 v23, v22, v22, 4
v_and_b32 v22, s98, v22
v_and_b32 v23, s98, v23
v_perm_b32 v24, v22, v25, s96
v_perm_b32 v25, v22, v25, s97
v_perm_b32 v26, v23, v27, s96
v_perm_b32 v27, v23, v27, s97
v_mfma_f32_16x16x32_bf16 v[40:43], v[28:31], v[60:63], v[40:43]
buffer_load_dwordx4 a[112:115], v7, %[rv], s15 offen offset:1024 nt
v_accvgpr_read_b32 v22, a171
v_accvgpr_read_b32 v29, a176
v_accvgpr_read_b32 v31, a177
v_alignbit_b32 v23, v22, v22, 4
v_and_b32 v22, s98, v22
v_and_b32 v23, s98, v23
v_perm_b32 v28, v22, v29, s96
v_perm_b32 v29, v22, v29, s97
v_perm_b32 v30, v23, v31, s96
v_perm_b32 v31, v23, v31, s97
v_mfma_f32_16x16x32_bf16 v[44:47], v[24:27], v[60:63], v[44:47]
v_accvgpr_read_b32 v22, a178
v_accvgpr_read_b32 v25, a179
v_accvgpr_read_b32 v27, a180
v_alignbit_b32 v23, v22, v22, 4
v_and_b32 v22, s98, v22
v_and_b32 v23, s98, v23
v_perm_b32 v24, v22, v25, s96
v_perm_b32 v25, v22, v25, s97
v_perm_b32 v26, v23, v27, s96
v_perm_b32 v27, v23, v27, s97
v_mfma_f32_16x16x32_bf16 v[48:51], v[28:31], v[60:63], v[48:51]
v_accvgpr_read_b32 v22, a181
v_accvgpr_read_b32 v29, a182
v_accvgpr_read_b32 v31, a183
v_alignbit_b32 v23, v22, v22, 4
v_and_b32 v22, s98, v22
v_and_b32 v23, s98, v23
v_perm_b32 v28, v22, v29, s96
v_perm_b32 v29, v22, v29, s97
v_perm_b32 v30, v23, v31, s96
v_perm_b32 v31, v23, v31, s97
v_mfma_f32_16x16x32_bf16 v[36:39], v[24:27], v[64:67], v[36:39]
v_accvgpr_read_b32 v22, a184
v_accvgpr_read_b32 v25, a185
v_accvgpr_read_b32 v27, a186
v_alignbit_b32 v23, v22, v22, 4
v_and_b32 v22, s98, v22
v_and_b32 v23, s98, v23
v_perm_b32 v24, v22, v25, s96
v_perm_b32 v25, v22, v25, s97
v_perm_b32 v26, v23, v27, s96
v_perm_b32 v27, v23, v27, s97
v_mfma_f32_16x16x32_bf16 v[40:43], v[28:31], v[64:67], v[40:43]
buffer_load_dwordx4 a[120:123], v7, %[rv], s15 offen offset:0 nt
)" R"(
v_accvgpr_read_b32 v22, a187
v_accvgpr_read_b32 v29, a192
v_accvgpr_read_b32 v31, a193
v_alignbit_b32 v23, v22, v22, 4
v_and_b32 v22, s98, v22
v_and_b32 v23, s98, v23
v_perm_b32 v28, v22, v29, s96
v_perm_b32 v29, v22, v29, s97
v_perm_b32 v30, v23, v31, s96
v_perm_b32 v31, v23, v31, s97
v_mfma_f32_16x16x32_bf16 v[44:47], v[24:27], v[64:67], v[44:47]
v_accvgpr_read_b32 v22, a194
v_accvgpr_read_b32 v25, a195
v_accvgpr_read_b32 v27, a200
v_alignbit_b32 v23, v22, v22, 4
v_and_b32 v22, s98, v22
v_and_b32 v23, s98, v23
v_perm_b32 v24, v22, v25, s96
v_perm_b32 v25, v22, v25, s97
v_perm_b32 v26, v23, v27, s96
v_perm_b32 v27, v23, v27, s97
v_mfma_f32_16x16x32_bf16 v[48:51], v[28:31], v[64:67], v[48:51]
v_accvgpr_read_b32 v22, a201
v_accvgpr_read_b32 v29, a202
v_accvgpr_read_b32 v31, a203
v_alignbit_b32 v23, v22, v22, 4
v_and_b32 v22, s98, v22
v_and_b32 v23, s98, v23
v_perm_b32 v28, v22, v29, s96
v_perm_b32 v29, v22, v29, s97
v_perm_b32 v30, v23, v31, s96
v_perm_b32 v31, v23, v31, s97
v_mfma_f32_16x16x32_bf16 v[36:39], v[24:27], v[68:71], v[36:39]
buffer_load_dwordx4 a[128:131], v7, %[rv], s23 offen offset:1024 nt
v_accvgpr_read_b32 v22, a92
v_accvgpr_read_b32 v25, a93
v_accvgpr_read_b32 v27, a94
v_alignbit_b32 v23, v22, v22, 4
v_and_b32 v22, s98, v22
v_and_b32 v23, s98, v23
v_perm_b32 v24, v22, v25, s96
v_perm_b32 v25, v22, v25, s97
v_perm_b32 v26, v23, v27, s96
v_perm_b32 v27, v23, v27, s97
v_mfma_f32_16x16x32_bf16 v[40:43], v[28:31], v[68:71], v[40:43]
v_accvgpr_read_b32 v22, a95
v_accvgpr_read_b32 v29, a100
v_accvgpr_read_b32 v31, a101
v_alignbit_b32 v23, v22, v22, 4
v_and_b32 v22, s98, v22
v_and_b32 v23, s98, v23
v_perm_b32 v28, v22, v29, s96
v_perm_b32 v29, v22, v29, s97
v_perm_b32 v30, v23, v31, s96
v_perm_b32 v31, v23, v31, s97
v_mfma_f32_16x16x32_bf16 v[44:47], v[24:27], v[68:71], v[44:47]
buffer_load_dwordx4 a[140:143], v7, %[rv], s23 offen offset:0 nt
v_accvgpr_read_b32 v22, a102
v_accvgpr_read_b32 v25, a103
v_accvgpr_read_b32 v27, a108
v_alignbit_b32 v23, v22, v22, 4
v_and_b32 v22, s98, v22
v_and_b32 v23, s98, v23
v_perm_b32 v24, v22, v25, s96
v_perm_b32 v25, v22, v25, s97
v_perm_b32 v26, v23, v27, s96
v_perm_b32 v27, v23, v27, s97
v_mfma_f32_16x16x32_bf16 v[48:51], v[28:31], v[68:71], v[48:51]
v_accvgpr_read_b32 v22, a109
v_accvgpr_read_b32 v29, a110
v_accvgpr_read_b32 v31, a111
v_alignbit_b32 v23, v22, v22, 4
v_and_b32 v22, s98, v22
v_and_b32 v23, s98, v23
v_perm_b32 v28, v22, v29, s96
v_perm_b32 v29, v22, v29, s97
v_perm_b32 v30, v23, v31, s96
v_perm_b32 v31, v23, v31, s97
v_cndmask_b32 v36, v36, v38, s[94:95]
v_cndmask_b32 v37, v37, v39, s[94:95]
v_cndmask_b32 v40, v40, v42, s[94:95]
v_cndmask_b32 v41, v41, v43, s[94:95]
v_cndmask_b32 v44, v44, v46, s[94:95]
v_cndmask_b32 v45, v45, v47, s[94:95]
v_cndmask_b32 v48, v48, v50, s[94:95]
v_cndmask_b32 v49, v49, v51, s[94:95]
v_cndmask_b32 v38, v40, v37, s[88:89]
v_cndmask_b32 v39, v36, v41, s[88:89]
v_cndmask_b32 v46, v48, v45, s[88:89]
v_cndmask_b32 v47, v44, v49, s[88:89]
v_add_f32_dpp v39, v38, v39 quad_perm:[1,0,3,2] row_mask:0xf bank_mask:0xf
v_add_f32_dpp v47, v46, v47 quad_perm:[1,0,3,2] row_mask:0xf bank_mask:0xf
v_cndmask_b32 v40, v47, v39, s[94:95]
v_cndmask_b32 v41, v39, v47, s[94:95]
v_add_f32_e64 v41, v41, v14
v_add_f32_dpp v14, v40, v41 quad_perm:[2,3,0,1] row_mask:0xf bank_mask:0xf
v_mfma_f32_16x16x32_bf16 v[36:39], v[24:27], v[56:59], 0
v_cvt_pk_bf16_f32 v14, 0, v14
v_mul_f32 v14, 0x3db504f3, v14
buffer_load_dwordx4 a[148:151], v7, %[rv], s14 offen offset:3072 nt
v_accvgpr_read_b32 v22, a132
v_accvgpr_read_b32 v25, a133
v_accvgpr_read_b32 v27, a134
v_alignbit_b32 v23, v22, v22, 4
v_and_b32 v22, s98, v22
v_and_b32 v23, s98, v23
v_perm_b32 v24, v22, v25, s96
v_perm_b32 v25, v22, v25, s97
v_perm_b32 v26, v23, v27, s96
v_perm_b32 v27, v23, v27, s97
v_mfma_f32_16x16x32_bf16 v[40:43], v[28:31], v[56:59], 0
v_cvt_pk_bf16_f32 v14, 0, v14
v_mul_f32 v14, 0x3fb8aa3b, v14
v_accvgpr_read_b32 v22, a135
v_accvgpr_read_b32 v29, a136
v_accvgpr_read_b32 v31, a137
v_alignbit_b32 v23, v22, v22, 4
v_and_b32 v22, s98, v22
v_and_b32 v23, s98, v23
v_perm_b32 v28, v22, v29, s96
v_perm_b32 v29, v22, v29, s97
v_perm_b32 v30, v23, v31, s96
v_perm_b32 v31, v23, v31, s97
v_mfma_f32_16x16x32_bf16 v[44:47], v[24:27], v[56:59], 0
buffer_load_dwordx4 a[156:159], v7, %[rv], s14 offen offset:2048 nt
v_accvgpr_read_b32 v22, a138
v_accvgpr_read_b32 v25, a139
v_accvgpr_read_b32 v27, a152
v_alignbit_b32 v23, v22, v22, 4
v_and_b32 v22, s98, v22
v_and_b32 v23, s98, v23
v_perm_b32 v24, v22, v25, s96
v_perm_b32 v25, v22, v25, s97
v_perm_b32 v26, v23, v27, s96
v_perm_b32 v27, v23, v27, s97
v_mfma_f32_16x16x32_bf16 v[48:51], v[28:31], v[56:59], 0
v_accvgpr_read_b32 v22, a153
v_accvgpr_read_b32 v29, a154
v_accvgpr_read_b32 v31, a155
v_alignbit_b32 v23, v22, v22, 4
v_and_b32 v22, s98, v22
v_and_b32 v23, s98, v23
v_perm_b32 v28, v22, v29, s96
v_perm_b32 v29, v22, v29, s97
v_perm_b32 v30, v23, v31, s96
v_perm_b32 v31, v23, v31, s97
v_mfma_f32_16x16x32_bf16 v[36:39], v[24:27], v[60:63], v[36:39]
v_accvgpr_read_b32 v22, a164
v_accvgpr_read_b32 v25, a165
v_accvgpr_read_b32 v27, a166
v_alignbit_b32 v23, v22, v22, 4
v_and_b32 v22, s98, v22
v_and_b32 v23, s98, v23
v_perm_b32 v24, v22, v25, s96
v_perm_b32 v25, v22, v25, s97
v_perm_b32 v26, v23, v27, s96
v_perm_b32 v27, v23, v27, s97
v_mfma_f32_16x16x32_bf16 v[40:43], v[28:31], v[60:63], v[40:43]
v_accvgpr_read_b32 v22, a167
v_accvgpr_read_b32 v29, a172
v_accvgpr_read_b32 v31, a173
v_alignbit_b32 v23, v22, v22, 4
v_and_b32 v22, s98, v22
v_and_b32 v23, s98, v23
v_perm_b32 v28, v22, v29, s96
v_perm_b32 v29, v22, v29, s97
v_perm_b32 v30, v23, v31, s96
v_perm_b32 v31, v23, v31, s97
v_mfma_f32_16x16x32_bf16 v[44:47], v[24:27], v[60:63], v[44:47]
buffer_load_dwordx4 a[168:171], v7, %[rv], s22 offen offset:3072 nt
v_accvgpr_read_b32 v22, a174
v_accvgpr_read_b32 v25, a175
v_alignbit_b32 v23, v22, v22, 4
v_and_b32 v22, s98, v22
v_and_b32 v23, s98, v23
v_perm_b32 v24, v22, v25, s96
v_perm_b32 v25, v22, v25, s97
v_perm_b32 v26, v23, v144, s96
v_perm_b32 v27, v23, v144, s97
v_mfma_f32_16x16x32_bf16 v[48:51], v[28:31], v[60:63], v[48:51]
v_accvgpr_read_b32 v22, a52
v_accvgpr_read_b32 v29, a53
v_accvgpr_read_b32 v31, a54
v_alignbit_b32 v23, v22, v22, 4
v_and_b32 v22, s98, v22
v_and_b32 v23, s98, v23
v_perm_b32 v28, v22, v29, s96
v_perm_b32 v29, v22, v29, s97
v_perm_b32 v30, v23, v31, s96
v_perm_b32 v31, v23, v31, s97
v_mfma_f32_16x16x32_bf16 v[36:39], v[24:27], v[64:67], v[36:39]
buffer_load_dwordx4 a[176:179], v7, %[rv], s22 offen offset:2048 nt
v_accvgpr_read_b32 v22, a55
v_accvgpr_read_b32 v25, a64
v_accvgpr_read_b32 v27, a65
v_alignbit_b32 v23, v22, v22, 4
v_and_b32 v22, s98, v22
v_and_b32 v23, s98, v23
v_perm_b32 v24, v22, v25, s96
v_perm_b32 v25, v22, v25, s97
v_perm_b32 v26, v23, v27, s96
v_perm_b32 v27, v23, v27, s97
v_mfma_f32_16x16x32_bf16 v[40:43], v[28:31], v[64:67], v[40:43]
v_accvgpr_read_b32 v22, a66
v_accvgpr_read_b32 v29, a67
v_accvgpr_read_b32 v31, a188
v_alignbit_b32 v23, v22, v22, 4
v_and_b32 v22, s98, v22
v_and_b32 v23, s98, v23
v_perm_b32 v28, v22, v29, s96
v_perm_b32 v29, v22, v29, s97
v_perm_b32 v30, v23, v31, s96
v_perm_b32 v31, v23, v31, s97
v_mfma_f32_16x16x32_bf16 v[44:47], v[24:27], v[64:67], v[44:47]
buffer_load_dwordx4 a[180:183], v7, %[rv], s14 offen offset:1024 nt
v_accvgpr_read_b32 v22, a189
v_accvgpr_read_b32 v25, a190
v_accvgpr_read_b32 v27, a191
v_alignbit_b32 v23, v22, v22, 4
v_and_b32 v22, s98, v22
v_and_b32 v23, s98, v23
v_perm_b32 v24, v22, v25, s96
v_perm_b32 v25, v22, v25, s97
v_perm_b32 v26, v23, v27, s96
v_perm_b32 v27, v23, v27, s97
v_mfma_f32_16x16x32_bf16 v[48:51], v[28:31], v[64:67], v[48:51]
v_accvgpr_read_b32 v22, a72
v_accvgpr_read_b32 v29, a73
v_accvgpr_read_b32 v31, a74
v_alignbit_b32 v23, v22, v22, 4
v_and_b32 v22, s98, v22
v_and_b32 v23, s98, v23
v_perm_b32 v28, v22, v29, s96
v_perm_b32 v29, v22, v29, s97
v_perm_b32 v30, v23, v31, s96
v_perm_b32 v31, v23, v31, s97
v_mfma_f32_16x16x32_bf16 v[36:39], v[24:27], v[68:71], v[36:39]
v_accvgpr_read_b32 v22, a75
v_accvgpr_read_b32 v25, a196
v_accvgpr_read_b32 v27, a197
v_alignbit_b32 v23, v22, v22, 4
v_and_b32 v22, s98, v22
v_and_b32 v23, s98, v23
v_perm_b32 v24, v22, v25, s96
v_perm_b32 v25, v22, v25, s97
v_perm_b32 v26, v23, v27, s96
v_perm_b32 v27, v23, v27, s97
v_mfma_f32_16x16x32_bf16 v[40:43], v[28:31], v[68:71], v[40:43]
buffer_load_dwordx4 a[184:187], v7, %[rv], s14 offen offset:0 nt
v_accvgpr_read_b32 v22, a198
v_accvgpr_read_b32 v29, a199
v_accvgpr_read_b32 v31, a76
v_alignbit_b32 v23, v22, v22, 4
v_and_b32 v22, s98, v22
v_and_b32 v23, s98, v23
v_perm_b32 v28, v22, v29, s96
v_perm_b32 v29, v22, v29, s97
v_perm_b32 v30, v23, v31, s96
v_perm_b32 v31, v23, v31, s97
v_mfma_f32_16x16x32_bf16 v[44:47], v[24:27], v[68:71], v[44:47]
s_waitcnt lgkmcnt(3)
v_alignbit_b32 v23, v252, v252, 4
v_and_b32 v22, s98, v252
v_and_b32_e64 v23, s98, v23
v_perm_b32 v24, v22, v253, s96
v_perm_b32 v25, v22, v253, s97
v_perm_b32 v26, v23, v254, s96
v_perm_b32 v27, v23, v254, s97
v_mfma_f32_16x16x32_bf16 v[48:51], v[28:31], v[68:71], v[48:51]
ds_read_b128 v[124:127], v53 offset:2048
s_waitcnt lgkmcnt(3)
v_alignbit_b32 v23, v255, v255, 4
v_and_b32 v22, s98, v255
v_and_b32_e64 v23, s98, v23
v_perm_b32 v28, v22, v248, s96
v_perm_b32 v29, v22, v248, s97
v_perm_b32 v30, v23, v249, s96
v_perm_b32 v31, v23, v249, s97
v_cndmask_b32 v36, v36, v38, s[94:95]
v_cndmask_b32 v37, v37, v39, s[94:95]
v_cndmask_b32 v40, v40, v42, s[94:95]
v_cndmask_b32 v41, v41, v43, s[94:95]
v_cndmask_b32 v44, v44, v46, s[94:95]
v_cndmask_b32 v45, v45, v47, s[94:95]
v_cndmask_b32 v48, v48, v50, s[94:95]
v_cndmask_b32 v49, v49, v51, s[94:95]
v_cndmask_b32 v38, v40, v37, s[88:89]
v_cndmask_b32 v39, v36, v41, s[88:89]
v_cndmask_b32 v46, v48, v45, s[88:89]
v_cndmask_b32 v47, v44, v49, s[88:89]
v_add_f32_dpp v39, v38, v39 quad_perm:[1,0,3,2] row_mask:0xf bank_mask:0xf
v_add_f32_dpp v47, v46, v47 quad_perm:[1,0,3,2] row_mask:0xf bank_mask:0xf
v_cndmask_b32 v40, v47, v39, s[94:95]
v_cndmask_b32 v41, v39, v47, s[94:95]
v_add_f32_e64 v41, v41, v15
v_add_f32_dpp v15, v40, v41 quad_perm:[2,3,0,1] row_mask:0xf bank_mask:0xf
v_mfma_f32_16x16x32_bf16 v[36:39], v[24:27], v[56:59], 0
v_cvt_pk_bf16_f32 v15, 0, v15
v_mul_f32 v15, 0x3db504f3, v15
buffer_load_dwordx4 a[192:195], v7, %[rv], s22 offen offset:1024 nt
ds_read_b128 v[128:131], v53 offset:3072
v_accvgpr_read_b32 v22, a212
v_accvgpr_read_b32 v25, a213
v_accvgpr_read_b32 v27, a214
v_alignbit_b32 v23, v22, v22, 4
v_and_b32 v22, s98, v22
v_and_b32 v23, s98, v23
v_perm_b32 v24, v22, v25, s96
v_perm_b32 v25, v22, v25, s97
v_perm_b32 v26, v23, v27, s96
v_perm_b32 v27, v23, v27, s97
v_mfma_f32_16x16x32_bf16 v[40:43], v[28:31], v[56:59], 0
v_cvt_pk_bf16_f32 v15, 0, v15
v_mul_f32 v15, 0x3fb8aa3b, v15
buffer_load_dwordx4 a[200:203], v7, %[rv], s22 offen offset:0 nt
v_accvgpr_read_b32 v22, a215
v_accvgpr_read_b32 v29, a216
v_accvgpr_read_b32 v31, a217
v_alignbit_b32 v23, v22, v22, 4
v_and_b32 v22, s98, v22
v_and_b32 v23, s98, v23
v_perm_b32 v28, v22, v29, s96
v_perm_b32 v29, v22, v29, s97
v_perm_b32 v30, v23, v31, s96
v_perm_b32 v31, v23, v31, s97
v_mfma_f32_16x16x32_bf16 v[44:47], v[24:27], v[56:59], 0
v_max3_f32 v4, v4, v14, v15
v_accvgpr_read_b32 v22, a218
v_accvgpr_read_b32 v25, a219
v_accvgpr_read_b32 v27, a220
v_alignbit_b32 v23, v22, v22, 4
v_and_b32 v22, s98, v22
v_and_b32 v23, s98, v23
v_perm_b32 v24, v22, v25, s96
v_perm_b32 v25, v22, v25, s97
v_perm_b32 v26, v23, v27, s96
v_perm_b32 v27, v23, v27, s97
v_mfma_f32_16x16x32_bf16 v[48:51], v[28:31], v[56:59], 0
buffer_load_dwordx4 a[92:95], v7, %[rv], s13 offen offset:3072 nt
v_accvgpr_read_b32 v22, a221
v_accvgpr_read_b32 v29, a222
v_accvgpr_read_b32 v31, a223
v_alignbit_b32 v23, v22, v22, 4
v_and_b32 v22, s98, v22
v_and_b32 v23, s98, v23
v_perm_b32 v28, v22, v29, s96
v_perm_b32 v29, v22, v29, s97
v_perm_b32 v30, v23, v31, s96
v_perm_b32 v31, v23, v31, s97
v_mfma_f32_16x16x32_bf16 v[36:39], v[24:27], v[60:63], v[36:39]
v_accvgpr_read_b32 v22, a224
v_accvgpr_read_b32 v25, a225
v_accvgpr_read_b32 v27, a226
)" R"(
v_alignbit_b32 v23, v22, v22, 4
v_and_b32 v22, s98, v22
v_and_b32 v23, s98, v23
v_perm_b32 v24, v22, v25, s96
v_perm_b32 v25, v22, v25, s97
v_perm_b32 v26, v23, v27, s96
v_perm_b32 v27, v23, v27, s97
v_mfma_f32_16x16x32_bf16 v[40:43], v[28:31], v[60:63], v[40:43]
v_accvgpr_read_b32 v22, a227
v_accvgpr_read_b32 v29, a228
v_accvgpr_read_b32 v31, a229
v_alignbit_b32 v23, v22, v22, 4
v_and_b32 v22, s98, v22
v_and_b32 v23, s98, v23
v_perm_b32 v28, v22, v29, s96
v_perm_b32 v29, v22, v29, s97
v_perm_b32 v30, v23, v31, s96
v_perm_b32 v31, v23, v31, s97
v_mfma_f32_16x16x32_bf16 v[44:47], v[24:27], v[60:63], v[44:47]
v_accvgpr_read_b32 v22, a230
v_accvgpr_read_b32 v25, a231
v_accvgpr_read_b32 v27, a232
v_alignbit_b32 v23, v22, v22, 4
v_and_b32 v22, s98, v22
v_and_b32 v23, s98, v23
v_perm_b32 v24, v22, v25, s96
v_perm_b32 v25, v22, v25, s97
v_perm_b32 v26, v23, v27, s96
v_perm_b32 v27, v23, v27, s97
v_mfma_f32_16x16x32_bf16 v[48:51], v[28:31], v[60:63], v[48:51]
buffer_load_dwordx4 a[100:103], v7, %[rv], s13 offen offset:2048 nt
v_accvgpr_read_b32 v22, a233
v_accvgpr_read_b32 v29, a234
v_accvgpr_read_b32 v31, a235
v_alignbit_b32 v23, v22, v22, 4
v_and_b32 v22, s98, v22
v_and_b32 v23, s98, v23
v_perm_b32 v28, v22, v29, s96
v_perm_b32 v29, v22, v29, s97
v_perm_b32 v30, v23, v31, s96
v_perm_b32 v31, v23, v31, s97
v_mfma_f32_16x16x32_bf16 v[36:39], v[24:27], v[64:67], v[36:39]
v_accvgpr_read_b32 v22, a236
v_accvgpr_read_b32 v25, a237
v_accvgpr_read_b32 v27, a238
v_alignbit_b32 v23, v22, v22, 4
v_and_b32 v22, s98, v22
v_and_b32 v23, s98, v23
v_perm_b32 v24, v22, v25, s96
v_perm_b32 v25, v22, v25, s97
v_perm_b32 v26, v23, v27, s96
v_perm_b32 v27, v23, v27, s97
v_mfma_f32_16x16x32_bf16 v[40:43], v[28:31], v[64:67], v[40:43]
v_accvgpr_read_b32 v22, a239
v_accvgpr_read_b32 v29, a240
v_accvgpr_read_b32 v31, a241
v_alignbit_b32 v23, v22, v22, 4
v_and_b32 v22, s98, v22
v_and_b32 v23, s98, v23
v_perm_b32 v28, v22, v29, s96
v_perm_b32 v29, v22, v29, s97
v_perm_b32 v30, v23, v31, s96
v_perm_b32 v31, v23, v31, s97
v_mfma_f32_16x16x32_bf16 v[44:47], v[24:27], v[64:67], v[44:47]
v_accvgpr_read_b32 v22, a242
v_accvgpr_read_b32 v25, a243
v_accvgpr_read_b32 v27, a244
v_alignbit_b32 v23, v22, v22, 4
v_and_b32 v22, s98, v22
v_and_b32 v23, s98, v23
v_perm_b32 v24, v22, v25, s96
v_perm_b32 v25, v22, v25, s97
v_perm_b32 v26, v23, v27, s96
v_perm_b32 v27, v23, v27, s97
v_mfma_f32_16x16x32_bf16 v[48:51], v[28:31], v[64:67], v[48:51]
buffer_load_dwordx4 a[108:111], v7, %[rv], s21 offen offset:3072 nt
v_accvgpr_read_b32 v22, a245
v_accvgpr_read_b32 v29, a246
v_accvgpr_read_b32 v31, a247
v_alignbit_b32 v23, v22, v22, 4
v_and_b32 v22, s98, v22
v_and_b32 v23, s98, v23
v_perm_b32 v28, v22, v29, s96
v_perm_b32 v29, v22, v29, s97
v_perm_b32 v30, v23, v31, s96
v_perm_b32 v31, v23, v31, s97
v_mfma_f32_16x16x32_bf16 v[36:39], v[24:27], v[68:71], v[36:39]
buffer_load_dwordx4 a[132:135], v7, %[rv], s21 offen offset:2048 nt
v_accvgpr_read_b32 v22, a248
v_accvgpr_read_b32 v25, a249
v_accvgpr_read_b32 v27, a250
v_alignbit_b32 v23, v22, v22, 4
v_and_b32 v22, s98, v22
v_and_b32 v23, s98, v23
v_perm_b32 v24, v22, v25, s96
v_perm_b32 v25, v22, v25, s97
v_perm_b32 v26, v23, v27, s96
v_perm_b32 v27, v23, v27, s97
v_mfma_f32_16x16x32_bf16 v[40:43], v[28:31], v[68:71], v[40:43]
v_accvgpr_read_b32 v22, a251
v_accvgpr_read_b32 v29, a252
v_accvgpr_read_b32 v31, a253
v_alignbit_b32 v23, v22, v22, 4
v_and_b32 v22, s98, v22
v_and_b32 v23, s98, v23
v_perm_b32 v28, v22, v29, s96
v_perm_b32 v29, v22, v29, s97
v_perm_b32 v30, v23, v31, s96
v_perm_b32 v31, v23, v31, s97
v_mfma_f32_16x16x32_bf16 v[44:47], v[24:27], v[68:71], v[44:47]
buffer_load_dwordx4 a[136:139], v7, %[rv], s13 offen offset:1024 nt
s_waitcnt lgkmcnt(3)
v_alignbit_b32 v23, v116, v116, 4
v_and_b32 v22, s98, v116
v_and_b32_e64 v23, s98, v23
v_perm_b32 v24, v22, v117, s96
v_perm_b32 v25, v22, v117, s97
v_perm_b32 v26, v23, v118, s96
v_perm_b32 v27, v23, v118, s97
v_mfma_f32_16x16x32_bf16 v[48:51], v[28:31], v[68:71], v[48:51]
ds_read_b128 v[252:255], v53 offset:4096
s_waitcnt lgkmcnt(3)
v_alignbit_b32 v23, v119, v119, 4
v_and_b32 v22, s98, v119
v_and_b32_e64 v23, s98, v23
v_perm_b32 v28, v22, v120, s96
v_perm_b32 v29, v22, v120, s97
v_perm_b32 v30, v23, v121, s96
v_perm_b32 v31, v23, v121, s97
v_cndmask_b32 v36, v36, v38, s[94:95]
v_cndmask_b32 v37, v37, v39, s[94:95]
v_cndmask_b32 v40, v40, v42, s[94:95]
v_cndmask_b32 v41, v41, v43, s[94:95]
v_cndmask_b32 v44, v44, v46, s[94:95]
v_cndmask_b32 v45, v45, v47, s[94:95]
v_cndmask_b32 v48, v48, v50, s[94:95]
v_cndmask_b32 v49, v49, v51, s[94:95]
v_cndmask_b32 v38, v40, v37, s[88:89]
v_cndmask_b32 v39, v36, v41, s[88:89]
v_cndmask_b32 v46, v48, v45, s[88:89]
v_cndmask_b32 v47, v44, v49, s[88:89]
v_add_f32_dpp v39, v38, v39 quad_perm:[1,0,3,2] row_mask:0xf bank_mask:0xf
v_add_f32_dpp v47, v46, v47 quad_perm:[1,0,3,2] row_mask:0xf bank_mask:0xf
v_cndmask_b32 v40, v47, v39, s[94:95]
v_cndmask_b32 v41, v39, v47, s[94:95]
v_add_f32_e64 v41, v41, v11
v_add_f32_dpp v11, v40, v41 quad_perm:[2,3,0,1] row_mask:0xf bank_mask:0xf
v_mfma_f32_16x16x32_bf16 v[36:39], v[24:27], v[56:59], 0
v_cvt_pk_bf16_f32 v11, 0, v11
v_mul_f32 v11, 0x3db504f3, v11
buffer_load_dwordx4 a[152:155], v7, %[rv], s13 offen offset:0 nt
ds_read_b128 v[248:251], v53 offset:5120
s_waitcnt lgkmcnt(3)
v_alignbit_b32 v23, v122, v122, 4
v_and_b32 v22, s98, v122
v_and_b32_e64 v23, s98, v23
v_perm_b32 v24, v22, v123, s96
v_perm_b32 v25, v22, v123, s97
v_perm_b32 v26, v23, v124, s96
v_perm_b32 v27, v23, v124, s97
v_mfma_f32_16x16x32_bf16 v[40:43], v[28:31], v[56:59], 0
v_cvt_pk_bf16_f32 v11, 0, v11
v_mul_f32 v11, 0x3fb8aa3b, v11
ds_read_b128 v[116:119], v53 offset:6144
v_alignbit_b32 v23, v125, v125, 4
v_and_b32 v22, s98, v125
v_and_b32 v23, s98, v23
v_perm_b32 v28, v22, v126, s96
v_perm_b32 v29, v22, v126, s97
v_perm_b32 v30, v23, v127, s96
v_perm_b32 v31, v23, v127, s97
v_mfma_f32_16x16x32_bf16 v[44:47], v[24:27], v[56:59], 0
s_waitcnt lgkmcnt(3)
v_alignbit_b32 v23, v128, v128, 4
v_and_b32 v22, s98, v128
v_and_b32_e64 v23, s98, v23
v_perm_b32 v24, v22, v129, s96
v_perm_b32 v25, v22, v129, s97
v_perm_b32 v26, v23, v130, s96
v_perm_b32 v27, v23, v130, s97
v_mfma_f32_16x16x32_bf16 v[48:51], v[28:31], v[56:59], 0
ds_read_b128 v[120:123], v53 offset:7168
s_waitcnt lgkmcnt(3)
v_alignbit_b32 v23, v131, v131, 4
v_and_b32 v22, s98, v131
v_and_b32_e64 v23, s98, v23
v_perm_b32 v28, v22, v252, s96
v_perm_b32 v29, v22, v252, s97
v_perm_b32 v30, v23, v253, s96
v_perm_b32 v31, v23, v253, s97
v_mfma_f32_16x16x32_bf16 v[36:39], v[24:27], v[60:63], v[36:39]
ds_read_b128 v[124:127], v53 offset:8192
s_waitcnt lgkmcnt(3)
v_alignbit_b32 v23, v254, v254, 4
v_and_b32 v22, s98, v254
v_and_b32_e64 v23, s98, v23
v_perm_b32 v24, v22, v255, s96
v_perm_b32 v25, v22, v255, s97
v_perm_b32 v26, v23, v248, s96
v_perm_b32 v27, v23, v248, s97
v_mfma_f32_16x16x32_bf16 v[40:43], v[28:31], v[60:63], v[40:43]
buffer_load_dwordx4 a[164:167], v7, %[rv], s12 offen offset:3072 nt
ds_read_b128 v[128:131], v53 offset:9216
v_alignbit_b32 v23, v249, v249, 4
v_and_b32 v22, s98, v249
v_and_b32 v23, s98, v23
v_perm_b32 v28, v22, v250, s96
v_perm_b32 v29, v22, v250, s97
v_perm_b32 v30, v23, v251, s96
v_perm_b32 v31, v23, v251, s97
v_mfma_f32_16x16x32_bf16 v[44:47], v[24:27], v[60:63], v[44:47]
s_waitcnt lgkmcnt(3)
v_alignbit_b32 v23, v116, v116, 4
v_and_b32 v22, s98, v116
v_and_b32_e64 v23, s98, v23
v_perm_b32 v24, v22, v117, s96
v_perm_b32 v25, v22, v117, s97
v_perm_b32 v26, v23, v118, s96
v_perm_b32 v27, v23, v118, s97
v_mfma_f32_16x16x32_bf16 v[48:51], v[28:31], v[60:63], v[48:51]
ds_read_b128 v[252:255], v53 offset:10240
s_waitcnt lgkmcnt(3)
v_alignbit_b32 v23, v119, v119, 4
v_and_b32 v22, s98, v119
v_and_b32_e64 v23, s98, v23
v_perm_b32 v28, v22, v120, s96
v_perm_b32 v29, v22, v120, s97
v_perm_b32 v30, v23, v121, s96
v_perm_b32 v31, v23, v121, s97
v_mfma_f32_16x16x32_bf16 v[36:39], v[24:27], v[64:67], v[36:39]
ds_read_b128 v[248:251], v53 offset:11264
s_waitcnt lgkmcnt(3)
v_alignbit_b32 v23, v122, v122, 4
v_and_b32 v22, s98, v122
v_and_b32_e64 v23, s98, v23
v_perm_b32 v24, v22, v123, s96
v_perm_b32 v25, v22, v123, s97
v_perm_b32 v26, v23, v124, s96
v_perm_b32 v27, v23, v124, s97
v_mfma_f32_16x16x32_bf16 v[40:43], v[28:31], v[64:67], v[40:43]
buffer_load_dwordx4 a[172:175], v7, %[rv], s12 offen offset:2048 nt
ds_read_b128 v[116:119], v53 offset:12288
v_alignbit_b32 v23, v125, v125, 4
v_and_b32 v22, s98, v125
v_and_b32 v23, s98, v23
v_perm_b32 v28, v22, v126, s96
v_perm_b32 v29, v22, v126, s97
v_perm_b32 v30, v23, v127, s96
v_perm_b32 v31, v23, v127, s97
v_mfma_f32_16x16x32_bf16 v[44:47], v[24:27], v[64:67], v[44:47]
s_waitcnt lgkmcnt(3)
v_alignbit_b32 v23, v128, v128, 4
v_and_b32 v22, s98, v128
v_and_b32_e64 v23, s98, v23
v_perm_b32 v24, v22, v129, s96
v_perm_b32 v25, v22, v129, s97
v_perm_b32 v26, v23, v130, s96
v_perm_b32 v27, v23, v130, s97
v_mfma_f32_16x16x32_bf16 v[48:51], v[28:31], v[64:67], v[48:51]
ds_read_b128 v[120:123], v53 offset:13312
s_waitcnt lgkmcnt(3)
v_alignbit_b32 v23, v131, v131, 4
v_and_b32 v22, s98, v131
v_and_b32_e64 v23, s98, v23
v_perm_b32 v28, v22, v252, s96
v_perm_b32 v29, v22, v252, s97
v_perm_b32 v30, v23, v253, s96
v_perm_b32 v31, v23, v253, s97
v_mfma_f32_16x16x32_bf16 v[36:39], v[24:27], v[68:71], v[36:39]
ds_read_b128 v[124:127], v53 offset:14336
s_waitcnt lgkmcnt(3)
v_alignbit_b32 v23, v254, v254, 4
v_and_b32 v22, s98, v254
v_and_b32_e64 v23, s98, v23
v_perm_b32 v24, v22, v255, s96
v_perm_b32 v25, v22, v255, s97
v_perm_b32 v26, v23, v248, s96
v_perm_b32 v27, v23, v248, s97
v_mfma_f32_16x16x32_bf16 v[40:43], v[28:31], v[68:71], v[40:43]
buffer_load_dwordx4 a[52:55], v7, %[rv], s21 offen offset:1024 nt
ds_read_b128 v[128:131], v53 offset:15360
v_alignbit_b32 v23, v249, v249, 4
v_and_b32 v22, s98, v249
v_and_b32 v23, s98, v23
v_perm_b32 v28, v22, v250, s96
v_perm_b32 v29, v22, v250, s97
v_perm_b32 v30, v23, v251, s96
v_perm_b32 v31, v23, v251, s97
v_mfma_f32_16x16x32_bf16 v[44:47], v[24:27], v[68:71], v[44:47]
s_waitcnt lgkmcnt(3)
v_alignbit_b32 v23, v116, v116, 4
v_and_b32 v22, s98, v116
v_and_b32_e64 v23, s98, v23
v_perm_b32 v24, v22, v117, s96
v_perm_b32 v25, v22, v117, s97
v_perm_b32 v26, v23, v118, s96
v_perm_b32 v27, v23, v118, s97
v_mfma_f32_16x16x32_bf16 v[48:51], v[28:31], v[68:71], v[48:51]
buffer_load_dwordx4 a[64:67], v7, %[rv], s21 offen offset:0 nt
ds_read_b128 v[252:255], v53 offset:16384
s_waitcnt lgkmcnt(3)
v_alignbit_b32 v23, v119, v119, 4
v_and_b32 v22, s98, v119
v_and_b32_e64 v23, s98, v23
v_perm_b32 v28, v22, v120, s96
v_perm_b32 v29, v22, v120, s97
v_perm_b32 v30, v23, v121, s96
v_perm_b32 v31, v23, v121, s97
v_cndmask_b32 v36, v36, v38, s[94:95]
v_cndmask_b32 v37, v37, v39, s[94:95]
v_cndmask_b32 v40, v40, v42, s[94:95]
v_cndmask_b32 v41, v41, v43, s[94:95]
v_cndmask_b32 v44, v44, v46, s[94:95]
v_cndmask_b32 v45, v45, v47, s[94:95]
v_cndmask_b32 v48, v48, v50, s[94:95]
v_cndmask_b32 v49, v49, v51, s[94:95]
v_cndmask_b32 v38, v40, v37, s[88:89]
v_cndmask_b32 v39, v36, v41, s[88:89]
v_cndmask_b32 v46, v48, v45, s[88:89]
v_cndmask_b32 v47, v44, v49, s[88:89]
v_add_f32_dpp v39, v38, v39 quad_perm:[1,0,3,2] row_mask:0xf bank_mask:0xf
v_add_f32_dpp v47, v46, v47 quad_perm:[1,0,3,2] row_mask:0xf bank_mask:0xf
v_cndmask_b32 v40, v47, v39, s[94:95]
v_cndmask_b32 v41, v39, v47, s[94:95]
v_add_f32_e64 v41, v41, v8
v_add_f32_dpp v8, v40, v41 quad_perm:[2,3,0,1] row_mask:0xf bank_mask:0xf
v_mfma_f32_16x16x32_bf16 v[36:39], v[24:27], v[56:59], 0
v_cvt_pk_bf16_f32 v8, 0, v8
v_mul_f32 v8, 0x3db504f3, v8
buffer_load_dwordx4 a[188:191], v7, %[rv], s12 offen offset:1024 nt
ds_read_b128 v[248:251], v53 offset:17408
s_waitcnt lgkmcnt(3)
v_alignbit_b32 v23, v122, v122, 4
v_and_b32 v22, s98, v122
v_and_b32_e64 v23, s98, v23
v_perm_b32 v24, v22, v123, s96
v_perm_b32 v25, v22, v123, s97
v_perm_b32 v26, v23, v124, s96
v_perm_b32 v27, v23, v124, s97
v_mfma_f32_16x16x32_bf16 v[40:43], v[28:31], v[56:59], 0
v_cvt_pk_bf16_f32 v8, 0, v8
v_mul_f32 v8, 0x3fb8aa3b, v8
ds_read_b128 v[116:119], v53 offset:18432
)" R"(
v_alignbit_b32 v23, v125, v125, 4
v_and_b32 v22, s98, v125
v_and_b32 v23, s98, v23
v_perm_b32 v28, v22, v126, s96
v_perm_b32 v29, v22, v126, s97
v_perm_b32 v30, v23, v127, s96
v_perm_b32 v31, v23, v127, s97
v_mfma_f32_16x16x32_bf16 v[44:47], v[24:27], v[56:59], 0
v_max3_f32 v4, v4, v11, v8
s_waitcnt lgkmcnt(3)
v_alignbit_b32 v23, v128, v128, 4
v_and_b32 v22, s98, v128
v_and_b32_e64 v23, s98, v23
v_perm_b32 v24, v22, v129, s96
v_perm_b32 v25, v22, v129, s97
v_perm_b32 v26, v23, v130, s96
v_perm_b32 v27, v23, v130, s97
v_mfma_f32_16x16x32_bf16 v[48:51], v[28:31], v[56:59], 0
buffer_load_dwordx4 a[72:75], v7, %[rv], s12 offen offset:0 nt
ds_read_b128 v[120:123], v53 offset:19456
s_waitcnt lgkmcnt(3)
v_alignbit_b32 v23, v131, v131, 4
v_and_b32 v22, s98, v131
v_and_b32_e64 v23, s98, v23
v_perm_b32 v28, v22, v252, s96
v_perm_b32 v29, v22, v252, s97
v_perm_b32 v30, v23, v253, s96
v_perm_b32 v31, v23, v253, s97
v_mfma_f32_16x16x32_bf16 v[36:39], v[24:27], v[60:63], v[36:39]
ds_read_b128 v[124:127], v53 offset:20480
s_waitcnt lgkmcnt(3)
v_alignbit_b32 v23, v254, v254, 4
v_and_b32 v22, s98, v254
v_and_b32_e64 v23, s98, v23
v_perm_b32 v24, v22, v255, s96
v_perm_b32 v25, v22, v255, s97
v_perm_b32 v26, v23, v248, s96
v_perm_b32 v27, v23, v248, s97
v_mfma_f32_16x16x32_bf16 v[40:43], v[28:31], v[60:63], v[40:43]
ds_read_b128 v[128:131], v53 offset:21504
v_alignbit_b32 v23, v249, v249, 4
v_and_b32 v22, s98, v249
v_and_b32 v23, s98, v23
v_perm_b32 v28, v22, v250, s96
v_perm_b32 v29, v22, v250, s97
v_perm_b32 v30, v23, v251, s96
v_perm_b32 v31, v23, v251, s97
v_mfma_f32_16x16x32_bf16 v[44:47], v[24:27], v[60:63], v[44:47]
s_waitcnt lgkmcnt(3)
v_alignbit_b32 v23, v116, v116, 4
v_and_b32 v22, s98, v116
v_and_b32_e64 v23, s98, v23
v_perm_b32 v24, v22, v117, s96
v_perm_b32 v25, v22, v117, s97
v_perm_b32 v26, v23, v118, s96
v_perm_b32 v27, v23, v118, s97
v_mfma_f32_16x16x32_bf16 v[48:51], v[28:31], v[60:63], v[48:51]
ds_read_b128 v[252:255], v53 offset:22528
s_waitcnt lgkmcnt(3)
v_alignbit_b32 v23, v119, v119, 4
v_and_b32 v22, s98, v119
v_and_b32_e64 v23, s98, v23
v_perm_b32 v28, v22, v120, s96
v_perm_b32 v29, v22, v120, s97
v_perm_b32 v30, v23, v121, s96
v_perm_b32 v31, v23, v121, s97
v_mfma_f32_16x16x32_bf16 v[36:39], v[24:27], v[64:67], v[36:39]
buffer_load_dwordx4 a[196:199], v7, %[rv], s20 offen offset:3072 nt
ds_read_b128 v[248:251], v53 offset:23552
s_waitcnt lgkmcnt(3)
v_alignbit_b32 v23, v122, v122, 4
v_and_b32 v22, s98, v122
v_and_b32_e64 v23, s98, v23
v_perm_b32 v24, v22, v123, s96
v_perm_b32 v25, v22, v123, s97
v_perm_b32 v26, v23, v124, s96
v_perm_b32 v27, v23, v124, s97
v_mfma_f32_16x16x32_bf16 v[40:43], v[28:31], v[64:67], v[40:43]
ds_read_b128 v[116:119], v53 offset:24576
v_alignbit_b32 v23, v125, v125, 4
v_and_b32 v22, s98, v125
v_and_b32 v23, s98, v23
v_perm_b32 v28, v22, v126, s96
v_perm_b32 v29, v22, v126, s97
v_perm_b32 v30, v23, v127, s96
v_perm_b32 v31, v23, v127, s97
v_mfma_f32_16x16x32_bf16 v[44:47], v[24:27], v[64:67], v[44:47]
buffer_load_dwordx4 a[212:215], v7, %[rv], s20 offen offset:2048 nt
s_waitcnt lgkmcnt(3)
v_alignbit_b32 v23, v128, v128, 4
v_and_b32 v22, s98, v128
v_and_b32_e64 v23, s98, v23
v_perm_b32 v24, v22, v129, s96
v_perm_b32 v25, v22, v129, s97
v_perm_b32 v26, v23, v130, s96
v_perm_b32 v27, v23, v130, s97
v_mfma_f32_16x16x32_bf16 v[48:51], v[28:31], v[64:67], v[48:51]
ds_read_b128 v[120:123], v53 offset:25600
s_waitcnt lgkmcnt(3)
v_alignbit_b32 v23, v131, v131, 4
v_and_b32 v22, s98, v131
v_and_b32_e64 v23, s98, v23
v_perm_b32 v28, v22, v252, s96
v_perm_b32 v29, v22, v252, s97
v_perm_b32 v30, v23, v253, s96
v_perm_b32 v31, v23, v253, s97
v_mfma_f32_16x16x32_bf16 v[36:39], v[24:27], v[68:71], v[36:39]
buffer_load_dwordx4 a[216:219], v7, %[rv], s11 offen offset:3072 nt
ds_read_b128 v[124:127], v53 offset:26624
s_waitcnt lgkmcnt(3)
v_alignbit_b32 v23, v254, v254, 4
v_and_b32 v22, s98, v254
v_and_b32_e64 v23, s98, v23
v_perm_b32 v24, v22, v255, s96
v_perm_b32 v25, v22, v255, s97
v_perm_b32 v26, v23, v248, s96
v_perm_b32 v27, v23, v248, s97
v_mfma_f32_16x16x32_bf16 v[40:43], v[28:31], v[68:71], v[40:43]
ds_read_b128 v[128:131], v53 offset:27648
v_alignbit_b32 v23, v249, v249, 4
v_and_b32 v22, s98, v249
v_and_b32 v23, s98, v23
v_perm_b32 v28, v22, v250, s96
v_perm_b32 v29, v22, v250, s97
v_perm_b32 v30, v23, v251, s96
v_perm_b32 v31, v23, v251, s97
v_mfma_f32_16x16x32_bf16 v[44:47], v[24:27], v[68:71], v[44:47]
s_waitcnt lgkmcnt(3)
v_alignbit_b32 v23, v116, v116, 4
v_and_b32 v22, s98, v116
v_and_b32_e64 v23, s98, v23
v_perm_b32 v24, v22, v117, s96
v_perm_b32 v25, v22, v117, s97
v_perm_b32 v26, v23, v118, s96
v_perm_b32 v27, v23, v118, s97
v_mfma_f32_16x16x32_bf16 v[48:51], v[28:31], v[68:71], v[48:51]
ds_read_b128 v[252:255], v53 offset:28672
s_waitcnt lgkmcnt(3)
v_alignbit_b32 v23, v119, v119, 4
v_and_b32 v22, s98, v119
v_and_b32_e64 v23, s98, v23
v_perm_b32 v28, v22, v120, s96
v_perm_b32 v29, v22, v120, s97
v_perm_b32 v30, v23, v121, s96
v_perm_b32 v31, v23, v121, s97
v_cndmask_b32 v36, v36, v38, s[94:95]
v_cndmask_b32 v37, v37, v39, s[94:95]
v_cndmask_b32 v40, v40, v42, s[94:95]
v_cndmask_b32 v41, v41, v43, s[94:95]
v_cndmask_b32 v44, v44, v46, s[94:95]
v_cndmask_b32 v45, v45, v47, s[94:95]
v_cndmask_b32 v48, v48, v50, s[94:95]
v_cndmask_b32 v49, v49, v51, s[94:95]
v_cndmask_b32 v38, v40, v37, s[88:89]
v_cndmask_b32 v39, v36, v41, s[88:89]
v_cndmask_b32 v46, v48, v45, s[88:89]
v_cndmask_b32 v47, v44, v49, s[88:89]
v_add_f32_dpp v39, v38, v39 quad_perm:[1,0,3,2] row_mask:0xf bank_mask:0xf
v_add_f32_dpp v47, v46, v47 quad_perm:[1,0,3,2] row_mask:0xf bank_mask:0xf
v_cndmask_b32 v40, v47, v39, s[94:95]
v_cndmask_b32 v41, v39, v47, s[94:95]
v_add_f32_e64 v41, v41, v9
v_add_f32_dpp v9, v40, v41 quad_perm:[2,3,0,1] row_mask:0xf bank_mask:0xf
v_mfma_f32_16x16x32_bf16 v[36:39], v[24:27], v[56:59], 0
v_cvt_pk_bf16_f32 v9, 0, v9
v_mul_f32 v9, 0x3db504f3, v9
buffer_load_dwordx4 a[220:223], v7, %[rv], s11 offen offset:2048 nt
ds_read_b128 v[248:251], v53 offset:29696
s_waitcnt lgkmcnt(3)
v_alignbit_b32 v23, v122, v122, 4
v_and_b32 v22, s98, v122
v_and_b32_e64 v23, s98, v23
v_perm_b32 v24, v22, v123, s96
v_perm_b32 v25, v22, v123, s97
v_perm_b32 v26, v23, v124, s96
v_perm_b32 v27, v23, v124, s97
v_mfma_f32_16x16x32_bf16 v[40:43], v[28:31], v[56:59], 0
v_cvt_pk_bf16_f32 v9, 0, v9
v_mul_f32 v9, 0x3fb8aa3b, v9
ds_read_b128 v[116:119], v53 offset:30720
v_alignbit_b32 v23, v125, v125, 4
v_and_b32 v22, s98, v125
v_and_b32 v23, s98, v23
v_perm_b32 v28, v22, v126, s96
v_perm_b32 v29, v22, v126, s97
v_perm_b32 v30, v23, v127, s96
v_perm_b32 v31, v23, v127, s97
v_mfma_f32_16x16x32_bf16 v[44:47], v[24:27], v[56:59], 0
buffer_load_dwordx4 a[224:227], v7, %[rv], s20 offen offset:1024 nt
s_waitcnt lgkmcnt(3)
v_alignbit_b32 v23, v128, v128, 4
v_and_b32 v22, s98, v128
v_and_b32_e64 v23, s98, v23
v_perm_b32 v24, v22, v129, s96
v_perm_b32 v25, v22, v129, s97
v_perm_b32 v26, v23, v130, s96
v_perm_b32 v27, v23, v130, s97
v_mfma_f32_16x16x32_bf16 v[48:51], v[28:31], v[56:59], 0
ds_read_b128 v[120:123], v53 offset:31744
s_waitcnt lgkmcnt(3)
v_alignbit_b32 v23, v131, v131, 4
v_and_b32 v22, s98, v131
v_and_b32_e64 v23, s98, v23
v_perm_b32 v28, v22, v252, s96
v_perm_b32 v29, v22, v252, s97
v_perm_b32 v30, v23, v253, s96
v_perm_b32 v31, v23, v253, s97
v_mfma_f32_16x16x32_bf16 v[36:39], v[24:27], v[60:63], v[36:39]
ds_read_b128 v[124:127], v53 offset:32768
s_waitcnt lgkmcnt(3)
v_alignbit_b32 v23, v254, v254, 4
v_and_b32 v22, s98, v254
v_and_b32_e64 v23, s98, v23
v_perm_b32 v24, v22, v255, s96
v_perm_b32 v25, v22, v255, s97
v_perm_b32 v26, v23, v248, s96
v_perm_b32 v27, v23, v248, s97
v_mfma_f32_16x16x32_bf16 v[40:43], v[28:31], v[60:63], v[40:43]
buffer_load_dwordx4 a[228:231], v7, %[rv], s20 offen offset:0 nt
ds_read_b128 v[128:131], v53 offset:33792
v_alignbit_b32 v23, v249, v249, 4
v_and_b32 v22, s98, v249
v_and_b32 v23, s98, v23
v_perm_b32 v28, v22, v250, s96
v_perm_b32 v29, v22, v250, s97
v_perm_b32 v30, v23, v251, s96
v_perm_b32 v31, v23, v251, s97
v_mfma_f32_16x16x32_bf16 v[44:47], v[24:27], v[60:63], v[44:47]
s_waitcnt lgkmcnt(3)
v_alignbit_b32 v23, v116, v116, 4
v_and_b32 v22, s98, v116
v_and_b32_e64 v23, s98, v23
v_perm_b32 v24, v22, v117, s96
v_perm_b32 v25, v22, v117, s97
v_perm_b32 v26, v23, v118, s96
v_perm_b32 v27, v23, v118, s97
v_mfma_f32_16x16x32_bf16 v[48:51], v[28:31], v[60:63], v[48:51]
buffer_load_dwordx4 a[232:235], v7, %[rv], s11 offen offset:1024 nt
ds_read_b128 v[252:255], v53 offset:34816
s_waitcnt lgkmcnt(3)
v_alignbit_b32 v23, v119, v119, 4
v_and_b32 v22, s98, v119
v_and_b32_e64 v23, s98, v23
v_perm_b32 v28, v22, v120, s96
v_perm_b32 v29, v22, v120, s97
v_perm_b32 v30, v23, v121, s96
v_perm_b32 v31, v23, v121, s97
v_mfma_f32_16x16x32_bf16 v[36:39], v[24:27], v[64:67], v[36:39]
ds_read_b128 v[248:251], v53 offset:35840
s_waitcnt lgkmcnt(3)
v_alignbit_b32 v23, v122, v122, 4
v_and_b32 v22, s98, v122
v_and_b32_e64 v23, s98, v23
v_perm_b32 v24, v22, v123, s96
v_perm_b32 v25, v22, v123, s97
v_perm_b32 v26, v23, v124, s96
v_perm_b32 v27, v23, v124, s97
v_mfma_f32_16x16x32_bf16 v[40:43], v[28:31], v[64:67], v[40:43]
v_alignbit_b32 v23, v125, v125, 4
v_and_b32 v22, s98, v125
v_and_b32 v23, s98, v23
v_perm_b32 v28, v22, v126, s96
v_perm_b32 v29, v22, v126, s97
v_perm_b32 v30, v23, v127, s96
v_perm_b32 v31, v23, v127, s97
v_mfma_f32_16x16x32_bf16 v[44:47], v[24:27], v[64:67], v[44:47]
s_waitcnt lgkmcnt(2)
v_alignbit_b32 v23, v128, v128, 4
v_and_b32 v22, s98, v128
v_and_b32_e64 v23, s98, v23
v_perm_b32 v24, v22, v129, s96
v_perm_b32 v25, v22, v129, s97
v_perm_b32 v26, v23, v130, s96
v_perm_b32 v27, v23, v130, s97
v_mfma_f32_16x16x32_bf16 v[48:51], v[28:31], v[64:67], v[48:51]
buffer_load_dwordx4 a[236:239], v7, %[rv], s11 offen offset:0 nt
s_waitcnt lgkmcnt(1)
v_alignbit_b32 v23, v131, v131, 4
v_and_b32 v22, s98, v131
v_and_b32_e64 v23, s98, v23
v_perm_b32 v28, v22, v252, s96
v_perm_b32 v29, v22, v252, s97
v_perm_b32 v30, v23, v253, s96
v_perm_b32 v31, v23, v253, s97
v_mfma_f32_16x16x32_bf16 v[36:39], v[24:27], v[68:71], v[36:39]
s_waitcnt lgkmcnt(0)
v_alignbit_b32 v23, v254, v254, 4
v_and_b32 v22, s98, v254
v_and_b32_e64 v23, s98, v23
v_perm_b32 v24, v22, v255, s96
v_perm_b32 v25, v22, v255, s97
v_perm_b32 v26, v23, v248, s96
v_perm_b32 v27, v23, v248, s97
v_mfma_f32_16x16x32_bf16 v[40:43], v[28:31], v[68:71], v[40:43]
v_alignbit_b32 v23, v249, v249, 4
v_and_b32 v22, s98, v249
v_and_b32 v23, s98, v23
v_perm_b32 v28, v22, v250, s96
v_perm_b32 v29, v22, v250, s97
v_perm_b32 v30, v23, v251, s96
v_perm_b32 v31, v23, v251, s97
v_mfma_f32_16x16x32_bf16 v[44:47], v[24:27], v[68:71], v[44:47]
s_nop 0
v_mfma_f32_16x16x32_bf16 v[48:51], v[28:31], v[68:71], v[48:51]
v_mov_b32 v124, 0
v_mov_b32 v125, 0
v_mov_b32 v126, 0
v_mov_b32_e64 v127, 0
v_accvgpr_read_b32 v22, a77
v_accvgpr_read_b32 v25, a78
v_accvgpr_read_b32 v27, a79
v_cndmask_b32 v36, v36, v38, s[94:95]
v_cndmask_b32 v37, v37, v39, s[94:95]
v_cndmask_b32 v40, v40, v42, s[94:95]
v_cndmask_b32 v41, v41, v43, s[94:95]
v_cndmask_b32 v44, v44, v46, s[94:95]
v_cndmask_b32 v45, v45, v47, s[94:95]
v_cndmask_b32 v48, v48, v50, s[94:95]
v_cndmask_b32 v49, v49, v51, s[94:95]
v_cndmask_b32 v38, v40, v37, s[88:89]
v_cndmask_b32 v39, v36, v41, s[88:89]
v_cndmask_b32 v46, v48, v45, s[88:89]
v_cndmask_b32 v47, v44, v49, s[88:89]
v_add_f32_dpp v39, v38, v39 quad_perm:[1,0,3,2] row_mask:0xf bank_mask:0xf
v_add_f32_dpp v47, v46, v47 quad_perm:[1,0,3,2] row_mask:0xf bank_mask:0xf
v_cndmask_b32 v40, v47, v39, s[94:95]
v_cndmask_b32 v41, v39, v47, s[94:95]
v_add_f32_e64 v41, v41, v10
v_add_f32_dpp v10, v40, v41 quad_perm:[2,3,0,1] row_mask:0xf bank_mask:0xf
v_cvt_pk_bf16_f32 v10, 0, v10
v_mul_f32 v10, 0x3db504f3, v10
v_cvt_pk_bf16_f32 v10, 0, v10
v_mul_f32 v10, 0x3fb8aa3b, v10
v_max3_f32 v4, v4, v9, v10
v_alignbit_b32 v23, v22, v22, 4
v_and_b32_e64 v22, s98, v22
v_max_f32_dpp v4, v4, v4 quad_perm:[1,0,3,2] row_mask:0xf bank_mask:0xf
v_and_b32_e64 v23, s98, v23
v_perm_b32 v24, v22, v25, s96
v_max_f32_dpp v4, v4, v4 quad_perm:[2,3,0,1] row_mask:0xf bank_mask:0xf
)" R"(
v_perm_b32 v25, v22, v25, s97
v_perm_b32 v26, v23, v27, s96
v_max_f32_dpp v4, v4, v4 row_half_mirror row_mask:0xf bank_mask:0xf
v_perm_b32 v27, v23, v27, s97
v_accvgpr_read_b32 v22, a204
v_max_f32_dpp v4, v4, v4 row_ror:8 row_mask:0xf bank_mask:0xf
v_accvgpr_read_b32 v29, a205
v_accvgpr_read_b32 v31, a206
v_max_f32_dpp v4, v4, v4 row_bcast:15 row_mask:0xa bank_mask:0xf
v_alignbit_b32 v23, v22, v22, 4
v_and_b32_e64 v22, s98, v22
v_max_f32_dpp v4, v4, v4 row_bcast:31 row_mask:0xc bank_mask:0xf
v_and_b32_e64 v23, s98, v23
v_readlane_b32 s87, v4, 63
v_perm_b32 v28, v22, v29, s96
v_perm_b32 v29, v22, v29, s97
v_perm_b32 v30, v23, v31, s96
v_perm_b32 v31, v23, v31, s97
v_mov_b32 v120, 0
v_mov_b32 v121, 0
v_mov_b32 v122, 0
v_mov_b32 v123, 0
v_and_b32_e64 v55, 16, v7
v_lshlrev_b32 v55, v55, s99
ds_write_b128 v52, v[124:127]
buffer_load_dwordx4 a[240:243], v7, %[rv], s10 offen offset:3072 nt
buffer_load_dwordx4 a[244:247], v7, %[rv], s10 offen offset:2048 nt
v_subrev_f32 v8, s87, v8
v_subrev_f32 v9, s87, v9
v_subrev_f32 v10, s87, v10
v_subrev_f32 v11, s87, v11
v_subrev_f32 v12, s87, v12
v_subrev_f32 v13, s87, v13
v_subrev_f32 v14, s87, v14
v_subrev_f32 v15, s87, v15
v_exp_f32 v8, v8
v_exp_f32 v9, v9
v_exp_f32 v10, v10
v_exp_f32 v11, v11
v_exp_f32 v12, v12
v_exp_f32 v13, v13
v_exp_f32 v14, v14
v_exp_f32 v15, v15
ds_write_b128 v53, v[8:11] offset:0
ds_write_b128 v53, v[12:15] offset:1024
v_pk_add_f32 v[0:1], v[8:9], v[10:11]
v_pk_add_f32 v[2:3], v[12:13], v[14:15]
v_pk_add_f32 v[0:1], v[0:1], v[2:3]
v_add_f32_e64 v4, v0, v1
v_mul_f32_dpp v9, v9, v54 quad_perm:[0,1,2,3] row_mask:0xf bank_mask:0x8
v_mul_f32 v10, v10, v54
v_mul_f32 v11, v11, v54
v_mul_f32_dpp v12, v12, v54 quad_perm:[0,1,2,3] row_mask:0xf bank_mask:0x1
v_mul_f32_dpp v14, v14, v54 quad_perm:[0,1,2,3] row_mask:0xf bank_mask:0x8
v_mul_f32_e64 v0, v15, v54
v_cvt_pk_bf16_f32 v8, v8, v8
v_cvt_pk_bf16_f32 v9, v9, v9
v_cvt_pk_bf16_f32 v10, v10, v10
v_cvt_pk_bf16_f32 v11, v11, v11
v_cvt_pk_bf16_f32 v12, v12, v12
v_cvt_pk_bf16_f32 v13, v13, v13
v_cvt_pk_bf16_f32 v14, v14, v14
v_cvt_pk_bf16_f32 v15, v15, v15
v_cvt_pk_bf16_f32 v0, v0, v0
v_and_b32_dpp v36, v8, v55 quad_perm:[0,0,1,1] row_mask:0xf bank_mask:0xf
v_and_b32_dpp v37, v8, v55 quad_perm:[2,2,3,3] row_mask:0xf bank_mask:0xf
v_and_b32_dpp v38, v9, v55 quad_perm:[0,0,1,1] row_mask:0xf bank_mask:0xf
v_and_b32_dpp v39, v9, v55 quad_perm:[2,2,3,3] row_mask:0xf bank_mask:0xf
v_and_b32_dpp v40, v10, v55 quad_perm:[0,0,1,1] row_mask:0xf bank_mask:0xf
v_and_b32_dpp v41, v10, v55 quad_perm:[2,2,3,3] row_mask:0xf bank_mask:0xf
v_and_b32_dpp v42, v11, v55 quad_perm:[0,0,1,1] row_mask:0xf bank_mask:0xf
v_and_b32_dpp v43, v11, v55 quad_perm:[2,2,3,3] row_mask:0xf bank_mask:0xf
v_and_b32_dpp v44, v12, v55 quad_perm:[0,0,1,1] row_mask:0xf bank_mask:0xf
v_and_b32_dpp v45, v12, v55 quad_perm:[2,2,3,3] row_mask:0xf bank_mask:0xf
v_and_b32_dpp v46, v13, v55 quad_perm:[0,0,1,1] row_mask:0xf bank_mask:0xf
v_and_b32_dpp v47, v13, v55 quad_perm:[2,2,3,3] row_mask:0xf bank_mask:0xf
v_and_b32_dpp v48, v14, v55 quad_perm:[0,0,1,1] row_mask:0xf bank_mask:0xf
v_and_b32_dpp v49, v14, v55 quad_perm:[2,2,3,3] row_mask:0xf bank_mask:0xf
v_and_b32_dpp v50, v15, v55 quad_perm:[0,0,1,1] row_mask:0xf bank_mask:0xc
v_and_b32_dpp v50, v0, v55 quad_perm:[0,0,1,1] row_mask:0xf bank_mask:0x3
v_and_b32_dpp v51, v15, v55 quad_perm:[2,2,3,3] row_mask:0xf bank_mask:0xe
v_and_b32_dpp v51, v0, v55 quad_perm:[2,2,3,3] row_mask:0xf bank_mask:0x1
v_lshrrev_b32 v54, 2, v7
v_add_u32 v54, s38, v54
v_mov_b32_dpp v120, v50 row_ror:8 row_mask:0xf bank_mask:0x1
v_mov_b32_dpp v121, v50 row_ror:12 row_mask:0xf bank_mask:0x2
v_mov_b32_dpp v122, v50 quad_perm:[0,1,2,3] row_mask:0xf bank_mask:0x4
v_mov_b32_dpp v123, v50 row_ror:4 row_mask:0xf bank_mask:0x8
v_mov_b32_dpp v124, v50 row_ror:4 row_mask:0xf bank_mask:0x1
v_mov_b32_dpp v125, v50 row_ror:8 row_mask:0xf bank_mask:0x2
v_mfma_f32_16x16x32_bf16 v[0:3], v[188:191], v[120:123], 0
v_mov_b32_dpp v126, v50 row_ror:12 row_mask:0xf bank_mask:0x4
v_mov_b32_dpp v127, v50 quad_perm:[0,1,2,3] row_mask:0xf bank_mask:0x8
v_add_f32_dpp v10, v4, v4 quad_perm:[1,0,3,2] row_mask:0xf bank_mask:0xf
v_mov_b32_e64 v8, s87
buffer_load_dwordx4 a[248:251], v7, %[rv], s19 offen offset:3072 nt
buffer_load_dword v64, v54, s[48:51], 0 offen offset:0
v_mfma_f32_16x16x32_bf16 v[116:119], v[244:247], v[120:123], 0
v_mov_b32_dpp v120, v51 row_ror:4 row_mask:0xf bank_mask:0x1
v_mov_b32_dpp v121, v51 row_ror:8 row_mask:0xf bank_mask:0x2
v_add_f32_dpp v10, v10, v10 quad_perm:[2,3,0,1] row_mask:0xf bank_mask:0xf
v_mov_b32_e64 v9, s41
buffer_load_dwordx4 a[252:255], v7, %[rv], s19 offen offset:2048 nt
v_mfma_f32_16x16x32_bf16 v[0:3], v[168:171], v[124:127], v[0:3]
v_mov_b32_dpp v122, v51 row_ror:12 row_mask:0xf bank_mask:0x4
v_mov_b32_dpp v123, v51 quad_perm:[0,1,2,3] row_mask:0xf bank_mask:0x8
v_add_f32_dpp v10, v10, v10 row_half_mirror row_mask:0xf bank_mask:0xf
v_mov_b32_e64 v11, s41
v_mfma_f32_16x16x32_bf16 v[116:119], v[104:107], v[124:127], v[116:119]
v_mov_b32_dpp v124, v38 row_ror:4 row_mask:0xf bank_mask:0x1
v_mov_b32_dpp v125, v38 row_ror:8 row_mask:0xf bank_mask:0x2
v_add_f32_dpp v10, v10, v10 row_ror:8 row_mask:0xf bank_mask:0xf
v_mov_b32_e64 v12, s41
v_mfma_f32_16x16x32_bf16 v[0:3], v[108:111], v[120:123], v[0:3]
v_mov_b32_dpp v126, v38 row_ror:12 row_mask:0xf bank_mask:0x4
v_mov_b32_dpp v127, v38 quad_perm:[0,1,2,3] row_mask:0xf bank_mask:0x8
v_add_f32_dpp v10, v10, v10 row_bcast:15 row_mask:0xa bank_mask:0xf
v_mov_b32_e64 v13, s90
v_mfma_f32_16x16x32_bf16 v[116:119], v[112:115], v[120:123], v[116:119]
v_mov_b32_dpp v120, v39 row_ror:4 row_mask:0xf bank_mask:0x1
v_mov_b32_dpp v121, v39 row_ror:8 row_mask:0xf bank_mask:0x2
v_add_f32_dpp v10, v10, v10 row_bcast:31 row_mask:0xc bank_mask:0xf
s_add_u32 s74, s39, s40
buffer_load_dword v65, v54, s[48:51], 0 offen offset:1024
v_mfma_f32_16x16x32_bf16 v[0:3], v[24:27], v[124:127], v[0:3]
v_mov_b32_dpp v122, v39 row_ror:12 row_mask:0xf bank_mask:0x4
v_mov_b32_dpp v123, v39 quad_perm:[0,1,2,3] row_mask:0xf bank_mask:0x8
s_brev_b64 exec, 1
buffer_store_dwordx4 v[8:11], off, %[rw], s74 sc1
buffer_store_dwordx2 v[12:13], off, %[rw], s42 sc1
s_mov_b64 exec, -1
s_waitcnt vmcnt(40)
buffer_load_dwordx4 a[76:79], v7, %[rv], s10 offen offset:1024 nt
v_accvgpr_read_b32 v22, a207
v_accvgpr_read_b32 v25, a80
v_accvgpr_read_b32 v27, a81
v_alignbit_b32 v23, v22, v22, 4
v_and_b32 v22, s98, v22
v_and_b32 v23, s98, v23
v_perm_b32 v24, v22, v25, s96
v_perm_b32 v25, v22, v25, s97
v_perm_b32 v26, v23, v27, s96
v_perm_b32 v27, v23, v27, s97
v_mfma_f32_16x16x32_bf16 v[116:119], v[28:31], v[124:127], v[116:119]
v_mov_b32_dpp v124, v40 quad_perm:[0,1,2,3] row_mask:0xf bank_mask:0x1
v_mov_b32_dpp v125, v40 row_ror:4 row_mask:0xf bank_mask:0x2
v_accvgpr_read_b32 v22, a82
v_accvgpr_read_b32 v29, a83
v_accvgpr_read_b32 v31, a84
v_alignbit_b32 v23, v22, v22, 4
v_and_b32 v22, s98, v22
v_and_b32 v23, s98, v23
v_perm_b32 v28, v22, v29, s96
v_perm_b32 v29, v22, v29, s97
v_perm_b32 v30, v23, v31, s96
v_perm_b32 v31, v23, v31, s97
v_mfma_f32_16x16x32_bf16 v[0:3], v[24:27], v[120:123], v[0:3]
v_mov_b32_dpp v126, v40 row_ror:8 row_mask:0xf bank_mask:0x4
v_mov_b32_dpp v127, v40 row_ror:12 row_mask:0xf bank_mask:0x8
ds_read_b32 v10, v20
s_cmp_gt_u32 s78, 48
s_cbranch_scc1 Lslowv
Lslowvr:
v_accvgpr_read_b32 v22, a85
v_accvgpr_read_b32 v25, a86
v_accvgpr_read_b32 v27, a87
v_alignbit_b32 v23, v22, v22, 4
v_and_b32 v22, s98, v22
v_and_b32 v23, s98, v23
v_perm_b32 v24, v22, v25, s96
v_perm_b32 v25, v22, v25, s97
v_perm_b32 v26, v23, v27, s96
v_perm_b32 v27, v23, v27, s97
v_mfma_f32_16x16x32_bf16 v[116:119], v[28:31], v[120:123], v[116:119]
v_mov_b32_dpp v120, v41 quad_perm:[0,1,2,3] row_mask:0xf bank_mask:0x1
v_mov_b32_dpp v121, v41 row_ror:4 row_mask:0xf bank_mask:0x2
buffer_load_dword v66, v54, s[48:51], 0 offen offset:2048
v_accvgpr_read_b32 v22, a208
v_accvgpr_read_b32 v29, a209
v_accvgpr_read_b32 v31, a210
v_alignbit_b32 v23, v22, v22, 4
v_and_b32 v22, s98, v22
v_and_b32 v23, s98, v23
v_perm_b32 v28, v22, v29, s96
v_perm_b32 v29, v22, v29, s97
v_perm_b32 v30, v23, v31, s96
v_perm_b32 v31, v23, v31, s97
v_mfma_f32_16x16x32_bf16 v[0:3], v[24:27], v[124:127], v[0:3]
v_mov_b32_dpp v122, v41 row_ror:8 row_mask:0xf bank_mask:0x4
v_mov_b32_dpp v123, v41 row_ror:12 row_mask:0xf bank_mask:0x8
buffer_load_dwordx4 a[204:207], v7, %[rv], s10 offen offset:0 nt
v_accvgpr_read_b32 v22, a211
v_accvgpr_read_b32 v25, a88
v_accvgpr_read_b32 v27, a89
v_alignbit_b32 v23, v22, v22, 4
v_and_b32 v22, s98, v22
v_and_b32 v23, s98, v23
v_perm_b32 v24, v22, v25, s96
v_perm_b32 v25, v22, v25, s97
v_perm_b32 v26, v23, v27, s96
v_perm_b32 v27, v23, v27, s97
v_mfma_f32_16x16x32_bf16 v[116:119], v[28:31], v[124:127], v[116:119]
v_mov_b32_dpp v124, v40 row_ror:12 row_mask:0xf bank_mask:0x1
v_mov_b32_dpp v125, v40 quad_perm:[0,1,2,3] row_mask:0xf bank_mask:0x2
s_waitcnt lgkmcnt(0)
s_nop 0
v_cvt_pk_bf16_f32 v9, 0, v10
v_accvgpr_read_b32 v22, a90
v_accvgpr_read_b32 v29, a91
v_accvgpr_read_b32 v31, a96
v_alignbit_b32 v23, v22, v22, 4
v_and_b32 v22, s98, v22
v_and_b32 v23, s98, v23
v_perm_b32 v28, v22, v29, s96
v_perm_b32 v29, v22, v29, s97
v_perm_b32 v30, v23, v31, s96
v_perm_b32 v31, v23, v31, s97
v_mfma_f32_16x16x32_bf16 v[0:3], v[24:27], v[120:123], v[0:3]
v_mov_b32_dpp v126, v40 row_ror:4 row_mask:0xf bank_mask:0x4
v_mov_b32_dpp v127, v40 row_ror:8 row_mask:0xf bank_mask:0x8
v_cvt_pk_bf16_f32 v10, v10, 0
v_dot2_f32_bf16 v12, v16, v9, 0
v_accvgpr_read_b32 v22, a97
v_accvgpr_read_b32 v25, a98
v_accvgpr_read_b32 v27, a99
v_alignbit_b32 v23, v22, v22, 4
v_and_b32 v22, s98, v22
v_and_b32 v23, s98, v23
v_perm_b32 v24, v22, v25, s96
v_perm_b32 v25, v22, v25, s97
v_perm_b32 v26, v23, v27, s96
v_perm_b32 v27, v23, v27, s97
v_mfma_f32_16x16x32_bf16 v[116:119], v[28:31], v[120:123], v[116:119]
v_mov_b32_dpp v120, v41 row_ror:12 row_mask:0xf bank_mask:0x1
v_mov_b32_dpp v121, v41 quad_perm:[0,1,2,3] row_mask:0xf bank_mask:0x2
v_dot2_f32_bf16 v16, v16, v10, 0
v_dot2_f32_bf16 v13, v17, v9, 0
buffer_load_dword v67, v54, s[48:51], 0 offen offset:3072
buffer_load_dwordx4 a[80:83], v7, %[rv], s19 offen offset:1024 nt
v_accvgpr_read_b32 v22, a104
v_accvgpr_read_b32 v29, a105
v_accvgpr_read_b32 v31, a106
v_alignbit_b32 v23, v22, v22, 4
v_and_b32 v22, s98, v22
v_and_b32 v23, s98, v23
v_perm_b32 v28, v22, v29, s96
v_perm_b32 v29, v22, v29, s97
v_perm_b32 v30, v23, v31, s96
v_perm_b32 v31, v23, v31, s97
v_mfma_f32_16x16x32_bf16 v[0:3], v[24:27], v[124:127], v[0:3]
v_mov_b32_dpp v122, v41 row_ror:4 row_mask:0xf bank_mask:0x4
v_mov_b32_dpp v123, v41 row_ror:8 row_mask:0xf bank_mask:0x8
v_dot2_f32_bf16 v17, v17, v10, 0
v_dot2_f32_bf16 v14, v18, v9, 0
buffer_load_dword v68, v54, s[48:51], s8 offen offset:0
v_accvgpr_read_b32 v22, a107
v_accvgpr_read_b32 v25, a116
v_accvgpr_read_b32 v27, a117
v_alignbit_b32 v23, v22, v22, 4
v_and_b32 v22, s98, v22
v_and_b32 v23, s98, v23
v_perm_b32 v24, v22, v25, s96
v_perm_b32 v25, v22, v25, s97
v_perm_b32 v26, v23, v27, s96
v_perm_b32 v27, v23, v27, s97
v_mfma_f32_16x16x32_bf16 v[116:119], v[28:31], v[124:127], v[116:119]
v_mov_b32_dpp v124, v40 row_ror:8 row_mask:0xf bank_mask:0x1
v_mov_b32_dpp v125, v40 row_ror:12 row_mask:0xf bank_mask:0x2
v_dot2_f32_bf16 v18, v18, v10, 0
v_dot2_f32_bf16 v15, v19, v9, 0
buffer_load_dwordx4 a[84:87], v7, %[rv], s19 offen offset:0 nt
v_accvgpr_read_b32 v22, a118
v_accvgpr_read_b32 v29, a119
v_accvgpr_read_b32 v31, a124
v_alignbit_b32 v23, v22, v22, 4
v_and_b32 v22, s98, v22
v_and_b32 v23, s98, v23
v_perm_b32 v28, v22, v29, s96
v_perm_b32 v29, v22, v29, s97
v_perm_b32 v30, v23, v31, s96
v_perm_b32 v31, v23, v31, s97
v_mfma_f32_16x16x32_bf16 v[0:3], v[24:27], v[120:123], v[0:3]
v_mov_b32_dpp v126, v40 quad_perm:[0,1,2,3] row_mask:0xf bank_mask:0x4
v_mov_b32_dpp v127, v40 row_ror:4 row_mask:0xf bank_mask:0x8
v_dot2_f32_bf16 v19, v19, v10, 0
buffer_load_dword v69, v54, s[48:51], s8 offen offset:1024
v_accvgpr_read_b32 v22, a125
v_accvgpr_read_b32 v25, a126
v_accvgpr_read_b32 v27, a127
v_alignbit_b32 v23, v22, v22, 4
)" R"(
v_and_b32 v22, s98, v22
v_and_b32 v23, s98, v23
v_perm_b32 v24, v22, v25, s96
v_perm_b32 v25, v22, v25, s97
v_perm_b32 v26, v23, v27, s96
v_perm_b32 v27, v23, v27, s97
v_mfma_f32_16x16x32_bf16 v[116:119], v[28:31], v[120:123], v[116:119]
v_mov_b32_dpp v120, v41 row_ror:8 row_mask:0xf bank_mask:0x1
v_mov_b32_dpp v121, v41 row_ror:12 row_mask:0xf bank_mask:0x2
s_mov_b64 exec, s[76:77]
s_nop 0
ds_add_f32 v21, v16 offset:0
ds_add_f32 v21, v12 offset:16
ds_add_f32 v21, v17 offset:32
ds_add_f32 v21, v13 offset:48
ds_add_f32 v21, v18 offset:64
ds_add_f32 v21, v14 offset:80
ds_add_f32 v21, v19 offset:96
ds_add_f32 v21, v15 offset:112
s_mov_b64 exec, -1
s_nop 0
ds_read_b128 v[16:19], v52
buffer_load_dwordx4 a[208:211], v7, %[rv], s9 offen offset:3072 nt
v_accvgpr_read_b32 v22, a144
v_accvgpr_read_b32 v29, a145
v_accvgpr_read_b32 v31, a146
v_alignbit_b32 v23, v22, v22, 4
v_and_b32 v22, s98, v22
v_and_b32 v23, s98, v23
v_perm_b32 v28, v22, v29, s96
v_perm_b32 v29, v22, v29, s97
v_perm_b32 v30, v23, v31, s96
v_perm_b32 v31, v23, v31, s97
v_mfma_f32_16x16x32_bf16 v[0:3], v[24:27], v[124:127], v[0:3]
v_mov_b32_dpp v122, v41 quad_perm:[0,1,2,3] row_mask:0xf bank_mask:0x4
v_mov_b32_dpp v123, v41 row_ror:4 row_mask:0xf bank_mask:0x8
v_and_b32 v15, 0x1f0, v7
s_sub_u32 s74, s43, 159744
v_accvgpr_read_b32 v22, a147
v_accvgpr_read_b32 v25, a160
v_accvgpr_read_b32 v27, a161
v_alignbit_b32 v23, v22, v22, 4
v_and_b32 v22, s98, v22
v_and_b32 v23, s98, v23
v_perm_b32 v24, v22, v25, s96
v_perm_b32 v25, v22, v25, s97
v_perm_b32 v26, v23, v27, s96
v_perm_b32 v27, v23, v27, s97
v_mfma_f32_16x16x32_bf16 v[116:119], v[28:31], v[124:127], v[116:119]
v_mov_b32_dpp v124, v40 row_ror:4 row_mask:0xf bank_mask:0x1
v_mov_b32_dpp v125, v40 row_ror:8 row_mask:0xf bank_mask:0x2
s_lshr_b32 s74, s74, 3
s_nop 0
s_add_u32 s74, s74, 159744
buffer_load_dword v70, v54, s[48:51], s8 offen offset:2048
buffer_load_dwordx4 a[88:91], v7, %[rv], s9 offen offset:2048 nt
v_accvgpr_read_b32 v22, a162
v_accvgpr_read_b32 v29, a163
v_alignbit_b32 v23, v22, v22, 4
v_and_b32 v22, s98, v22
v_and_b32 v23, s98, v23
v_perm_b32 v28, v22, v29, s96
v_perm_b32 v29, v22, v29, s97
v_perm_b32 v30, v23, v145, s96
v_perm_b32 v31, v23, v145, s97
v_mfma_f32_16x16x32_bf16 v[0:3], v[24:27], v[120:123], v[0:3]
v_mov_b32_dpp v126, v40 row_ror:12 row_mask:0xf bank_mask:0x4
v_mov_b32_dpp v127, v40 quad_perm:[0,1,2,3] row_mask:0xf bank_mask:0x8
v_add_u32_e64 v14, s74, v7
v_alignbit_b32 v23, v146, v146, 4
v_and_b32 v22, s98, v146
v_and_b32 v23, s98, v23
v_perm_b32 v24, v22, v147, s96
v_perm_b32 v25, v22, v147, s97
v_perm_b32 v26, v23, v152, s96
v_perm_b32 v27, v23, v152, s97
v_mfma_f32_16x16x32_bf16 v[116:119], v[28:31], v[120:123], v[116:119]
v_mov_b32_dpp v120, v41 row_ror:4 row_mask:0xf bank_mask:0x1
v_mov_b32_dpp v121, v41 row_ror:8 row_mask:0xf bank_mask:0x2
v_alignbit_b32 v23, v153, v153, 4
v_and_b32 v22, s98, v153
v_and_b32 v23, s98, v23
v_perm_b32 v28, v22, v154, s96
v_perm_b32 v29, v22, v154, s97
v_perm_b32 v30, v23, v155, s96
v_perm_b32 v31, v23, v155, s97
v_mfma_f32_16x16x32_bf16 v[0:3], v[24:27], v[124:127], v[0:3]
v_mov_b32_dpp v122, v41 row_ror:12 row_mask:0xf bank_mask:0x4
v_mov_b32_dpp v123, v41 quad_perm:[0,1,2,3] row_mask:0xf bank_mask:0x8
v_alignbit_b32 v23, v164, v164, 4
v_and_b32 v22, s98, v164
v_and_b32 v23, s98, v23
v_perm_b32 v24, v22, v165, s96
v_perm_b32 v25, v22, v165, s97
v_perm_b32 v26, v23, v166, s96
v_perm_b32 v27, v23, v166, s97
v_mfma_f32_16x16x32_bf16 v[116:119], v[28:31], v[124:127], v[116:119]
v_mov_b32_dpp v124, v42 quad_perm:[0,1,2,3] row_mask:0xf bank_mask:0x1
v_mov_b32_dpp v125, v42 row_ror:4 row_mask:0xf bank_mask:0x2
buffer_load_dword v71, v54, s[48:51], s8 offen offset:3072
v_alignbit_b32 v23, v167, v167, 4
v_and_b32 v22, s98, v167
v_and_b32 v23, s98, v23
v_perm_b32 v28, v22, v172, s96
v_perm_b32 v29, v22, v172, s97
v_perm_b32 v30, v23, v173, s96
v_perm_b32 v31, v23, v173, s97
v_mfma_f32_16x16x32_bf16 v[0:3], v[24:27], v[120:123], v[0:3]
v_mov_b32_dpp v126, v42 row_ror:8 row_mask:0xf bank_mask:0x4
v_mov_b32_dpp v127, v42 row_ror:12 row_mask:0xf bank_mask:0x8
buffer_load_dwordx4 a[96:99], v7, %[rv], s18 offen offset:3072 nt
v_alignbit_b32 v23, v174, v174, 4
v_and_b32 v22, s98, v174
v_and_b32 v23, s98, v23
v_perm_b32 v24, v22, v175, s96
v_perm_b32 v25, v22, v175, s97
v_perm_b32 v26, v23, v180, s96
v_perm_b32 v27, v23, v180, s97
v_mfma_f32_16x16x32_bf16 v[116:119], v[28:31], v[120:123], v[116:119]
v_mov_b32_dpp v120, v43 quad_perm:[0,1,2,3] row_mask:0xf bank_mask:0x1
v_mov_b32_dpp v121, v43 row_ror:4 row_mask:0xf bank_mask:0x2
v_alignbit_b32 v23, v181, v181, 4
v_and_b32 v22, s98, v181
v_and_b32 v23, s98, v23
v_perm_b32 v28, v22, v182, s96
v_perm_b32 v29, v22, v182, s97
v_perm_b32 v30, v23, v183, s96
v_perm_b32 v31, v23, v183, s97
v_mfma_f32_16x16x32_bf16 v[0:3], v[24:27], v[124:127], v[0:3]
v_mov_b32_dpp v122, v43 row_ror:8 row_mask:0xf bank_mask:0x4
v_mov_b32_dpp v123, v43 row_ror:12 row_mask:0xf bank_mask:0x8
buffer_load_dwordx4 a[104:107], v7, %[rv], s18 offen offset:2048 nt
v_alignbit_b32 v23, v192, v192, 4
v_and_b32 v22, s98, v192
v_and_b32 v23, s98, v23
v_perm_b32 v24, v22, v193, s96
v_perm_b32 v25, v22, v193, s97
v_perm_b32 v26, v23, v194, s96
v_perm_b32 v27, v23, v194, s97
v_mfma_f32_16x16x32_bf16 v[116:119], v[28:31], v[124:127], v[116:119]
v_mov_b32_dpp v124, v42 row_ror:12 row_mask:0xf bank_mask:0x1
v_mov_b32_dpp v125, v42 quad_perm:[0,1,2,3] row_mask:0xf bank_mask:0x2
v_alignbit_b32 v23, v195, v195, 4
v_and_b32 v22, s98, v195
v_and_b32 v23, s98, v23
v_perm_b32 v28, v22, v200, s96
v_perm_b32 v29, v22, v200, s97
v_perm_b32 v30, v23, v201, s96
v_perm_b32 v31, v23, v201, s97
v_mfma_f32_16x16x32_bf16 v[0:3], v[24:27], v[120:123], v[0:3]
v_mov_b32_dpp v126, v42 row_ror:4 row_mask:0xf bank_mask:0x4
v_mov_b32_dpp v127, v42 row_ror:8 row_mask:0xf bank_mask:0x8
buffer_load_dwordx4 a[116:119], v7, %[rv], s9 offen offset:1024 nt
v_alignbit_b32 v23, v202, v202, 4
v_and_b32 v22, s98, v202
v_and_b32 v23, s98, v23
v_perm_b32 v24, v22, v203, s96
v_perm_b32 v25, v22, v203, s97
v_perm_b32 v26, v23, v208, s96
v_perm_b32 v27, v23, v208, s97
v_mfma_f32_16x16x32_bf16 v[116:119], v[28:31], v[120:123], v[116:119]
v_mov_b32_dpp v120, v43 row_ror:12 row_mask:0xf bank_mask:0x1
v_mov_b32_dpp v121, v43 quad_perm:[0,1,2,3] row_mask:0xf bank_mask:0x2
v_alignbit_b32 v23, v209, v209, 4
v_and_b32 v22, s98, v209
v_and_b32 v23, s98, v23
v_perm_b32 v28, v22, v210, s96
v_perm_b32 v29, v22, v210, s97
v_perm_b32 v30, v23, v211, s96
v_perm_b32 v31, v23, v211, s97
v_mfma_f32_16x16x32_bf16 v[0:3], v[24:27], v[124:127], v[0:3]
v_mov_b32_dpp v122, v43 row_ror:4 row_mask:0xf bank_mask:0x4
v_mov_b32_dpp v123, v43 row_ror:8 row_mask:0xf bank_mask:0x8
buffer_load_dwordx4 v[8:11], v15, %[rw], s39 offen sc1
buffer_load_dwordx2 v[12:13], v15, %[rw], 0 offen offset:2560 sc1
v_alignbit_b32 v23, v212, v212, 4
v_and_b32 v22, s98, v212
v_and_b32 v23, s98, v23
v_perm_b32 v24, v22, v213, s96
v_perm_b32 v25, v22, v213, s97
v_perm_b32 v26, v23, v214, s96
v_perm_b32 v27, v23, v214, s97
v_mfma_f32_16x16x32_bf16 v[116:119], v[28:31], v[124:127], v[116:119]
v_mov_b32_dpp v124, v42 row_ror:8 row_mask:0xf bank_mask:0x1
v_mov_b32_dpp v125, v42 row_ror:12 row_mask:0xf bank_mask:0x2
v_alignbit_b32 v23, v215, v215, 4
v_and_b32 v22, s98, v215
v_and_b32 v23, s98, v23
v_perm_b32 v28, v22, v216, s96
v_perm_b32 v29, v22, v216, s97
v_perm_b32 v30, v23, v217, s96
v_perm_b32 v31, v23, v217, s97
v_mfma_f32_16x16x32_bf16 v[0:3], v[24:27], v[120:123], v[0:3]
v_mov_b32_dpp v126, v42 quad_perm:[0,1,2,3] row_mask:0xf bank_mask:0x4
v_mov_b32_dpp v127, v42 row_ror:4 row_mask:0xf bank_mask:0x8
s_waitcnt vmcnt(40)
buffer_load_dwordx4 a[124:127], v7, %[rv], s9 offen offset:0 nt
v_alignbit_b32 v23, v218, v218, 4
v_and_b32 v22, s98, v218
v_and_b32_e64 v23, s98, v23
v_perm_b32 v24, v22, v219, s96
v_perm_b32 v25, v22, v219, s97
v_perm_b32 v26, v23, v220, s96
v_perm_b32 v27, v23, v220, s97
v_mfma_f32_16x16x32_bf16 v[116:119], v[28:31], v[120:123], v[116:119]
v_mov_b32_dpp v120, v43 row_ror:8 row_mask:0xf bank_mask:0x1
v_mov_b32_dpp v121, v43 row_ror:12 row_mask:0xf bank_mask:0x2
v_alignbit_b32 v23, v221, v221, 4
v_and_b32 v22, s98, v221
v_and_b32 v23, s98, v23
v_perm_b32 v28, v22, v222, s96
v_perm_b32 v29, v22, v222, s97
v_perm_b32 v30, v23, v223, s96
v_perm_b32 v31, v23, v223, s97
v_mfma_f32_16x16x32_bf16 v[0:3], v[24:27], v[124:127], v[0:3]
v_mov_b32_dpp v122, v43 quad_perm:[0,1,2,3] row_mask:0xf bank_mask:0x4
v_mov_b32_dpp v123, v43 row_ror:4 row_mask:0xf bank_mask:0x8
v_alignbit_b32 v23, v232, v232, 4
v_and_b32 v22, s98, v232
v_and_b32 v23, s98, v23
v_perm_b32 v24, v22, v233, s96
v_perm_b32 v25, v22, v233, s97
v_perm_b32 v26, v23, v234, s96
v_perm_b32 v27, v23, v234, s97
v_mfma_f32_16x16x32_bf16 v[116:119], v[28:31], v[124:127], v[116:119]
v_mov_b32_dpp v124, v42 row_ror:4 row_mask:0xf bank_mask:0x1
v_mov_b32_dpp v125, v42 row_ror:8 row_mask:0xf bank_mask:0x2
v_alignbit_b32 v23, v235, v235, 4
v_and_b32 v22, s98, v235
v_and_b32 v23, s98, v23
v_perm_b32 v28, v22, v240, s96
v_perm_b32 v29, v22, v240, s97
v_perm_b32 v30, v23, v241, s96
v_perm_b32 v31, v23, v241, s97
v_mfma_f32_16x16x32_bf16 v[0:3], v[24:27], v[120:123], v[0:3]
v_mov_b32_dpp v126, v42 row_ror:12 row_mask:0xf bank_mask:0x4
v_mov_b32_dpp v127, v42 quad_perm:[0,1,2,3] row_mask:0xf bank_mask:0x8
buffer_load_dwordx4 a[144:147], v7, %[rv], s18 offen offset:1024 nt
v_alignbit_b32 v23, v242, v242, 4
v_and_b32 v22, s98, v242
v_and_b32 v23, s98, v23
v_perm_b32 v24, v22, v243, s96
v_perm_b32 v25, v22, v243, s97
v_perm_b32 v26, v23, v132, s96
v_perm_b32 v27, v23, v132, s97
v_mfma_f32_16x16x32_bf16 v[116:119], v[28:31], v[120:123], v[116:119]
v_mov_b32_dpp v120, v43 row_ror:4 row_mask:0xf bank_mask:0x1
v_mov_b32_dpp v121, v43 row_ror:8 row_mask:0xf bank_mask:0x2
v_alignbit_b32 v23, v133, v133, 4
v_and_b32 v22, s98, v133
v_and_b32 v23, s98, v23
v_perm_b32 v28, v22, v134, s96
v_perm_b32 v29, v22, v134, s97
v_perm_b32 v30, v23, v135, s96
v_perm_b32 v31, v23, v135, s97
v_mfma_f32_16x16x32_bf16 v[0:3], v[24:27], v[124:127], v[0:3]
v_mov_b32_dpp v122, v43 row_ror:12 row_mask:0xf bank_mask:0x4
v_mov_b32_dpp v123, v43 quad_perm:[0,1,2,3] row_mask:0xf bank_mask:0x8
buffer_load_dwordx4 a[160:163], v7, %[rv], s18 offen offset:0 nt
v_alignbit_b32 v23, v156, v156, 4
v_and_b32 v22, s98, v156
v_and_b32 v23, s98, v23
v_perm_b32 v24, v22, v157, s96
v_perm_b32 v25, v22, v157, s97
v_perm_b32 v26, v23, v158, s96
v_perm_b32 v27, v23, v158, s97
v_mfma_f32_16x16x32_bf16 v[116:119], v[28:31], v[124:127], v[116:119]
v_mov_b32_dpp v124, v44 quad_perm:[0,1,2,3] row_mask:0xf bank_mask:0x1
v_mov_b32_dpp v125, v44 row_ror:4 row_mask:0xf bank_mask:0x2
v_alignbit_b32 v23, v159, v159, 4
v_and_b32 v22, s98, v159
v_and_b32 v23, s98, v23
v_perm_b32 v28, v22, v160, s96
v_perm_b32 v29, v22, v160, s97
v_perm_b32 v30, v23, v161, s96
v_perm_b32 v31, v23, v161, s97
v_mfma_f32_16x16x32_bf16 v[0:3], v[24:27], v[120:123], v[0:3]
v_mov_b32_dpp v126, v44 row_ror:8 row_mask:0xf bank_mask:0x4
v_mov_b32_dpp v127, v44 row_ror:12 row_mask:0xf bank_mask:0x8
buffer_load_dwordx4 v[128:131], v7, %[rv], s8 offen offset:3072 nt
v_alignbit_b32 v23, v162, v162, 4
v_and_b32 v22, s98, v162
v_and_b32 v23, s98, v23
v_perm_b32 v24, v22, v163, s96
v_perm_b32 v25, v22, v163, s97
v_perm_b32 v26, v23, v176, s96
v_perm_b32 v27, v23, v176, s97
v_mfma_f32_16x16x32_bf16 v[116:119], v[28:31], v[120:123], v[116:119]
v_mov_b32_dpp v120, v45 quad_perm:[0,1,2,3] row_mask:0xf bank_mask:0x1
v_mov_b32_dpp v121, v45 row_ror:4 row_mask:0xf bank_mask:0x2
v_alignbit_b32 v23, v177, v177, 4
v_and_b32 v22, s98, v177
v_and_b32 v23, s98, v23
v_perm_b32 v28, v22, v178, s96
v_perm_b32 v29, v22, v178, s97
v_perm_b32 v30, v23, v179, s96
v_perm_b32 v31, v23, v179, s97
v_mfma_f32_16x16x32_bf16 v[0:3], v[24:27], v[124:127], v[0:3]
v_mov_b32_dpp v122, v45 row_ror:8 row_mask:0xf bank_mask:0x4
v_mov_b32_dpp v123, v45 row_ror:12 row_mask:0xf bank_mask:0x8
v_alignbit_b32 v23, v196, v196, 4
v_and_b32 v22, s98, v196
v_and_b32 v23, s98, v23
v_perm_b32 v24, v22, v197, s96
)" R"(
v_perm_b32 v25, v22, v197, s97
v_perm_b32 v26, v23, v198, s96
v_perm_b32 v27, v23, v198, s97
v_mfma_f32_16x16x32_bf16 v[116:119], v[28:31], v[124:127], v[116:119]
v_mov_b32_dpp v124, v48 row_ror:4 row_mask:0xf bank_mask:0x1
v_mov_b32_dpp v125, v48 row_ror:8 row_mask:0xf bank_mask:0x2
v_alignbit_b32 v23, v199, v199, 4
v_and_b32 v22, s98, v199
v_and_b32 v23, s98, v23
v_perm_b32 v28, v22, v204, s96
v_perm_b32 v29, v22, v204, s97
v_perm_b32 v30, v23, v205, s96
v_perm_b32 v31, v23, v205, s97
v_mfma_f32_16x16x32_bf16 v[0:3], v[24:27], v[120:123], v[0:3]
v_mov_b32_dpp v126, v48 row_ror:12 row_mask:0xf bank_mask:0x4
v_mov_b32_dpp v127, v48 quad_perm:[0,1,2,3] row_mask:0xf bank_mask:0x8
buffer_load_dwordx4 v[252:255], v7, %[rv], s8 offen offset:2048 nt
v_alignbit_b32 v23, v206, v206, 4
v_and_b32 v22, s98, v206
v_and_b32 v23, s98, v23
v_perm_b32 v24, v22, v207, s96
v_perm_b32 v25, v22, v207, s97
v_perm_b32 v26, v23, v184, s96
v_perm_b32 v27, v23, v184, s97
v_mfma_f32_16x16x32_bf16 v[116:119], v[28:31], v[120:123], v[116:119]
v_mov_b32_dpp v120, v49 row_ror:4 row_mask:0xf bank_mask:0x1
v_mov_b32_dpp v121, v49 row_ror:8 row_mask:0xf bank_mask:0x2
v_alignbit_b32 v23, v185, v185, 4
v_and_b32 v22, s98, v185
v_and_b32 v23, s98, v23
v_perm_b32 v28, v22, v186, s96
v_perm_b32 v29, v22, v186, s97
v_perm_b32 v30, v23, v187, s96
v_perm_b32 v31, v23, v187, s97
v_mfma_f32_16x16x32_bf16 v[0:3], v[24:27], v[124:127], v[0:3]
v_mov_b32_dpp v122, v49 row_ror:12 row_mask:0xf bank_mask:0x4
v_mov_b32_dpp v123, v49 quad_perm:[0,1,2,3] row_mask:0xf bank_mask:0x8
v_alignbit_b32 v23, v224, v224, 4
v_and_b32 v22, s98, v224
v_and_b32 v23, s98, v23
v_perm_b32 v24, v22, v225, s96
v_perm_b32 v25, v22, v225, s97
v_perm_b32 v26, v23, v226, s96
v_perm_b32 v27, v23, v226, s97
v_mfma_f32_16x16x32_bf16 v[116:119], v[28:31], v[124:127], v[116:119]
v_mov_b32_dpp v124, v50 quad_perm:[0,1,2,3] row_mask:0xf bank_mask:0x1
v_mov_b32_dpp v125, v50 row_ror:4 row_mask:0xf bank_mask:0x2
v_alignbit_b32 v23, v227, v227, 4
v_and_b32 v22, s98, v227
v_and_b32 v23, s98, v23
v_perm_b32 v28, v22, v228, s96
v_perm_b32 v29, v22, v228, s97
v_perm_b32 v30, v23, v229, s96
v_perm_b32 v31, v23, v229, s97
v_mfma_f32_16x16x32_bf16 v[0:3], v[24:27], v[120:123], v[0:3]
v_mov_b32_dpp v126, v50 row_ror:8 row_mask:0xf bank_mask:0x4
v_mov_b32_dpp v127, v50 row_ror:12 row_mask:0xf bank_mask:0x8
buffer_load_dwordx4 v[248:251], v7, %[rv], s8 offen offset:1024 nt
v_alignbit_b32 v23, v230, v230, 4
v_and_b32 v22, s98, v230
v_and_b32 v23, s98, v23
v_perm_b32 v24, v22, v231, s96
v_perm_b32 v25, v22, v231, s97
v_perm_b32 v26, v23, v236, s96
v_perm_b32 v27, v23, v236, s97
v_mfma_f32_16x16x32_bf16 v[116:119], v[28:31], v[120:123], v[116:119]
v_mov_b32_dpp v120, v51 quad_perm:[0,1,2,3] row_mask:0xf bank_mask:0x1
v_mov_b32_dpp v121, v51 row_ror:4 row_mask:0xf bank_mask:0x2
v_alignbit_b32 v23, v237, v237, 4
v_and_b32 v22, s98, v237
v_and_b32 v23, s98, v23
v_perm_b32 v28, v22, v238, s96
v_perm_b32 v29, v22, v238, s97
v_perm_b32 v30, v23, v239, s96
v_perm_b32 v31, v23, v239, s97
v_mfma_f32_16x16x32_bf16 v[0:3], v[24:27], v[124:127], v[0:3]
v_mov_b32_dpp v122, v51 row_ror:8 row_mask:0xf bank_mask:0x4
v_mov_b32_dpp v123, v51 row_ror:12 row_mask:0xf bank_mask:0x8
v_alignbit_b32 v23, v136, v136, 4
v_and_b32 v22, s98, v136
v_and_b32 v23, s98, v23
v_perm_b32 v24, v22, v137, s96
v_perm_b32 v25, v22, v137, s97
v_perm_b32 v26, v23, v138, s96
v_perm_b32 v27, v23, v138, s97
v_mfma_f32_16x16x32_bf16 v[116:119], v[28:31], v[124:127], v[116:119]
v_mov_b32_dpp v124, v50 row_ror:12 row_mask:0xf bank_mask:0x1
v_mov_b32_dpp v125, v50 quad_perm:[0,1,2,3] row_mask:0xf bank_mask:0x2
v_alignbit_b32 v23, v139, v139, 4
v_and_b32 v22, s98, v139
v_and_b32 v23, s98, v23
v_perm_b32 v28, v22, v140, s96
v_perm_b32 v29, v22, v140, s97
v_perm_b32 v30, v23, v141, s96
v_perm_b32 v31, v23, v141, s97
v_mfma_f32_16x16x32_bf16 v[0:3], v[24:27], v[120:123], v[0:3]
v_mov_b32_dpp v126, v50 row_ror:4 row_mask:0xf bank_mask:0x4
v_mov_b32_dpp v127, v50 row_ror:8 row_mask:0xf bank_mask:0x8
buffer_load_dwordx4 v[188:191], v7, %[rv], s8 offen offset:0 nt
v_alignbit_b32 v23, v142, v142, 4
v_and_b32 v22, s98, v142
v_and_b32 v23, s98, v23
v_perm_b32 v24, v22, v143, s96
v_perm_b32 v25, v22, v143, s97
v_perm_b32 v26, v23, v148, s96
v_perm_b32 v27, v23, v148, s97
v_mfma_f32_16x16x32_bf16 v[116:119], v[28:31], v[120:123], v[116:119]
v_mov_b32_dpp v120, v47 quad_perm:[0,1,2,3] row_mask:0xf bank_mask:0x1
v_mov_b32_dpp v121, v47 row_ror:4 row_mask:0xf bank_mask:0x2
v_alignbit_b32 v23, v149, v149, 4
v_and_b32 v22, s98, v149
v_and_b32 v23, s98, v23
v_perm_b32 v28, v22, v150, s96
v_perm_b32 v29, v22, v150, s97
v_perm_b32 v30, v23, v151, s96
v_perm_b32 v31, v23, v151, s97
v_mfma_f32_16x16x32_bf16 v[0:3], v[24:27], v[124:127], v[0:3]
v_mov_b32_dpp v122, v47 row_ror:8 row_mask:0xf bank_mask:0x4
v_mov_b32_dpp v123, v47 row_ror:12 row_mask:0xf bank_mask:0x8
v_mfma_f32_16x16x32_bf16 v[116:119], v[28:31], v[124:127], v[116:119]
v_mov_b32_dpp v124, v46 quad_perm:[0,1,2,3] row_mask:0xf bank_mask:0x1
v_mov_b32_dpp v125, v46 row_ror:4 row_mask:0xf bank_mask:0x2
buffer_load_dwordx4 v[244:247], v7, %[rv], 0 offen offset:3072 nt
buffer_load_dwordx4 v[168:171], v7, %[rv], 0 offen offset:2048 nt
buffer_load_dwordx4 v[104:107], v7, %[rv], 0 offen offset:1024 nt
buffer_load_dwordx4 v[108:111], v7, %[rv], 0 offen offset:0 nt
v_mfma_f32_16x16x32_bf16 v[116:119], a[0:3], v[120:123], v[116:119]
v_mov_b32_dpp v126, v46 row_ror:8 row_mask:0xf bank_mask:0x4
v_mov_b32_dpp v127, v46 row_ror:12 row_mask:0xf bank_mask:0x8
v_mfma_f32_16x16x32_bf16 v[0:3], a[4:7], v[120:123], v[0:3]
v_mov_b32_dpp v120, v51 row_ror:8 row_mask:0xf bank_mask:0x1
v_mov_b32_dpp v121, v51 row_ror:12 row_mask:0xf bank_mask:0x2
v_mfma_f32_16x16x32_bf16 v[116:119], a[8:11], v[124:127], v[116:119]
v_mov_b32_dpp v122, v51 quad_perm:[0,1,2,3] row_mask:0xf bank_mask:0x4
v_mov_b32_dpp v123, v51 row_ror:4 row_mask:0xf bank_mask:0x8
v_mfma_f32_16x16x32_bf16 v[0:3], a[12:15], v[124:127], v[0:3]
v_mov_b32_dpp v124, v45 row_ror:4 row_mask:0xf bank_mask:0x1
v_mov_b32_dpp v125, v45 row_ror:8 row_mask:0xf bank_mask:0x2
v_mfma_f32_16x16x32_bf16 v[116:119], a[16:19], v[120:123], v[116:119]
v_mov_b32_dpp v126, v45 row_ror:12 row_mask:0xf bank_mask:0x4
v_mov_b32_dpp v127, v45 quad_perm:[0,1,2,3] row_mask:0xf bank_mask:0x8
v_mfma_f32_16x16x32_bf16 v[0:3], a[20:23], v[120:123], v[0:3]
v_mov_b32_dpp v120, v51 row_ror:12 row_mask:0xf bank_mask:0x1
v_mov_b32_dpp v121, v51 quad_perm:[0,1,2,3] row_mask:0xf bank_mask:0x2
v_mfma_f32_16x16x32_bf16 v[116:119], a[24:27], v[124:127], v[116:119]
v_mov_b32_dpp v122, v51 row_ror:4 row_mask:0xf bank_mask:0x4
v_mov_b32_dpp v123, v51 row_ror:8 row_mask:0xf bank_mask:0x8
v_mfma_f32_16x16x32_bf16 v[0:3], a[28:31], v[124:127], v[0:3]
v_mov_b32_dpp v124, v44 row_ror:4 row_mask:0xf bank_mask:0x1
v_mov_b32_dpp v125, v44 row_ror:8 row_mask:0xf bank_mask:0x2
v_mfma_f32_16x16x32_bf16 v[116:119], a[32:35], v[120:123], v[116:119]
v_mov_b32_dpp v126, v44 row_ror:12 row_mask:0xf bank_mask:0x4
v_mov_b32_dpp v127, v44 quad_perm:[0,1,2,3] row_mask:0xf bank_mask:0x8
v_mfma_f32_16x16x32_bf16 v[0:3], a[36:39], v[120:123], v[0:3]
v_mov_b32_dpp v120, v49 row_ror:8 row_mask:0xf bank_mask:0x1
v_mov_b32_dpp v121, v49 row_ror:12 row_mask:0xf bank_mask:0x2
v_mfma_f32_16x16x32_bf16 v[116:119], a[40:43], v[124:127], v[116:119]
v_mov_b32_dpp v122, v49 quad_perm:[0,1,2,3] row_mask:0xf bank_mask:0x4
v_mov_b32_dpp v123, v49 row_ror:4 row_mask:0xf bank_mask:0x8
v_mfma_f32_16x16x32_bf16 v[0:3], a[44:47], v[124:127], v[0:3]
v_mov_b32_dpp v124, v45 row_ror:8 row_mask:0xf bank_mask:0x1
v_mov_b32_dpp v125, v45 row_ror:12 row_mask:0xf bank_mask:0x2
v_mfma_f32_16x16x32_bf16 v[116:119], a[48:51], v[120:123], v[116:119]
v_mov_b32_dpp v126, v45 quad_perm:[0,1,2,3] row_mask:0xf bank_mask:0x4
v_mov_b32_dpp v127, v45 row_ror:4 row_mask:0xf bank_mask:0x8
v_mfma_f32_16x16x32_bf16 v[0:3], a[56:59], v[120:123], v[0:3]
v_mov_b32_dpp v120, v44 row_ror:8 row_mask:0xf bank_mask:0x1
v_mov_b32_dpp v121, v44 row_ror:12 row_mask:0xf bank_mask:0x2
v_mfma_f32_16x16x32_bf16 v[116:119], a[60:63], v[124:127], v[116:119]
v_mov_b32_dpp v122, v44 quad_perm:[0,1,2,3] row_mask:0xf bank_mask:0x4
v_mov_b32_dpp v123, v44 row_ror:4 row_mask:0xf bank_mask:0x8
v_mfma_f32_16x16x32_bf16 v[0:3], a[68:71], v[124:127], v[0:3]
v_mov_b32_dpp v124, v48 row_ror:8 row_mask:0xf bank_mask:0x1
v_mov_b32_dpp v125, v48 row_ror:12 row_mask:0xf bank_mask:0x2
v_mfma_f32_16x16x32_bf16 v[116:119], a[112:115], v[120:123], v[116:119]
v_mov_b32_dpp v126, v48 quad_perm:[0,1,2,3] row_mask:0xf bank_mask:0x4
v_mov_b32_dpp v127, v48 row_ror:4 row_mask:0xf bank_mask:0x8
v_mfma_f32_16x16x32_bf16 v[0:3], a[120:123], v[120:123], v[0:3]
v_mov_b32_dpp v120, v45 row_ror:12 row_mask:0xf bank_mask:0x1
v_mov_b32_dpp v121, v45 quad_perm:[0,1,2,3] row_mask:0xf bank_mask:0x2
v_mfma_f32_16x16x32_bf16 v[116:119], a[128:131], v[124:127], v[116:119]
v_mov_b32_dpp v122, v45 row_ror:4 row_mask:0xf bank_mask:0x4
v_mov_b32_dpp v123, v45 row_ror:8 row_mask:0xf bank_mask:0x8
v_mfma_f32_16x16x32_bf16 v[0:3], a[140:143], v[124:127], v[0:3]
v_mov_b32_dpp v124, v49 row_ror:12 row_mask:0xf bank_mask:0x1
v_mov_b32_dpp v125, v49 quad_perm:[0,1,2,3] row_mask:0xf bank_mask:0x2
v_mfma_f32_16x16x32_bf16 v[116:119], a[148:151], v[120:123], v[116:119]
v_mov_b32_dpp v126, v49 row_ror:4 row_mask:0xf bank_mask:0x4
v_mov_b32_dpp v127, v49 row_ror:8 row_mask:0xf bank_mask:0x8
v_mfma_f32_16x16x32_bf16 v[0:3], a[156:159], v[120:123], v[0:3]
v_mov_b32_dpp v120, v44 row_ror:12 row_mask:0xf bank_mask:0x1
v_mov_b32_dpp v121, v44 quad_perm:[0,1,2,3] row_mask:0xf bank_mask:0x2
v_mfma_f32_16x16x32_bf16 v[116:119], a[168:171], v[124:127], v[116:119]
v_mov_b32_dpp v122, v44 row_ror:4 row_mask:0xf bank_mask:0x4
v_mov_b32_dpp v123, v44 row_ror:8 row_mask:0xf bank_mask:0x8
v_mfma_f32_16x16x32_bf16 v[0:3], a[176:179], v[124:127], v[0:3]
v_mov_b32_dpp v124, v48 row_ror:12 row_mask:0xf bank_mask:0x1
v_mov_b32_dpp v125, v48 quad_perm:[0,1,2,3] row_mask:0xf bank_mask:0x2
v_mfma_f32_16x16x32_bf16 v[116:119], a[180:183], v[120:123], v[116:119]
v_mov_b32_dpp v126, v48 row_ror:4 row_mask:0xf bank_mask:0x4
v_mov_b32_dpp v127, v48 row_ror:8 row_mask:0xf bank_mask:0x8
v_mfma_f32_16x16x32_bf16 v[0:3], a[184:187], v[120:123], v[0:3]
v_mov_b32_dpp v120, v39 row_ror:8 row_mask:0xf bank_mask:0x1
v_mov_b32_dpp v121, v39 row_ror:12 row_mask:0xf bank_mask:0x2
v_mfma_f32_16x16x32_bf16 v[116:119], a[192:195], v[124:127], v[116:119]
v_mov_b32_dpp v122, v39 quad_perm:[0,1,2,3] row_mask:0xf bank_mask:0x4
v_mov_b32_dpp v123, v39 row_ror:4 row_mask:0xf bank_mask:0x8
v_mfma_f32_16x16x32_bf16 v[0:3], a[200:203], v[124:127], v[0:3]
v_mov_b32_dpp v124, v49 quad_perm:[0,1,2,3] row_mask:0xf bank_mask:0x1
v_mov_b32_dpp v125, v49 row_ror:4 row_mask:0xf bank_mask:0x2
v_mfma_f32_16x16x32_bf16 v[116:119], a[92:95], v[120:123], v[116:119]
v_mov_b32_dpp v126, v49 row_ror:8 row_mask:0xf bank_mask:0x4
v_mov_b32_dpp v127, v49 row_ror:12 row_mask:0xf bank_mask:0x8
v_mfma_f32_16x16x32_bf16 v[0:3], a[100:103], v[120:123], v[0:3]
v_mov_b32_dpp v120, v38 row_ror:8 row_mask:0xf bank_mask:0x1
v_mov_b32_dpp v121, v38 row_ror:12 row_mask:0xf bank_mask:0x2
v_mfma_f32_16x16x32_bf16 v[116:119], a[108:111], v[124:127], v[116:119]
v_mov_b32_dpp v122, v38 quad_perm:[0,1,2,3] row_mask:0xf bank_mask:0x4
v_mov_b32_dpp v123, v38 row_ror:4 row_mask:0xf bank_mask:0x8
v_mfma_f32_16x16x32_bf16 v[0:3], a[132:135], v[124:127], v[0:3]
v_mov_b32_dpp v124, v39 row_ror:12 row_mask:0xf bank_mask:0x1
v_mov_b32_dpp v125, v39 quad_perm:[0,1,2,3] row_mask:0xf bank_mask:0x2
v_mfma_f32_16x16x32_bf16 v[116:119], a[136:139], v[120:123], v[116:119]
v_mov_b32_dpp v126, v39 row_ror:4 row_mask:0xf bank_mask:0x4
v_mov_b32_dpp v127, v39 row_ror:8 row_mask:0xf bank_mask:0x8
s_waitcnt vmcnt(47)
s_nop 0
v_mfma_f32_16x16x32_bf16 v[0:3], a[152:155], v[120:123], v[0:3]
v_mov_b32_dpp v120, v48 quad_perm:[0,1,2,3] row_mask:0xf bank_mask:0x1
v_mov_b32_dpp v121, v48 row_ror:4 row_mask:0xf bank_mask:0x2
v_mfma_f32_16x16x32_bf16 v[116:119], a[164:167], v[124:127], v[116:119]
)" R"(
v_mov_b32_dpp v122, v48 row_ror:8 row_mask:0xf bank_mask:0x4
v_mov_b32_dpp v123, v48 row_ror:12 row_mask:0xf bank_mask:0x8
v_mfma_f32_16x16x32_bf16 v[0:3], a[172:175], v[124:127], v[0:3]
v_mov_b32_dpp v124, v38 row_ror:12 row_mask:0xf bank_mask:0x1
v_mov_b32_dpp v125, v38 quad_perm:[0,1,2,3] row_mask:0xf bank_mask:0x2
v_mfma_f32_16x16x32_bf16 v[116:119], a[52:55], v[120:123], v[116:119]
v_mov_b32_dpp v126, v38 row_ror:4 row_mask:0xf bank_mask:0x4
v_mov_b32_dpp v127, v38 row_ror:8 row_mask:0xf bank_mask:0x8
s_waitcnt vmcnt(43)
s_nop 0
v_mfma_f32_16x16x32_bf16 v[0:3], a[64:67], v[120:123], v[0:3]
v_mov_b32_dpp v120, v47 row_ror:4 row_mask:0xf bank_mask:0x1
v_mov_b32_dpp v121, v47 row_ror:8 row_mask:0xf bank_mask:0x2
v_mfma_f32_16x16x32_bf16 v[116:119], a[188:191], v[124:127], v[116:119]
v_mov_b32_dpp v122, v47 row_ror:12 row_mask:0xf bank_mask:0x4
v_mov_b32_dpp v123, v47 quad_perm:[0,1,2,3] row_mask:0xf bank_mask:0x8
v_mfma_f32_16x16x32_bf16 v[0:3], a[72:75], v[124:127], v[0:3]
v_mov_b32_dpp v124, v39 quad_perm:[0,1,2,3] row_mask:0xf bank_mask:0x1
v_mov_b32_dpp v125, v39 row_ror:4 row_mask:0xf bank_mask:0x2
v_mfma_f32_16x16x32_bf16 v[116:119], a[196:199], v[120:123], v[116:119]
v_mov_b32_dpp v126, v39 row_ror:8 row_mask:0xf bank_mask:0x4
v_mov_b32_dpp v127, v39 row_ror:12 row_mask:0xf bank_mask:0x8
s_waitcnt vmcnt(39)
s_nop 0
v_mfma_f32_16x16x32_bf16 v[0:3], a[212:215], v[120:123], v[0:3]
v_mov_b32_dpp v120, v46 row_ror:4 row_mask:0xf bank_mask:0x1
v_mov_b32_dpp v121, v46 row_ror:8 row_mask:0xf bank_mask:0x2
v_mfma_f32_16x16x32_bf16 v[116:119], a[216:219], v[124:127], v[116:119]
v_mov_b32_dpp v122, v46 row_ror:12 row_mask:0xf bank_mask:0x4
v_mov_b32_dpp v123, v46 quad_perm:[0,1,2,3] row_mask:0xf bank_mask:0x8
v_mfma_f32_16x16x32_bf16 v[0:3], a[220:223], v[124:127], v[0:3]
v_mov_b32_dpp v124, v38 quad_perm:[0,1,2,3] row_mask:0xf bank_mask:0x1
v_mov_b32_dpp v125, v38 row_ror:4 row_mask:0xf bank_mask:0x2
v_mfma_f32_16x16x32_bf16 v[116:119], a[224:227], v[120:123], v[116:119]
v_mov_b32_dpp v126, v38 row_ror:8 row_mask:0xf bank_mask:0x4
v_mov_b32_dpp v127, v38 row_ror:12 row_mask:0xf bank_mask:0x8
s_waitcnt vmcnt(12)
v_mfma_f32_16x16x32_bf16 v[0:3], a[228:231], v[120:123], v[0:3]
v_mov_b32_dpp v120, v37 row_ror:4 row_mask:0xf bank_mask:0x1
v_mov_b32_dpp v121, v37 row_ror:8 row_mask:0xf bank_mask:0x2
v_cmp_ne_u32 vcc, s41, v9
v_cmp_ne_u32 s[88:89], s41, v11
s_or_b64 s[88:89], s[88:89], vcc
s_cbranch_scc1 Lml
Lmlr:
v_max_f32_dpp v20, v8, v8 quad_perm:[1,0,3,2] row_mask:0xf bank_mask:0xf
s_mov_b32 s85, 0x05040100
s_mov_b32 s86, 0x07060302
v_perm_b32 v56, v65, v64, s85
v_perm_b32 v60, v65, v64, s86
v_perm_b32 v57, v67, v66, s85
v_perm_b32 v61, v67, v66, s86
v_perm_b32 v58, v69, v68, s85
v_perm_b32 v62, v69, v68, s86
v_perm_b32 v59, v71, v70, s85
v_perm_b32 v63, v71, v70, s86
v_mfma_f32_16x16x32_bf16 v[116:119], a[232:235], v[124:127], v[116:119]
v_mov_b32_dpp v122, v37 row_ror:12 row_mask:0xf bank_mask:0x4
v_mov_b32_dpp v123, v37 quad_perm:[0,1,2,3] row_mask:0xf bank_mask:0x8
v_mfma_f32_16x16x32_bf16 v[0:3], a[236:239], v[124:127], v[0:3]
v_mov_b32_dpp v124, v47 row_ror:8 row_mask:0xf bank_mask:0x1
v_mov_b32_dpp v125, v47 row_ror:12 row_mask:0xf bank_mask:0x2
v_max_f32_dpp v20, v20, v20 quad_perm:[2,3,0,1] row_mask:0xf bank_mask:0xf
v_mfma_f32_16x16x32_bf16 v[116:119], a[240:243], v[120:123], v[116:119]
v_mov_b32_dpp v126, v47 quad_perm:[0,1,2,3] row_mask:0xf bank_mask:0x4
v_mov_b32_dpp v127, v47 row_ror:4 row_mask:0xf bank_mask:0x8
v_max_f32_dpp v20, v20, v20 row_half_mirror row_mask:0xf bank_mask:0xf
v_mfma_f32_16x16x32_bf16 v[0:3], a[244:247], v[120:123], v[0:3]
v_mov_b32_dpp v120, v36 row_ror:4 row_mask:0xf bank_mask:0x1
v_mov_b32_dpp v121, v36 row_ror:8 row_mask:0xf bank_mask:0x2
v_max_f32_dpp v20, v20, v20 row_ror:8 row_mask:0xf bank_mask:0xf
v_mfma_f32_16x16x32_bf16 v[116:119], a[248:251], v[124:127], v[116:119]
v_mov_b32_dpp v122, v36 row_ror:12 row_mask:0xf bank_mask:0x4
v_mov_b32_dpp v123, v36 quad_perm:[0,1,2,3] row_mask:0xf bank_mask:0x8
v_max_f32_dpp v20, v20, v20 row_bcast:15 row_mask:0xa bank_mask:0xf
v_mfma_f32_16x16x32_bf16 v[0:3], a[252:255], v[124:127], v[0:3]
v_mov_b32_dpp v124, v46 row_ror:8 row_mask:0xf bank_mask:0x1
v_mov_b32_dpp v125, v46 row_ror:12 row_mask:0xf bank_mask:0x2
v_readlane_b32 s74, v20, 31
s_lshr_b32 s75, s40, 4
s_nop 0
v_mfma_f32_16x16x32_bf16 v[116:119], a[76:79], v[120:123], v[116:119]
v_mov_b32_dpp v126, v46 quad_perm:[0,1,2,3] row_mask:0xf bank_mask:0x4
v_mov_b32_dpp v127, v46 row_ror:4 row_mask:0xf bank_mask:0x8
v_mfma_f32_16x16x32_bf16 v[0:3], a[204:207], v[120:123], v[0:3]
v_mov_b32_dpp v120, v37 row_ror:8 row_mask:0xf bank_mask:0x1
v_mov_b32_dpp v121, v37 row_ror:12 row_mask:0xf bank_mask:0x2
v_subrev_f32 v20, s74, v8
v_exp_f32 v20, v20
v_mfma_f32_16x16x32_bf16 v[116:119], a[80:83], v[124:127], v[116:119]
v_mov_b32_dpp v122, v37 quad_perm:[0,1,2,3] row_mask:0xf bank_mask:0x4
v_mov_b32_dpp v123, v37 row_ror:4 row_mask:0xf bank_mask:0x8
v_mfma_f32_16x16x32_bf16 v[0:3], a[84:87], v[124:127], v[0:3]
v_mov_b32_dpp v124, v47 row_ror:12 row_mask:0xf bank_mask:0x1
v_mov_b32_dpp v125, v47 quad_perm:[0,1,2,3] row_mask:0xf bank_mask:0x2
v_mul_f32_e64 v21, v20, v10
v_readlane_b32 s74, v20, s75
v_mfma_f32_16x16x32_bf16 v[116:119], a[208:211], v[120:123], v[116:119]
v_mov_b32_dpp v126, v47 row_ror:4 row_mask:0xf bank_mask:0x4
v_mov_b32_dpp v127, v47 row_ror:8 row_mask:0xf bank_mask:0x8
v_add_f32_dpp v21, v21, v21 quad_perm:[1,0,3,2] row_mask:0xf bank_mask:0xf
v_mfma_f32_16x16x32_bf16 v[0:3], a[88:91], v[120:123], v[0:3]
v_mov_b32_dpp v120, v36 row_ror:8 row_mask:0xf bank_mask:0x1
v_mov_b32_dpp v121, v36 row_ror:12 row_mask:0xf bank_mask:0x2
v_add_f32_dpp v21, v21, v21 quad_perm:[2,3,0,1] row_mask:0xf bank_mask:0xf
v_mfma_f32_16x16x32_bf16 v[116:119], a[96:99], v[124:127], v[116:119]
v_mov_b32_dpp v122, v36 quad_perm:[0,1,2,3] row_mask:0xf bank_mask:0x4
v_mov_b32_dpp v123, v36 row_ror:4 row_mask:0xf bank_mask:0x8
v_add_f32_dpp v21, v21, v21 row_half_mirror row_mask:0xf bank_mask:0xf
v_mfma_f32_16x16x32_bf16 v[0:3], a[104:107], v[124:127], v[0:3]
v_mov_b32_dpp v124, v46 row_ror:12 row_mask:0xf bank_mask:0x1
v_mov_b32_dpp v125, v46 quad_perm:[0,1,2,3] row_mask:0xf bank_mask:0x2
v_add_f32_dpp v21, v21, v21 row_ror:8 row_mask:0xf bank_mask:0xf
v_mfma_f32_16x16x32_bf16 v[116:119], a[116:119], v[120:123], v[116:119]
v_mov_b32_dpp v126, v46 row_ror:4 row_mask:0xf bank_mask:0x4
v_mov_b32_dpp v127, v46 row_ror:8 row_mask:0xf bank_mask:0x8
v_add_f32_dpp v21, v21, v21 row_bcast:15 row_mask:0xa bank_mask:0xf
s_waitcnt vmcnt(8)
v_mfma_f32_16x16x32_bf16 v[0:3], a[124:127], v[120:123], v[0:3]
v_mov_b32_dpp v120, v37 row_ror:12 row_mask:0xf bank_mask:0x1
v_mov_b32_dpp v121, v37 quad_perm:[0,1,2,3] row_mask:0xf bank_mask:0x2
v_rcp_f32 v21, v21
v_mfma_f32_16x16x32_bf16 v[116:119], a[144:147], v[124:127], v[116:119]
v_mov_b32_dpp v122, v37 row_ror:4 row_mask:0xf bank_mask:0x4
v_mov_b32_dpp v123, v37 row_ror:8 row_mask:0xf bank_mask:0x8
v_mul_f32_e64 v21, s74, v21
v_mfma_f32_16x16x32_bf16 v[0:3], a[160:163], v[124:127], v[0:3]
v_mov_b32_dpp v124, v36 row_ror:12 row_mask:0xf bank_mask:0x1
v_mov_b32_dpp v125, v36 quad_perm:[0,1,2,3] row_mask:0xf bank_mask:0x2
v_readlane_b32 s74, v21, 31
s_waitcnt vmcnt(7)
v_mfma_f32_16x16x32_bf16 v[116:119], v[128:131], v[120:123], v[116:119]
v_mov_b32_dpp v126, v36 row_ror:4 row_mask:0xf bank_mask:0x4
v_mov_b32_dpp v127, v36 row_ror:8 row_mask:0xf bank_mask:0x8
v_mov_b32 v4, s74
s_waitcnt vmcnt(6)
v_mfma_f32_16x16x32_bf16 v[0:3], v[252:255], v[120:123], v[0:3]
v_mov_b32_dpp v120, v37 quad_perm:[0,1,2,3] row_mask:0xf bank_mask:0x1
v_mov_b32_dpp v121, v37 row_ror:4 row_mask:0xf bank_mask:0x2
s_waitcnt vmcnt(5)
v_mfma_f32_16x16x32_bf16 v[116:119], v[248:251], v[124:127], v[116:119]
v_mov_b32_dpp v122, v37 row_ror:8 row_mask:0xf bank_mask:0x4
v_mov_b32_dpp v123, v37 row_ror:12 row_mask:0xf bank_mask:0x8
s_waitcnt vmcnt(4)
v_mfma_f32_16x16x32_bf16 v[0:3], v[188:191], v[124:127], v[0:3]
v_mov_b32_dpp v124, v36 quad_perm:[0,1,2,3] row_mask:0xf bank_mask:0x1
v_mov_b32_dpp v125, v36 row_ror:4 row_mask:0xf bank_mask:0x2
s_waitcnt vmcnt(3)
v_mfma_f32_16x16x32_bf16 v[116:119], v[244:247], v[120:123], v[116:119]
v_mov_b32_dpp v126, v36 row_ror:8 row_mask:0xf bank_mask:0x4
v_mov_b32_dpp v127, v36 row_ror:12 row_mask:0xf bank_mask:0x8
s_waitcnt vmcnt(2)
v_mfma_f32_16x16x32_bf16 v[0:3], v[168:171], v[120:123], v[0:3]
s_waitcnt vmcnt(1)
v_mfma_f32_16x16x32_bf16 v[116:119], v[104:107], v[124:127], v[116:119]
s_waitcnt vmcnt(0)
v_mfma_f32_16x16x32_bf16 v[0:3], v[108:111], v[124:127], v[0:3]
s_waitcnt lgkmcnt(0)
s_nop 4
v_add_f32 v116, v116, v16
v_add_f32 v117, v117, v17
v_add_f32 v118, v118, v18
v_add_f32_e64 v119, v119, v19
v_add_f32_dpp v0, v116, v0 quad_perm:[2,3,2,3] row_mask:0xf bank_mask:0xf
v_add_f32_dpp v1, v117, v1 quad_perm:[2,3,2,3] row_mask:0xf bank_mask:0xf
v_add_f32_dpp v2, v118, v2 quad_perm:[2,3,2,3] row_mask:0xf bank_mask:0xf
v_add_f32_dpp v3, v119, v3 quad_perm:[2,3,2,3] row_mask:0xf bank_mask:0xf
s_mov_b32 exec_lo, 0x33333333
s_mov_b32 exec_hi, 0x33333333
v_pk_mul_f32 v[0:1], v[0:1], v[4:5] op_sel_hi:[1,0]
v_pk_mul_f32 v[2:3], v[2:3], v[4:5] op_sel_hi:[1,0]
ds_write_b128 v52, v[0:3]
s_mov_b64 exec, 0xff
s_waitcnt lgkmcnt(0)
s_barrier
ds_read_b128 v[36:39], v14 offset:0
ds_read_b128 v[40:43], v14 offset:1024
ds_read_b128 v[44:47], v14 offset:2048
ds_read_b128 v[48:51], v14 offset:3072
s_waitcnt lgkmcnt(2)
v_pk_add_f32 v[36:37], v[36:37], v[40:41]
v_pk_add_f32 v[38:39], v[38:39], v[42:43]
s_waitcnt lgkmcnt(0)
v_pk_add_f32 v[44:45], v[44:45], v[48:49]
v_pk_add_f32 v[46:47], v[46:47], v[50:51]
v_pk_add_f32 v[36:37], v[36:37], v[44:45]
v_pk_add_f32 v[38:39], v[38:39], v[46:47]
v_min_u32 v52, -2, v36
v_min_u32 v53, -2, v37
v_min_u32 v54, -2, v38
v_min_u32 v55, -2, v39
buffer_store_dwordx4 v[52:55], v7, %[rw], s68 offen sc1
s_mov_b64 exec, -1
s_add_u32 s87, s69, 0
s_add_u32 s88, s69, 4096
s_movk_i32 s84, 31
s_branch LtR2_%=p
LtR2_%=r:
s_mov_b64 exec, vcc
s_sub_u32 s84, s84, 1
s_cbranch_scc1 LtP4_%=
buffer_load_dwordx4 v[36:39], v32, %[rw], s87 offen offset:0 sc1
buffer_load_dwordx4 v[40:43], v32, %[rw], s87 offen offset:2048 sc1
buffer_load_dwordx4 v[44:47], v32, %[rw], s88 offen offset:0 sc1
buffer_load_dwordx4 v[48:51], v32, %[rw], s88 offen offset:2048 sc1
s_branch LtR2_%=w
LtR2_%=p:
v_subrev_u32_e64 v16, s90, v13
v_cmp_eq_u32 s[94:95], s41, v12
v_max_i32 v16, -800, v16
v_min_i32 v16, 800, v16
v_cndmask_b32 v16, 0, v16, s[94:95]
s_nop 1
v_add_u32_dpp v16, v16, v16 quad_perm:[1,0,3,2] row_mask:0xf bank_mask:0xf
s_nop 1
v_add_u32_dpp v16, v16, v16 quad_perm:[2,3,0,1] row_mask:0xf bank_mask:0xf
s_nop 1
v_add_u32_dpp v16, v16, v16 row_half_mirror row_mask:0xf bank_mask:0xf
s_nop 0
v_readfirstlane_b32 s74, v16
s_nop 1
s_ashr_i32 s74, s74, 3
s_add_u32 s99, s90, s74
s_and_b32 s74, %[g], 1
s_mul_i32 s74, s74, 10
s_add_u32 s99, s99, s74
s_add_u32 s91, s99, 745
s_memrealtime s[92:93]
s_waitcnt lgkmcnt(0)
s_sub_u32 s92, s92, s91
s_cmp_lt_i32 s92, 12
s_cbranch_scc1 LtW1_%=a
s_branch LtR2_%=
LtW1_%=:
s_memrealtime s[92:93]
s_waitcnt lgkmcnt(0)
s_sub_u32 s92, s92, s91
LtW1_%=a:
s_cmp_lt_i32 s92, 0
s_cbranch_scc1 LtW1_%=
LtR2_%=:
buffer_load_dwordx4 v[36:39], v32, %[rw], s87 offen offset:0 sc1
buffer_load_dwordx4 v[40:43], v32, %[rw], s87 offen offset:2048 sc1
buffer_load_dwordx4 v[44:47], v32, %[rw], s88 offen offset:0 sc1
buffer_load_dwordx4 v[48:51], v32, %[rw], s88 offen offset:2048 sc1
v_add_u32 v13, 159744, v7
v_lshlrev_b32_e64 v14, 7, v7
v_mov_b32 v15, 0x7ff00000
s_mov_b32 s94, -1
s_movk_i32 s95, 0xfff
v_cndmask_b32 v15, v15, v14, s[94:95]
v_lshrrev_b32 v2, 1, v7
v_lshrrev_b32 v3, 7, v7
v_and_b32_e64 v3, 6, v3
s_mov_b32 s89, 0x3b000000
v_mov_b32 v4, 0x3f803f80
v_mov_b32_e64 v55, 0
s_mov_b32 s90, 0x3727c5ac
v_writelane_b32 v55, s90, 32
v_lshlrev_b32_e64 v0, 16, v5
v_and_b32 v1, 0xffff0000, v5
LtR2_%=w:
s_waitcnt vmcnt(2)
v_pk_add_f32 v[16:17], v[36:37], v[40:41]
v_pk_add_f32 v[18:19], v[38:39], v[42:43]
s_waitcnt vmcnt(0)
v_pk_add_f32 v[20:21], v[44:45], v[48:49]
)" R"(
v_pk_add_f32 v[22:23], v[46:47], v[50:51]
v_pk_add_f32 v[16:17], v[16:17], v[20:21]
v_pk_add_f32 v[18:19], v[18:19], v[22:23]
v_pk_add_f32 v[20:21], v[16:17], v[18:19]
v_cmp_u_f32 vcc, v20, v21
s_cbranch_vccnz LtR2_%=r
s_mov_b64 exec, -1
s_nop 0
LtD3_%=:
v_add_f32_dpp v24, v16, v16 row_ror:8 row_mask:0xf bank_mask:0x3
v_add_f32_dpp v25, v17, v17 row_ror:8 row_mask:0xf bank_mask:0x3
v_add_f32_dpp v24, v18, v18 row_ror:8 row_mask:0xf bank_mask:0xc
v_add_f32_dpp v25, v19, v19 row_ror:8 row_mask:0xf bank_mask:0xc
v_cvt_pk_bf16_f32 v26, v24, v25
s_nop 1
v_mov_b32_dpp v28, v26 quad_perm:[0,0,0,0] row_mask:0xf bank_mask:0xf
v_mov_b32_dpp v29, v26 quad_perm:[1,1,1,1] row_mask:0xf bank_mask:0xf
v_mov_b32_dpp v30, v26 quad_perm:[2,2,2,2] row_mask:0xf bank_mask:0xf
v_mov_b32_dpp v31, v26 quad_perm:[3,3,3,3] row_mask:0xf bank_mask:0xf
s_nop 1
v_mfma_f32_16x16x32_bf16 v[72:75], v[28:31], v[72:75], 0
v_mfma_f32_16x16x32_bf16 v[76:79], v[28:31], v[76:79], 0
v_mfma_f32_16x16x32_bf16 v[80:83], v[28:31], v[80:83], 0
v_mfma_f32_16x16x32_bf16 v[84:87], v[28:31], v[84:87], 0
s_mov_b32 exec_lo, 0x00f0000f
s_mov_b32 exec_hi, 0xf0000f00
s_nop 3
ds_write2_b32 v33, v72, v76 offset0:192 offset1:196
s_nop 0
ds_write2_b32 v33, v80, v84 offset0:200 offset1:204
s_mov_b32 exec_lo, -1
s_mov_b32 exec_hi, 0
s_waitcnt lgkmcnt(0)
s_barrier
ds_read_b128 v[16:19], v34 offset:0
ds_read_b128 v[20:23], v34 offset:1024
ds_read_b128 v[24:27], v34 offset:2048
ds_read_b128 v[28:31], v34 offset:3072
s_waitcnt lgkmcnt(2)
v_pk_add_f32 v[16:17], v[16:17], v[20:21]
v_pk_add_f32 v[18:19], v[18:19], v[22:23]
s_waitcnt lgkmcnt(0)
v_pk_add_f32 v[24:25], v[24:25], v[28:29]
v_pk_add_f32 v[26:27], v[26:27], v[30:31]
v_pk_add_f32 v[16:17], v[16:17], v[18:19]
v_pk_add_f32 v[24:25], v[24:25], v[26:27]
v_pk_add_f32 v[16:17], v[16:17], v[24:25]
v_add_f32 v16, v16, v17
v_min_u32 v16, -2, v16
buffer_store_dword v16, v35, %[rw], s70 offen sc1
s_mov_b32 exec_hi, -1
s_add_u32 s87, s71, 0
s_add_u32 s88, s71, 4096
s_movk_i32 s84, 32
s_mov_b64 vcc, -1
s_add_u32 s91, s91, 76
s_memrealtime s[92:93]
s_waitcnt lgkmcnt(0)
s_sub_u32 s92, s92, s91
s_cmp_lt_i32 s92, 12
s_cbranch_scc1 LtW5_%=a
s_sleep 6
s_branch LtR6_%=
LtW5_%=:
s_memrealtime s[92:93]
s_waitcnt lgkmcnt(0)
s_sub_u32 s92, s92, s91
LtW5_%=a:
s_cmp_lt_i32 s92, 0
s_cbranch_scc1 LtW5_%=
LtR6_%=:
s_mov_b64 exec, vcc
s_sub_u32 s84, s84, 1
s_cbranch_scc1 LtP8_%=
buffer_load_dwordx4 v[36:39], v32, %[rw], s87 offen offset:0 sc1
buffer_load_dwordx4 v[40:43], v32, %[rw], s87 offen offset:2048 sc1
buffer_load_dwordx4 v[44:47], v32, %[rw], s88 offen offset:0 sc1
buffer_load_dwordx4 v[48:51], v32, %[rw], s88 offen offset:2048 sc1
s_waitcnt vmcnt(2)
v_pk_add_f32 v[16:17], v[36:37], v[40:41]
v_pk_add_f32 v[18:19], v[38:39], v[42:43]
s_waitcnt vmcnt(0)
s_nop 0
v_pk_add_f32 v[20:21], v[44:45], v[48:49]
v_pk_add_f32 v[22:23], v[46:47], v[50:51]
v_pk_add_f32 v[16:17], v[16:17], v[20:21]
v_pk_add_f32 v[18:19], v[18:19], v[22:23]
v_pk_add_f32 v[20:21], v[16:17], v[18:19]
v_cmp_u_f32 vcc, v20, v21
s_cbranch_vccnz LtR6_%=
s_mov_b64 exec, -1
s_nop 0
LtD7_%=:
s_mov_b32 vcc_lo, 0x22222222
s_mov_b32 vcc_hi, 0x22222222
v_add_f32_dpp v24, v16, v16 row_ror:8 row_mask:0xf bank_mask:0x3
v_add_f32_dpp v25, v17, v17 row_ror:8 row_mask:0xf bank_mask:0x3
v_add_f32_dpp v24, v18, v18 row_ror:8 row_mask:0xf bank_mask:0xc
v_add_f32_dpp v25, v19, v19 row_ror:8 row_mask:0xf bank_mask:0xc
v_cvt_pk_bf16_f32 v26, v24, v25
v_lshlrev_b32_e64 v24, 16, v26
v_and_b32 v25, 0xffff0000, v26
v_pk_add_f32 v[24:25], v[24:25], v[0:1]
v_cvt_pk_bf16_f32 v5, v24, v25
s_nop 1
v_cndmask_b32_dpp v28, v5, v4, vcc quad_perm:[0,0,0,0] row_mask:0xf bank_mask:0xf
v_cndmask_b32_dpp v29, v5, v4, vcc quad_perm:[1,1,1,1] row_mask:0xf bank_mask:0xf
v_cndmask_b32_dpp v30, v5, v4, vcc quad_perm:[2,2,2,2] row_mask:0xf bank_mask:0xf
v_cndmask_b32_dpp v31, v5, v4, vcc quad_perm:[3,3,3,3] row_mask:0xf bank_mask:0xf
s_nop 1
v_mfma_f32_16x16x32_bf16 v[16:19], v[28:31], v[28:31], 0
v_mfma_f32_16x16x32_bf16 v[88:91], v[28:31], v[88:91], 0
v_mfma_f32_16x16x32_bf16 v[92:95], v[28:31], v[92:95], 0
v_mfma_f32_16x16x32_bf16 v[96:99], v[28:31], v[96:99], 0
v_mfma_f32_16x16x32_bf16 v[100:103], v[28:31], v[100:103], 0
s_cmp_eq_u32 s34, 0
s_mov_b32 exec_lo, 0x00f0000f
s_mov_b32 exec_hi, 0xf0000f00
s_nop 0
ds_write2_b32 v33, v16, v17 offset0:128 offset1:132
ds_write2_b32 v33, v88, v89 offset0:0 offset1:64
ds_write2_b32 v33, v92, v93 offset0:4 offset1:68
ds_write2_b32 v33, v96, v97 offset0:8 offset1:72
ds_write2_b32 v33, v100, v101 offset0:12 offset1:76
s_mov_b64 exec, -1
s_waitcnt lgkmcnt(0)
s_barrier
s_cbranch_scc1 LtJ9_%=
ds_read_b128 v[16:19], v13 offset:0
ds_read_b128 v[20:23], v13 offset:1024
ds_read_b128 v[24:27], v13 offset:2048
ds_read_b128 v[28:31], v13 offset:3072
s_waitcnt lgkmcnt(2)
v_pk_add_f32 v[16:17], v[16:17], v[20:21]
v_pk_add_f32 v[18:19], v[18:19], v[22:23]
s_waitcnt lgkmcnt(0)
v_pk_add_f32 v[24:25], v[24:25], v[28:29]
v_pk_add_f32 v[26:27], v[26:27], v[30:31]
v_pk_add_f32 v[16:17], v[16:17], v[18:19]
v_pk_add_f32 v[24:25], v[24:25], v[26:27]
v_pk_add_f32 v[16:17], v[16:17], v[24:25]
v_add_f32_e64 v16, v16, v17
v_fma_f32 v18, v16, s89, v55
v_mov_b32_e64 v19, v16
v_readlane_b32 s87, v18, 33
v_readlane_b32 s88, v18, 32
v_permlane16_swap_b32 v16, v19
v_mul_f32 v20, s87, s87
v_sub_f32 v20, s88, v20
v_fma_f32 v16, -s87, v19, v16
v_max_f32 v20, 0x3727c5ac, v20
v_rsq_f32 v20, v20
s_nop 0
v_mul_f32_e64 v16, v16, v20
v_cvt_pk_bf16_f32 v16, v16, v16
v_lshlrev_b32_e64 v16, 16, v16
v_mul_f32 v17, 0xbfb8aa3b, v16
v_exp_f32 v17, v17
s_nop 0
v_add_f32 v17, 1.0, v17
v_rcp_f32 v17, v17
s_nop 0
v_cvt_pk_bf16_f32 v17, v17, v17
v_lshlrev_b32 v17, 16, v17
v_mul_f32_dpp v17, v16, v17 quad_perm:[2,3,2,3] row_mask:0xf bank_mask:0xf
s_nop 1
v_mov_b32_dpp v16, v17 quad_perm:[1,1,1,1] row_mask:0xf bank_mask:0xf
v_cvt_pk_bf16_f32 v17, v17, v16
s_nop 0
v_readlane_b32 s94, v17, 0
v_readlane_b32 s95, v17, 4
v_readlane_b32 s96, v17, 8
v_readlane_b32 s97, v17, 12
v_dot2_f32_bf16 v18, v56, s94, 0
v_dot2_f32_bf16 v19, v60, s94, 0
v_dot2_f32_bf16 v18, v57, s95, v18
v_dot2_f32_bf16 v19, v61, s95, v19
v_dot2_f32_bf16 v18, v58, s96, v18
v_dot2_f32_bf16 v19, v62, s96, v19
v_dot2_f32_bf16 v18, v59, s97, v18
v_dot2_f32_bf16 v19, v63, s97, v19
s_nop 1
v_min_u32 v18, -2, v18
v_min_u32_e64 v19, -2, v19
buffer_store_dwordx2 v[18:19], v2, %[rw], s32 offen sc1
LtJ9_%=:
s_cmp_eq_u32 s35, 0
s_cbranch_scc1 LtJ10_%=
s_add_u32 s89, s36, 8
v_readlane_b32 s87, v5, s36
v_readlane_b32 s88, v5, s89
s_lshl_b32 s89, s87, 16
s_and_b32 s87, s87, 0xffff0000
s_lshl_b32 s90, s88, 16
s_nop 0
s_and_b32 s88, s88, 0xffff0000
v_writelane_b32 v16, s89, 0
v_writelane_b32 v16, s87, 16
v_writelane_b32 v16, s90, 32
v_writelane_b32 v16, s88, 48
s_add_u32 s87, s33, 131072
s_add_u32 s88, s33, 262144
s_movk_i32 s84, 32
s_mov_b64 vcc, -1
s_add_u32 s91, s91, 98
s_memrealtime s[92:93]
s_waitcnt lgkmcnt(0)
s_sub_u32 s92, s92, s91
s_cmp_lt_i32 s92, 12
s_cbranch_scc1 LtW11_%=a
s_sleep 6
s_branch LtR12_%=
LtW11_%=:
s_memrealtime s[92:93]
s_waitcnt lgkmcnt(0)
s_sub_u32 s92, s92, s91
LtW11_%=a:
s_cmp_lt_i32 s92, 0
s_cbranch_scc1 LtW11_%=
LtR12_%=:
s_mov_b64 exec, vcc
s_sub_u32 s84, s84, 1
s_cbranch_scc1 LtP14_%=
buffer_load_dwordx4 v[36:39], v14, %[rw], s33 offen sc1
buffer_load_dwordx4 v[40:43], v14, %[rw], s87 offen sc1
buffer_load_dwordx4 v[44:47], v15, %[rw], s88 offen sc1
s_waitcnt vmcnt(1)
v_pk_add_f32 v[20:21], v[36:37], v[40:41]
v_pk_add_f32 v[22:23], v[38:39], v[42:43]
s_waitcnt vmcnt(0)
v_pk_add_f32 v[20:21], v[20:21], v[44:45]
v_pk_add_f32 v[22:23], v[22:23], v[46:47]
v_pk_add_f32 v[24:25], v[20:21], v[22:23]
v_cmp_u_f32 vcc, v24, v25
s_cbranch_vccnz LtR12_%=
s_mov_b64 exec, -1
LtD13_%=:
v_permlane32_swap_b32 v20, v22
v_permlane32_swap_b32 v21, v23
v_pk_add_f32 v[20:21], v[20:21], v[22:23]
s_nop 1
v_permlane16_swap_b32 v20, v21
v_add_f32 v20, v20, v21
s_nop 1
v_add_f32_dpp v20, v20, v20 quad_perm:[1,0,3,2] row_mask:0xf bank_mask:0xf
s_nop 1
v_add_f32_dpp v20, v20, v20 quad_perm:[2,3,0,1] row_mask:0xf bank_mask:0xf
s_nop 1
v_add_f32_dpp v20, v20, v20 row_half_mirror row_mask:0xf bank_mask:0xf
s_nop 1
v_add_f32_dpp v20, v20, v20 row_ror:8 row_mask:0xf bank_mask:0xf
v_cvt_pk_bf16_f32 v20, v20, v20
v_lshlrev_b32 v20, 16, v20
v_add_f32_e64 v20, v20, v16
v_cvt_pk_bf16_f32 v20, v20, v20
s_mov_b32 exec_lo, 0x00010001
s_mov_b32 exec_hi, 0x00010001
buffer_store_short v20, v3, s[44:47], s37 offen 
s_mov_b64 exec, -1
LtJ10_%=:
s_waitcnt vmcnt(0)
s_endpgm
Lexc:
v_mbcnt_lo_u32_b32 v108, vcc_lo, 0
v_mbcnt_hi_u32_b32 v108, vcc_hi, v108
v_mbcnt_lo_u32_b32 v110, -1, 0
v_mbcnt_hi_u32_b32 v110, -1, v110
s_bcnt1_i32_b64 s95, vcc
v_add_u32 v108, s78, v108
s_add_u32 s78, s78, s95
v_lshl_add_u32 v109, v108, 4, s79
v_lshl_add_u32 v108, v108, 2, s79
v_or_b32 v110, s94, v110
s_mov_b64 exec, vcc
s_cmp_gt_u32 s78, 48
s_cbranch_scc1 Lexc2
ds_write_b128 v109, v[104:107]
ds_write_b32 v108, v110 offset:768
s_waitcnt lgkmcnt(0)
Lexc2:
v_mov_b32 v106, 0
v_mov_b32 v107, 0
v_mov_b32 v114, 0
s_mov_b64 exec, -1
s_add_u32 s92, s92, 4
s_addc_u32 s93, s93, 0
s_setpc_b64 s[92:93]
LmP4_%=:
s_mov_b64 exec, -1
s_mov_b32 s84, 65536
LmR2_%=m:
v_max3_u32 v8, v36, v37, v38
v_max_u32 v8, v8, v39
v_max3_u32 v9, v40, v41, v42
v_max_u32_e64 v9, v9, v43
v_max3_u32 v10, v44, v45, v46
v_max_u32_e64 v10, v10, v47
v_max3_u32 v11, v48, v49, v50
v_max_u32_e64 v11, v11, v51
v_cmp_eq_u32 s[92:93], -1, v8
v_cmp_eq_u32 s[94:95], -1, v9
v_cmp_eq_u32 s[96:97], -1, v10
v_cmp_eq_u32 s[98:99], -1, v11
s_or_b64 vcc, s[92:93], s[94:95]
s_or_b64 vcc, vcc, s[96:97]
s_or_b64 vcc, vcc, s[98:99]
s_cselect_b32 s84, s84, 0
s_mov_b64 exec, s[92:93]
s_cbranch_execz LmR2_%=0
buffer_load_dwordx4 v[36:39], v32, %[rw], s87 offen offset:0 sc1
LmR2_%=0:
s_mov_b64 exec, s[94:95]
s_cbranch_execz LmR2_%=1
buffer_load_dwordx4 v[40:43], v32, %[rw], s87 offen offset:2048 sc1
LmR2_%=1:
s_mov_b64 exec, s[96:97]
s_cbranch_execz LmR2_%=2
buffer_load_dwordx4 v[44:47], v32, %[rw], s88 offen offset:0 sc1
LmR2_%=2:
s_mov_b64 exec, s[98:99]
s_cbranch_execz LmR2_%=3
buffer_load_dwordx4 v[48:51], v32, %[rw], s88 offen offset:2048 sc1
LmR2_%=3:
s_mov_b64 exec, -1
s_waitcnt vmcnt(3)
v_pk_add_f32 v[8:9], v[36:37], v[40:41]
v_pk_add_f32 v[10:11], v[38:39], v[42:43]
v_pk_add_f32 v[12:13], v[44:45], v[48:49]
v_pk_add_f32 v[14:15], v[46:47], v[50:51]
v_pk_add_f32 v[8:9], v[8:9], v[12:13]
v_pk_add_f32 v[10:11], v[10:11], v[14:15]
v_pk_add_f32 v[12:13], v[8:9], v[10:11]
v_cmp_u_f32 vcc, v12, v13
s_cbranch_vccz LmD3_%=
s_sub_u32 s84, s84, 1
s_cbranch_scc0 LmR2_%=m
s_branch LmD3_%=
LmP8_%=:
s_mov_b64 exec, -1
s_mov_b32 s84, 65536
LmR6_%=m:
v_max3_u32 v8, v36, v37, v38
v_max_u32_e64 v8, v8, v39
v_max3_u32 v9, v40, v41, v42
v_max_u32_e64 v9, v9, v43
v_max3_u32 v10, v44, v45, v46
v_max_u32_e64 v10, v10, v47
v_max3_u32 v11, v48, v49, v50
v_max_u32_e64 v11, v11, v51
v_cmp_eq_u32 s[92:93], -1, v8
v_cmp_eq_u32 s[94:95], -1, v9
v_cmp_eq_u32 s[96:97], -1, v10
v_cmp_eq_u32 s[98:99], -1, v11
s_or_b64 vcc, s[92:93], s[94:95]
s_or_b64 vcc, vcc, s[96:97]
s_or_b64 vcc, vcc, s[98:99]
s_cselect_b32 s84, s84, 0
s_mov_b64 exec, s[92:93]
s_cbranch_execz LmR6_%=0
buffer_load_dwordx4 v[36:39], v32, %[rw], s87 offen offset:0 sc1
LmR6_%=0:
s_mov_b64 exec, s[94:95]
s_cbranch_execz LmR6_%=1
buffer_load_dwordx4 v[40:43], v32, %[rw], s87 offen offset:2048 sc1
LmR6_%=1:
s_mov_b64 exec, s[96:97]
s_cbranch_execz LmR6_%=2
buffer_load_dwordx4 v[44:47], v32, %[rw], s88 offen offset:0 sc1
LmR6_%=2:
s_mov_b64 exec, s[98:99]
s_cbranch_execz LmR6_%=3
buffer_load_dwordx4 v[48:51], v32, %[rw], s88 offen offset:2048 sc1
LmR6_%=3:
s_mov_b64 exec, -1
s_waitcnt vmcnt(3)
v_pk_add_f32 v[8:9], v[36:37], v[40:41]
v_pk_add_f32 v[10:11], v[38:39], v[42:43]
v_pk_add_f32 v[12:13], v[44:45], v[48:49]
v_pk_add_f32 v[14:15], v[46:47], v[50:51]
v_pk_add_f32 v[8:9], v[8:9], v[12:13]
v_pk_add_f32 v[10:11], v[10:11], v[14:15]
v_pk_add_f32 v[12:13], v[8:9], v[10:11]
v_cmp_u_f32 vcc, v12, v13
s_cbranch_vccz LmD7_%=
s_sub_u32 s84, s84, 1
s_cbranch_scc0 LmR6_%=m
s_branch LmD7_%=
LmP18_%=:
s_mov_b64 exec, -1
)" R"(
s_mov_b32 s84, 65536
LmR16_%=m:
v_max3_u32 v8, v36, v37, v38
v_max_u32_e64 v8, v8, v39
v_max3_u32 v9, v40, v41, v42
v_max_u32_e64 v9, v9, v43
v_max3_u32 v10, v44, v45, v46
v_max_u32_e64 v10, v10, v47
v_max3_u32 v11, v48, v49, v50
v_max_u32_e64 v11, v11, v51
v_cmp_eq_u32 s[92:93], -1, v8
v_cmp_eq_u32 s[94:95], -1, v9
v_cmp_eq_u32 s[96:97], -1, v10
v_cmp_eq_u32 s[98:99], -1, v11
s_or_b64 vcc, s[92:93], s[94:95]
s_or_b64 vcc, vcc, s[96:97]
s_or_b64 vcc, vcc, s[98:99]
s_cselect_b32 s84, s84, 0
s_mov_b64 exec, s[92:93]
s_cbranch_execz LmR16_%=0
buffer_load_dwordx4 v[36:39], v32, %[rw], s87 offen offset:0 sc1
LmR16_%=0:
s_mov_b64 exec, s[94:95]
s_cbranch_execz LmR16_%=1
buffer_load_dwordx4 v[40:43], v32, %[rw], s87 offen offset:2048 sc1
LmR16_%=1:
s_mov_b64 exec, s[96:97]
s_cbranch_execz LmR16_%=2
buffer_load_dwordx4 v[44:47], v32, %[rw], s88 offen offset:0 sc1
LmR16_%=2:
s_mov_b64 exec, s[98:99]
s_cbranch_execz LmR16_%=3
buffer_load_dwordx4 v[48:51], v32, %[rw], s88 offen offset:2048 sc1
LmR16_%=3:
s_mov_b64 exec, -1
s_waitcnt vmcnt(3)
v_pk_add_f32 v[8:9], v[36:37], v[40:41]
v_pk_add_f32 v[10:11], v[38:39], v[42:43]
v_pk_add_f32 v[12:13], v[44:45], v[48:49]
v_pk_add_f32 v[14:15], v[46:47], v[50:51]
v_pk_add_f32 v[8:9], v[8:9], v[12:13]
v_pk_add_f32 v[10:11], v[10:11], v[14:15]
v_pk_add_f32 v[12:13], v[8:9], v[10:11]
v_cmp_u_f32 vcc, v12, v13
s_cbranch_vccz LmD17_%=
s_sub_u32 s84, s84, 1
s_cbranch_scc0 LmR16_%=m
s_branch LmD17_%=
Lslowk:
s_waitcnt lgkmcnt(0)
s_cmp_lg_u64 vcc, 0
s_cselect_b64 s[86:87], -1, 0
s_mov_b64 s[72:73], 0
s_mov_b32 s75, 0x07050301
v_and_b32 v47, 0xc0, v7
v_lshlrev_b32 v47, 2, v47
v_lshrrev_b32_e64 v46, 2, v7
v_and_b32 v46, 0xc0, v46
v_and_or_b32 v46, v7, 48, v46
v_add_u32 v46, v46, v47
s_mov_b32 s84, 0
Lslowk1:
s_lshl_b32 s85, s84, 10
buffer_load_dwordx4 v[36:39], v46, %[rk], s85 offen
s_and_b32 s92, s84, 15
s_lshl_b32 s92, s92, 4
s_lshr_b32 s93, s84, 2
s_and_b32 s93, s93, 12
s_add_u32 s92, s92, s93
s_cmp_lt_u32 s84, 64
s_cselect_b32 s93, s43, s79
s_add_u32 s92, s92, s93
v_add_u32 v50, s92, v47
s_waitcnt vmcnt(0)
s_nop 0
v_perm_b32 v44, v37, v36, s75
v_perm_b32 v45, v39, v38, s75
v_pk_mad_u16 v40, v36, 2, -1 op_sel_hi:[1,0,0]
v_pk_mad_u16 v41, v37, 2, -1 op_sel_hi:[1,0,0]
v_pk_mad_u16 v42, v38, 2, -1 op_sel_hi:[1,0,0]
v_pk_mad_u16 v43, v39, 2, -1 op_sel_hi:[1,0,0]
v_and_b32 v44, 0x7f7f7f7f, v44
v_and_b32 v45, 0x7f7f7f7f, v45
v_sub_u32 v44, s80, v44
v_sub_u32 v45, s80, v45
v_pk_min_u16 v40, v40, v41
v_pk_min_u16 v42, v42, v43
v_pk_min_u16 v40, v40, v42
v_or_b32_e64 v44, v44, v45
v_and_b32 v44, 0x80808080, v44
v_pk_sub_u16 v40, s81, v40 clamp
v_cmp_ne_u32_e64 vcc, v40, v44
v_dot2_f32_bf16 v48, v36, v0, 0
v_dot2_f32_bf16 v49, v38, v2, 0
v_dot2_f32_bf16 v48, v37, v1, v48
v_dot2_f32_bf16 v49, v39, v3, v49
s_nop 2
v_add_f32 v48, v48, v49
s_or_b64 exec, vcc, s[86:87]
ds_add_f32 v50, v48
s_mov_b64 exec, -1
s_waitcnt lgkmcnt(0)
s_add_u32 s84, s84, 1
s_cmp_lt_u32 s84, 128
s_cbranch_scc1 Lslowk1
s_nop 0
v_cndmask_b32 v0, v0, 0, s[86:87]
v_cndmask_b32 v1, v1, 0, s[86:87]
v_cndmask_b32 v2, v2, 0, s[86:87]
v_cndmask_b32 v3, v3, 0, s[86:87]
v_cndmask_b32 v4, v4, 0, s[86:87]
s_memrealtime s[92:93]
s_nop 7
s_branch Lslowkr
Lslowv:
s_waitcnt vmcnt(0) lgkmcnt(0)
s_mov_b32 s75, 0x07050301
v_and_b32 v21, 0xc0, v7
v_lshl_add_u32 v21, v21, 1, s43
v_lshrrev_b32 v11, 2, v7
v_and_b32 v11, 12, v11
v_add_u32 v21, v21, v11
v_and_b32 v11, 0xf0, v7
v_sub_u32 v11, v53, v11
s_mov_b32 s84, 0
s_mov_b32 s72, 0xf0000000
s_mov_b32 s73, 0xffffffff
Lslowv1:
s_cmp_eq_u32 s84, 64
s_cmov_b32 s72, 0xf
s_cmov_b32 s73, 0x3ff000
s_bitcmp1_b64 s[72:73], s84
s_cbranch_scc0 Lslowv2
s_lshl_b32 s85, s84, 10
buffer_load_dwordx4 v[16:19], v7, %[rv], s85 offen
s_and_b32 s92, s84, 15
s_lshl_b32 s92, s92, 4
s_lshr_b32 s93, s84, 2
s_and_b32 s93, s93, 12
s_add_u32 s92, s92, s93
s_and_b32 s93, s84, 64
s_lshl_b32 s93, s93, 4
s_add_u32 s92, s92, s93
v_add_u32_e64 v20, s92, v11
ds_read_b32 v10, v20
s_waitcnt vmcnt(0) lgkmcnt(0)
s_nop 0
v_perm_b32 v8, v17, v16, s75
v_perm_b32 v9, v19, v18, s75
v_pk_mad_u16 v12, v16, 2, -1 op_sel_hi:[1,0,0]
v_pk_mad_u16 v13, v17, 2, -1 op_sel_hi:[1,0,0]
v_pk_mad_u16 v14, v18, 2, -1 op_sel_hi:[1,0,0]
v_pk_mad_u16 v15, v19, 2, -1 op_sel_hi:[1,0,0]
v_and_b32 v8, 0x7f7f7f7f, v8
v_and_b32 v9, 0x7f7f7f7f, v9
v_sub_u32 v8, s82, v8
v_sub_u32 v9, s82, v9
v_pk_min_u16 v12, v12, v13
v_pk_min_u16 v14, v14, v15
v_pk_min_u16 v12, v12, v14
v_or_b32_e64 v8, v8, v9
v_and_b32 v8, 0x80808080, v8
v_pk_sub_u16 v12, s83, v12 clamp
v_cmp_ne_u32_e64 vcc, v12, v8
v_cvt_pk_bf16_f32 v9, 0, v10
v_cvt_pk_bf16_f32 v10, v10, 0
v_dot2_f32_bf16 v12, v16, v9, 0
v_dot2_f32_bf16 v16, v16, v10, 0
v_dot2_f32_bf16 v13, v17, v9, 0
v_dot2_f32_bf16 v17, v17, v10, 0
v_dot2_f32_bf16 v14, v18, v9, 0
v_dot2_f32_bf16 v18, v18, v10, 0
v_dot2_f32_bf16 v15, v19, v9, 0
v_dot2_f32_bf16 v19, v19, v10, 0
s_mov_b64 exec, vcc
s_nop 0
ds_add_f32 v21, v16 offset:0
ds_add_f32 v21, v12 offset:16
ds_add_f32 v21, v17 offset:32
ds_add_f32 v21, v13 offset:48
ds_add_f32 v21, v18 offset:64
ds_add_f32 v21, v14 offset:80
ds_add_f32 v21, v19 offset:96
ds_add_f32 v21, v15 offset:112
s_mov_b64 exec, -1
s_waitcnt lgkmcnt(0)
Lslowv2:
s_add_u32 s84, s84, 1
s_cmp_lt_u32 s84, 128
s_cbranch_scc1 Lslowv1
s_nop 7
s_branch Lslowvr
Lml:
s_mov_b32 s84, 65536
Lml1:
buffer_load_dwordx4 v[8:11], v15, %[rw], s39 offen sc1
s_waitcnt vmcnt(0)
v_cmp_ne_u32 vcc, s41, v9
v_cmp_ne_u32 s[88:89], s41, v11
s_or_b64 s[88:89], s[88:89], vcc
s_cbranch_scc0 Lmlr
s_sleep 2
s_sub_u32 s84, s84, 1
s_cbranch_scc0 Lml1
s_branch Lmlr
LtP4_%=:
s_mov_b64 exec, -1
s_mov_b32 s84, 65536
LtR2_%=m:
v_max3_u32 v16, v36, v37, v38
v_max_u32 v16, v16, v39
v_max3_u32 v17, v40, v41, v42
v_max_u32_e64 v17, v17, v43
v_max3_u32 v18, v44, v45, v46
v_max_u32_e64 v18, v18, v47
v_max3_u32 v19, v48, v49, v50
v_max_u32_e64 v19, v19, v51
v_cmp_eq_u32 s[92:93], -1, v16
v_cmp_eq_u32 s[94:95], -1, v17
v_cmp_eq_u32 s[96:97], -1, v18
v_cmp_eq_u32 s[98:99], -1, v19
s_or_b64 vcc, s[92:93], s[94:95]
s_or_b64 vcc, vcc, s[96:97]
s_or_b64 vcc, vcc, s[98:99]
s_cselect_b32 s84, s84, 0
s_mov_b64 exec, s[92:93]
s_cbranch_execz LtR2_%=0
buffer_load_dwordx4 v[36:39], v32, %[rw], s87 offen offset:0 sc1
LtR2_%=0:
s_mov_b64 exec, s[94:95]
s_cbranch_execz LtR2_%=1
buffer_load_dwordx4 v[40:43], v32, %[rw], s87 offen offset:2048 sc1
LtR2_%=1:
s_mov_b64 exec, s[96:97]
s_cbranch_execz LtR2_%=2
buffer_load_dwordx4 v[44:47], v32, %[rw], s88 offen offset:0 sc1
LtR2_%=2:
s_mov_b64 exec, s[98:99]
s_cbranch_execz LtR2_%=3
buffer_load_dwordx4 v[48:51], v32, %[rw], s88 offen offset:2048 sc1
LtR2_%=3:
s_mov_b64 exec, -1
s_waitcnt vmcnt(3)
v_pk_add_f32 v[16:17], v[36:37], v[40:41]
v_pk_add_f32 v[18:19], v[38:39], v[42:43]
v_pk_add_f32 v[20:21], v[44:45], v[48:49]
v_pk_add_f32 v[22:23], v[46:47], v[50:51]
v_pk_add_f32 v[16:17], v[16:17], v[20:21]
v_pk_add_f32 v[18:19], v[18:19], v[22:23]
v_pk_add_f32 v[20:21], v[16:17], v[18:19]
v_cmp_u_f32 vcc, v20, v21
s_cbranch_vccz LtD3_%=
s_sub_u32 s84, s84, 1
s_cbranch_scc0 LtR2_%=m
s_branch LtD3_%=
LtP8_%=:
s_mov_b64 exec, -1
s_mov_b32 s84, 65536
LtR6_%=m:
v_max3_u32 v16, v36, v37, v38
v_max_u32_e64 v16, v16, v39
v_max3_u32 v17, v40, v41, v42
v_max_u32_e64 v17, v17, v43
v_max3_u32 v18, v44, v45, v46
v_max_u32_e64 v18, v18, v47
v_max3_u32 v19, v48, v49, v50
v_max_u32_e64 v19, v19, v51
v_cmp_eq_u32 s[92:93], -1, v16
v_cmp_eq_u32 s[94:95], -1, v17
v_cmp_eq_u32 s[96:97], -1, v18
v_cmp_eq_u32 s[98:99], -1, v19
s_or_b64 vcc, s[92:93], s[94:95]
s_or_b64 vcc, vcc, s[96:97]
s_or_b64 vcc, vcc, s[98:99]
s_cselect_b32 s84, s84, 0
s_mov_b64 exec, s[92:93]
s_cbranch_execz LtR6_%=0
buffer_load_dwordx4 v[36:39], v32, %[rw], s87 offen offset:0 sc1
LtR6_%=0:
s_mov_b64 exec, s[94:95]
s_cbranch_execz LtR6_%=1
buffer_load_dwordx4 v[40:43], v32, %[rw], s87 offen offset:2048 sc1
LtR6_%=1:
s_mov_b64 exec, s[96:97]
s_cbranch_execz LtR6_%=2
buffer_load_dwordx4 v[44:47], v32, %[rw], s88 offen offset:0 sc1
LtR6_%=2:
s_mov_b64 exec, s[98:99]
s_cbranch_execz LtR6_%=3
buffer_load_dwordx4 v[48:51], v32, %[rw], s88 offen offset:2048 sc1
LtR6_%=3:
s_mov_b64 exec, -1
s_waitcnt vmcnt(3)
v_pk_add_f32 v[16:17], v[36:37], v[40:41]
v_pk_add_f32 v[18:19], v[38:39], v[42:43]
v_pk_add_f32 v[20:21], v[44:45], v[48:49]
v_pk_add_f32 v[22:23], v[46:47], v[50:51]
v_pk_add_f32 v[16:17], v[16:17], v[20:21]
v_pk_add_f32 v[18:19], v[18:19], v[22:23]
v_pk_add_f32 v[20:21], v[16:17], v[18:19]
v_cmp_u_f32 vcc, v20, v21
s_cbranch_vccz LtD7_%=
s_sub_u32 s84, s84, 1
s_cbranch_scc0 LtR6_%=m
s_branch LtD7_%=
LtP14_%=:
s_mov_b64 exec, -1
s_mov_b32 s84, 65536
LtR12_%=m:
v_max3_u32 v20, v36, v37, v38
v_max_u32_e64 v20, v20, v39
v_max3_u32 v21, v40, v41, v42
v_max_u32_e64 v21, v21, v43
v_max3_u32 v22, v44, v45, v46
v_max_u32_e64 v22, v22, v47
v_cmp_eq_u32 s[92:93], -1, v20
v_cmp_eq_u32 s[94:95], -1, v21
v_cmp_eq_u32 s[96:97], -1, v22
s_or_b64 vcc, s[92:93], s[94:95]
s_or_b64 vcc, vcc, s[96:97]
s_cselect_b32 s84, s84, 0
s_mov_b64 exec, s[92:93]
s_cbranch_execz LtR12_%=0
buffer_load_dwordx4 v[36:39], v14, %[rw], s33 offen sc1
LtR12_%=0:
s_mov_b64 exec, s[94:95]
s_cbranch_execz LtR12_%=1
buffer_load_dwordx4 v[40:43], v14, %[rw], s87 offen sc1
LtR12_%=1:
s_mov_b64 exec, s[96:97]
s_cbranch_execz LtR12_%=2
buffer_load_dwordx4 v[44:47], v15, %[rw], s88 offen sc1
LtR12_%=2:
s_mov_b64 exec, -1
s_waitcnt vmcnt(3)
s_nop 0
v_pk_add_f32 v[20:21], v[36:37], v[40:41]
v_pk_add_f32 v[22:23], v[38:39], v[42:43]
v_pk_add_f32 v[20:21], v[20:21], v[44:45]
v_pk_add_f32 v[22:23], v[22:23], v[46:47]
v_pk_add_f32 v[24:25], v[20:21], v[22:23]
v_cmp_u_f32 vcc, v24, v25
s_cbranch_vccz LtD13_%=
s_sub_u32 s84, s84, 1
s_cbranch_scc0 LtR12_%=m
s_branch LtD13_%=
Lcode1:
)"
      : "+{v[0:3]}"(acc), "+{v[4:7]}"(st), "+{v[8:23]}"(yv), "+{v[32:35]}"(ad), [g] "+s"(tg), "+{s78}"(rcn)
      : "{s[68:75]}"(sv), "{s[76:77]}"(sw), "{s[44:47]}"(mkrs(p.out, DM * 2)), "{s[48:51]}"(mkrs(p.wd, FF * DM * 2)), [b] "s"(c.b), [w] "s"(c.w), [ep] "s"(c.epoch), [rk] "{s[64:67]}"(c.rsK), [rv] "{s[60:63]}"(c.rsV), [rw] "{s[56:59]}"(c.rsW), [rc] "{s[52:55]}"(c.rsC), [t0] "s"(t0), "{s79}"(r_base(c)), "{s80}"(t4k), "{s81}"(thk), "{s82}"(t4v), "{s83}"(thv)
      : "memory", "vcc", "scc", "s84", "s85", "s86", "s87", "s88", "s89", "s90", "s91", "s92", "s93", "s94", "s95", "s96", "s97", "s98", "s99", "v24", "v25", "v26", "v27", "v28", "v29", "v30", "v31", "s32", "s33", "s34", "s35", "s36", "s37", "s38", "s39", "s40", "s41", "s42", "s43", "s8", "s9", "s10", "s11", "s12", "s13", "s14", "s15", "s16", "s17", "s18", "s19", "s20", "s21", "s22", "s23", "s24", "s25");

    c.stv = st[2];
  }
}

extern "C" void kernel_launch(void* const* d_in, const int* in_sizes, int n_in, void* d_out, int out_size, void* d_ws, size_t ws_size,
                              hipStream_t stream) {
  Params p;
  p.x = (const u16*)d_in[0]; p.K = (const u16*)d_in[1]; p.V = (const u16*)d_in[2];
  p.wq = (const u16*)d_in[3]; p.wo = (const u16*)d_in[4];
  p.wg = (const u16*)d_in[6]; p.wu = (const u16*)d_in[7]; p.wd = (const u16*)d_in[8];
  p.out = (u16*)d_out; p.ws = (unsigned char*)d_ws;
  (void)hipFuncSetAttribute((const void*)fused_decode, hipFuncAttributeMaxDynamicSharedMemorySize, LDS_BYTES);
  hipLaunchKernelGGL(fused_decode, dim3(256), dim3(256), LDS_BYTES, stream, p);
}
```
